```python
import torch

def _softmax(x, axis=-1):
    return torch.softmax(x, dim=axis)
def _gelu(x):
    return torch.nn.functional.gelu(x, approximate="tanh")
def _silu(x):
    return torch.nn.functional.silu(x)
def _sigmoid(x):
    return torch.sigmoid(x)
def _layernorm(x, axis=-1, eps=1e-5):
    m = x.mean(dim=axis, keepdim=True)
    v = x.var(dim=axis, keepdim=True, unbiased=False)
    return (x - m) / torch.sqrt(v + eps)
def _scatter_add(tgt, idx, src, axis=0):
    return tgt.clone().index_add_(axis, idx, src)
def _segment_sum(src, seg, n):
    out = torch.zeros((n,) + src.shape[1:], dtype=src.dtype)
    return out.index_add_(0, seg, src)
def _pad(x, pw):
    flat = tuple(v for pair in reversed(pw) for v in pair)
    return torch.nn.functional.pad(x, flat)

kernel_name = 'standalone__mha_block_cached__near_ridge'

def setup_inputs():

    _w = (lambda a: (lambda t: t.to(torch.bfloat16) if t.is_floating_point() else t)(torch.as_tensor(a)))
    B, Sq, D = 32, 192, 448
    H, Sk, Dh = 16, 512, 256
    return {
        "x": _w((lambda *s: torch.randn(*s, dtype=torch.bfloat16) * 0.8)(B, Sq, D)),
        "k_cache": _w((lambda *s: torch.randn(*s, dtype=torch.bfloat16) * 0.8)(B, H, Sk, Dh)),
        "v_cache": _w((lambda *s: torch.randn(*s, dtype=torch.bfloat16) * 0.05)(B, H, Sk, Dh)),
        "wq": _w((lambda *s: torch.randn(*s, dtype=torch.bfloat16) * 0.8)(D, H * Dh) * (D ** -0.5)),
        "wo": _w((lambda *s: torch.randn(*s, dtype=torch.bfloat16) * 0.05)(H * Dh, D) * ((H * Dh) ** -0.5)),
    }

def reference(x, k_cache, v_cache, wq, wo):
    B, Sq, D = x.shape
    H, Dh = 16, 256
    q = (lambda x, *d: x.permute(*d))((x @ wq).reshape(B, Sq, H, Dh), 0, 2, 1, 3)
    scores = torch.einsum("bhqd,bhkd->bhqk", q, k_cache) * (Dh ** -0.5)
    p = _softmax(scores, axis=-1)
    o = torch.einsum("bhqk,bhkd->bhqd", p, v_cache)
    return (lambda x, *d: x.permute(*d))(o, 0, 2, 1, 3).reshape(B, Sq, H * Dh) @ wo
```

```cpp
#include <hip/hip_runtime.h>
#include <hip/hip_bf16.h>
#include <stdint.h>

typedef unsigned u32x4 __attribute__((ext_vector_type(4)));
typedef unsigned u32x2 __attribute__((ext_vector_type(2)));

__global__ __launch_bounds__(256, 1) void mha_main(const void* __restrict__ x, const void* __restrict__ kc, const void* __restrict__ vc,
                                                   const void* __restrict__ wq, const void* __restrict__ wo, void* __restrict__ part, void* __restrict__ out) {
  __shared__ __attribute__((aligned(16))) char lds[153088];
  const unsigned tid = threadIdx.x, l = tid & 63u;
  const unsigned bid = blockIdx.x, c = bid & 7u, j = bid >> 3;
  const unsigned b = 8u * (c >> 1) + (j & 7u), hp = 4u * (c & 1u) + (j >> 3);
  const unsigned w = __builtin_amdgcn_readfirstlane(tid >> 6);
  const uint64_t xb = (uint64_t)x + (uint64_t)b * 172032u;
  const uint64_t kb = (uint64_t)kc + (uint64_t)(b * 16u + 2u * hp) * 262144u;
  const uint64_t vb = (uint64_t)vc + (uint64_t)(b * 16u + 2u * hp) * 262144u;
  const uint64_t wqb = (uint64_t)wq;
  const uint64_t wob = (uint64_t)wo + (uint64_t)hp * (512u * 896u);
  const uint64_t pb = (uint64_t)part + (uint64_t)b * 1380352u + (uint64_t)hp * 172032u;
  const uint64_t ob = (uint64_t)out + (uint64_t)b * 172032u;
  const unsigned dw = (l >> 5) * 32768u + 16u * (l & 31u);
  const unsigned dkv = (l >> 5) * 2048u + 16u * (l & 31u);
  const unsigned xr = l >> 2;
  const unsigned dx = xr * 896u + 16u * ((l & 3u) ^ ((4u - (xr >> 2)) & 3u));
  const unsigned bx = (l & 15u) * 64u + 16u * ((l >> 4) ^ ((4u - ((l & 15u) >> 2)) & 3u)) + 4224u * w;
  const unsigned bk = (l & 15u) * 1056u + 16u * (l >> 4);
  const unsigned pi = l >> 2;
  const unsigned ba = pi * 1056u + 8u * (l & 3u);
  const unsigned bb = 1024u * pi + 256u * (pi >> 3) + 64u * ((pi >> 1) & 3u) + 8u * (pi & 1u) + 16u * (l & 3u);
  const unsigned ldsb = (unsigned)(size_t)lds;
  asm volatile(
    "s_getpc_b64 s[80:81]\n"
    "L_pc_%=:\n"
    "s_and_b32 s81, s81, 65535\n"
    "s_mov_b32 s82, L_last_%=-L_pc_%=\n"
    "s_mov_b32 s83, 131072\n"
    "s_bfe_u32 s74, %[pk], 327684\n"
    "s_lshl_b32 s74, s74, 2\n"
    "s_lshr_b32 s75, %[pk], 12\n"
    "s_add_u32 s74, s74, s75\n"
    "s_lshl_b32 s74, s74, 9\n"
    "v_mbcnt_lo_u32_b32 v16, -1, 0\n"
    "v_mbcnt_hi_u32_b32 v16, -1, v16\n"
    "v_lshlrev_b32 v16, 3, v16\n"
    "buffer_load_dwordx2 a[250:251], v16, s[80:83], s74 offen\n"
    "s_mov_b32 s20, %[xlo]\n"
    "s_and_b32 s21, %[xhi], 65535\n"
    "s_mov_b32 s22, 172032\n"
    "s_mov_b32 s23, 131072\n"
    "s_lshr_b32 s48, %[pk], 12\n"
    "v_mov_b32_e64 v242, %[dx]\n"
    "v_mov_b32_e64 v240, %[dw]\n"
    "s_mul_i32 s51, s48, 4224\n"
    "s_add_u32 s51, s51, %[ldsb]\n"
    "s_mov_b32 s49, s51\n"
    "s_add_u32 s50, s51, 152064\n"
    "s_mul_i32 s67, s48, 43008\n"
    "s_add_u32 s68, s67, 13280\n"
    "s_add_u32 s69, s67, 26560\n"
    "s_mov_b32 s57, s67\n"
    "s_mov_b32 m0, s49\n"
    "s_nop 0\n"
    "buffer_load_dwordx4 v242, s[20:23], s67 offen offset:0 lds\n"
    "buffer_load_dwordx4 v242, s[20:23], s68 offen offset:1056 lds\n"
    "buffer_load_dwordx4 v242, s[20:23], s69 offen offset:2112 lds\n"
    "s_add_u32 s67, s67, 64\n"
    "s_add_u32 s68, s68, 64\n"
    "s_add_u32 s69, s69, 64\n"
    "s_add_u32 s49, s49, 16896\n"
    "s_cmp_eq_u32 s49, s50\n"
    "s_cselect_b32 s49, s51, s49\n"
    "s_mov_b32 s24, %[wqlo]\n"
    "s_and_b32 s25, %[wqhi], 65535\n"
    "s_mov_b32 s26, 3670016\n"
    "s_mov_b32 s27, 131072\n"
    "s_and_b32 s100, %[pk], 15\n"
    "s_lshl_b32 s100, s100, 10\n"
    "s_mul_i32 s54, s48, 65536\n"
    "s_add_u32 s54, s54, s100\n"
    "s_add_u32 s88, s54, 512\n"
    "s_mov_b32 m0, s49\n"
    "s_nop 0\n"
    "buffer_load_dwordx4 v240, s[24:27], s54 offen offset:0 lds\n"
    "s_add_u32 m0, s49, 1032\n"
    "s_add_u32 s75, s54, 8192\n"
    "buffer_load_dwordx4 v240, s[24:27], s75 offen offset:0 lds\n"
    "s_add_u32 m0, s49, 2112\n"
    "s_add_u32 s74, s54, 16384\n"
    "buffer_load_dwordx4 v240, s[24:27], s74 offen offset:0 lds\n"
    "s_add_u32 m0, s49, 3144\n"
    "s_add_u32 s75, s54, 24576\n"
    "buffer_load_dwordx4 v240, s[24:27], s75 offen offset:0 lds\n"
    "s_add_u32 s54, s54, 262144\n"
    "s_add_u32 s49, s49, 16896\n"
    "s_cmp_eq_u32 s49, s50\n"
    "s_cselect_b32 s49, s51, s49\n"
    "s_mov_b32 m0, s49\n"
    "s_nop 0\n"
    "buffer_load_dwordx4 v242, s[20:23], s67 offen offset:0 lds\n"
    "buffer_load_dwordx4 v242, s[20:23], s68 offen offset:1056 lds\n"
    "buffer_load_dwordx4 v242, s[20:23], s69 offen offset:2112 lds\n"
    "s_add_u32 s67, s67, 64\n"
    "s_add_u32 s68, s68, 64\n"
    "s_add_u32 s69, s69, 64\n"
    "s_add_u32 s49, s49, 16896\n"
    "s_cmp_eq_u32 s49, s50\n"
    "s_cselect_b32 s49, s51, s49\n"
    "s_mov_b32 m0, s49\n"
    "s_nop 0\n"
    "buffer_load_dwordx4 v240, s[24:27], s54 offen offset:0 lds\n"
    "s_add_u32 m0, s49, 1032\n"
    "s_add_u32 s75, s54, 8192\n"
    "buffer_load_dwordx4 v240, s[24:27], s75 offen offset:0 lds\n"
    "s_add_u32 m0, s49, 2112\n"
    "s_add_u32 s74, s54, 16384\n"
    "buffer_load_dwordx4 v240, s[24:27], s74 offen offset:0 lds\n"
    "s_add_u32 m0, s49, 3144\n"
    "s_add_u32 s75, s54, 24576\n"
    "buffer_load_dwordx4 v240, s[24:27], s75 offen offset:0 lds\n"
    "s_add_u32 s54, s54, 262144\n"
    "s_add_u32 s49, s49, 16896\n"
    "s_cmp_eq_u32 s49, s50\n"
    "s_cselect_b32 s49, s51, s49\n"
    "s_mov_b32 m0, s49\n"
    "s_nop 0\n"
    "buffer_load_dwordx4 v242, s[20:23], s67 offen offset:0 lds\n"
    "buffer_load_dwordx4 v242, s[20:23], s68 offen offset:1056 lds\n"
    "buffer_load_dwordx4 v242, s[20:23], s69 offen offset:2112 lds\n"
    "s_add_u32 s67, s67, 64\n"
    "s_add_u32 s68, s68, 64\n"
    "s_add_u32 s69, s69, 64\n"
    "s_add_u32 s49, s49, 16896\n"
    "s_cmp_eq_u32 s49, s50\n"
    "s_cselect_b32 s49, s51, s49\n"
    "s_mov_b32 m0, s49\n"
    "s_nop 0\n"
    "buffer_load_dwordx4 v240, s[24:27], s54 offen offset:0 lds\n"
    "s_add_u32 m0, s49, 1032\n"
    "s_add_u32 s75, s54, 8192\n"
    "buffer_load_dwordx4 v240, s[24:27], s75 offen offset:0 lds\n"
    "s_add_u32 m0, s49, 2112\n"
    "s_add_u32 s74, s54, 16384\n"
    "buffer_load_dwordx4 v240, s[24:27], s74 offen offset:0 lds\n"
    "s_add_u32 m0, s49, 3144\n"
    "s_add_u32 s75, s54, 24576\n"
    "buffer_load_dwordx4 v240, s[24:27], s75 offen offset:0 lds\n"
    "s_add_u32 s54, s54, 262144\n"
    "s_add_u32 s49, s49, 16896\n"
    "s_cmp_eq_u32 s49, s50\n"
    "s_cselect_b32 s49, s51, s49\n"
    "s_mov_b32 s28, %[klo]\n"
    "s_and_b32 s29, %[khi], 65535\n"
    "s_mov_b32 s30, 262144\n"
    "s_mov_b32 s31, 131072\n"
    "s_mov_b32 s32, %[vlo]\n"
    "s_and_b32 s33, %[vhi], 65535\n"
    "s_mov_b32 s34, 262144\n"
    "s_mov_b32 s35, 131072\n"
    "s_mov_b32 s36, %[wolo]\n"
    "s_and_b32 s37, %[wohi], 65535\n"
    "s_mov_b32 s38, 458752\n"
    "s_mov_b32 s39, 131072\n"
    "s_mov_b32 s40, %[plo]\n"
    "s_and_b32 s41, %[phi], 65535\n"
    "s_mov_b32 s42, 172032\n"
    "s_mov_b32 s43, 131072\n"
    "s_bfe_u32 s98, %[pk], 196612\n"
    "s_bfe_u32 s99, %[pk], 131079\n"
    "v_mov_b32_e64 v241, %[dkv]\n"
    "v_mov_b32_e64 v243, %[bx]\n"
    "v_mov_b32_e64 v254, %[bk]\n"
    "v_mov_b32_e64 v255, %[ba]\n"
    "v_mov_b32_e64 v244, %[bb]\n"
    "s_mov_b32 s79, %[ldsb]\n"
    "s_mov_b32 s52, s79\n"
    "s_add_u32 s53, s79, 152064\n"
    "s_mul_i32 s55, s48, 4096\n"
    "s_mov_b32 s56, s55\n"
    "s_mov_b32 s76, 0x3db8aa3b\n"
    "s_mov_b32 s77, 0x3db8aa3b\n"
    "s_mov_b32 s84, s24\n"
    "s_mov_b32 s85, s25\n"
    "s_mov_b32 s86, s26\n"
    "s_mov_b32 s87, s27\n"
    "s_mov_b32 s44, s20\n"
    "s_mov_b32 s45, s21\n"
    "s_mov_b32 s46, s22\n"
    "s_mov_b32 s47, s23\n"
    "s_mov_b32 s89, 262144\n"
    "s_mov_b32 s58, 64\n"
    "s_mov_b32 s90, 0\n"
    "s_mov_b32 s94, 0\n"
    "s_mov_b32 s59, 0\n"
    "s_mov_b32 s63, 0\n"
    "s_mov_b32 s91, 1032\n"
    "s_mov_b32 s95, 8192\n"
    "s_mov_b32 s60, 1056\n"
    "s_mov_b32 s64, 14336\n"
    "s_mov_b32 s92, 2112\n"
    "s_mov_b32 s96, 16384\n"
    "s_mov_b32 s61, 2112\n"
    "s_mov_b32 s65, 28672\n"
    "s_mov_b32 s93, 3144\n"
    "s_mov_b32 s97, 24576\n"
    "s_mov_b32 s62, 3168\n"
    "s_mov_b32 s66, 28672\n"
    "s_mov_b32 s71, 0\n"
    "s_mov_b32 s82, %[olo]\n"
    "s_mov_b32 s83, %[ohi]\n"
    "s_mov_b32 s3, s82\n"
    "s_mov_b32 s11, s83\n"
    "s_mov_b32 s12, s24\n"
    "s_mov_b32 s13, s25\n"
    "s_mov_b32 s14, s26\n"
    "s_mov_b32 s15, s27\n"
    "s_mov_b32 s16, s20\n"
    "s_mov_b32 s17, s21\n"
    "s_mov_b32 s18, s22\n"
    "s_mov_b32 s19, s23\n"
    "s_mul_i32 s0, s98, 458752\n"
    "s_mul_i32 s83, s48, 131072\n"
    "s_add_u32 s0, s0, s83\n"
    "s_add_u32 s0, s0, s100\n"
    "s_add_u32 s0, s0, 512\n"
    "s_mul_i32 s1, s99, 43008\n"
    "s_mul_i32 s83, s48, 10752\n"
    "s_add_u32 s1, s1, s83\n"
    "s_add_u32 s2, s1, 2560\n"
    "s_cmp_lg_u32 s48, 0\n"
    "s_cbranch_scc1 L_hs0_%=\n"
    "v_mbcnt_lo_u32_b32 v18, -1, 0\n"
    "v_mbcnt_hi_u32_b32 v18, -1, v18\n"
    "v_lshlrev_b32 v16, 7, v18\n"
    "v_cmp_gt_u32 vcc, 8, v18\n"
    "v_mov_b32 v18, 1048576\n"
    "s_nop 0\n"
    "v_cndmask_b32 v16, v18, v16, vcc\n"
    "v_mbcnt_lo_u32_b32 v18, -1, 0\n"
    "v_mbcnt_hi_u32_b32 v18, -1, v18\n"
    "v_lshlrev_b32 v17, 2, v18\n"
    "v_cmp_gt_u32 vcc, 8, v18\n"
    "v_mov_b32 v18, 1048576\n"
    "s_nop 0\n"
    "v_cndmask_b32 v17, v18, v17, vcc\n"
    "s_lshr_b32 s82, s100, 10\n"
    "s_mul_i32 s82, s82, 172032\n"
    "s_sub_u32 s80, s40, s82\n"
    "s_subb_u32 s81, s41, 0\n"
    "s_add_u32 s80, s80, 1376256\n"
    "s_addc_u32 s81, s81, 0\n"
    "s_mov_b32 s82, 3072\n"
    "s_mov_b32 s83, 131072\n"
    "s_lshr_b32 s74, s100, 10\n"
    "s_mul_i32 s74, s74, 4\n"
    "s_add_u32 s74, s74, 1024\n"
    "s_add_u32 m0, s79, 152064\n"
    "s_nop 1\n"
    "buffer_load_dword v16, s[80:83], s74 offen sc1 lds\n"
    "s_lshr_b32 s74, s100, 10\n"
    "s_mul_i32 s74, s74, 4\n"
    "s_add_u32 s74, s74, 2048\n"
    "s_add_u32 m0, s79, 152320\n"
    "s_nop 1\n"
    "buffer_load_dword v16, s[80:83], s74 offen sc1 lds\n"
    "s_lshr_b32 s74, s100, 10\n"
    "s_mul_i32 s74, s74, 128\n"
    "s_add_u32 s74, s74, 2048\n"
    "s_add_u32 m0, s79, 152576\n"
    "s_nop 1\n"
    "buffer_load_dword v17, s[80:83], s74 offen sc1 lds\n"
    "s_lshr_b32 s74, s100, 10\n"
    "s_mul_i32 s74, s74, 128\n"
    "s_add_u32 s74, s74, 0\n"
    "s_add_u32 m0, s79, 152832\n"
    "s_nop 1\n"
    "buffer_load_dword v17, s[80:83], s74 offen sc1 lds\n"
    "L_hs0_%=:\n"
    "s_waitcnt vmcnt(14)\n"
    "s_barrier\n"
    "v_add_u32_e64 v246, s52, v243\n"
    "s_add_u32 s52, s52, 16896\n"
    "s_cmp_eq_u32 s52, s53\n"
    "s_cselect_b32 s52, s79, s52\n"
    "v_add_u32_e64 v245, s52, v244\n"
    "s_add_u32 s52, s52, 16896\n"
    "s_cmp_eq_u32 s52, s53\n"
    "s_cselect_b32 s52, s79, s52\n"
    "ds_read_b64_tr_b16 v[224:225], v245 offset:0\n"
    "ds_read_b64_tr_b16 v[226:227], v245 offset:512\n"
    "ds_read_b64_tr_b16 v[228:229], v245 offset:8\n"
    "ds_read_b64_tr_b16 v[230:231], v245 offset:520\n"
    "ds_read_b64_tr_b16 v[232:233], v245 offset:64\n"
    "ds_read_b64_tr_b16 v[234:235], v245 offset:576\n"
    "ds_read_b128 v[192:195], v246 offset:0\n"
    "ds_read_b128 v[196:199], v246 offset:1056\n"
    "ds_read_b128 v[200:203], v246 offset:2112\n"
    "L_qp_%=:\n"
    "s_waitcnt lgkmcnt(0)\n"
    "v_mfma_f32_16x16x32_bf16 v[0:3], v[224:227], v[192:195], 0\n"
    "s_mov_b32 m0, s49\n"
    "ds_read_b64_tr_b16 v[236:237], v245 offset:72\n"
    "v_accvgpr_read_b32 v250, a0\n"
    "v_mfma_f32_16x16x32_bf16 v[8:11], v[224:227], v[196:199], 0\n"
    "ds_read_b64_tr_b16 v[238:239], v245 offset:584\n"
    "v_accvgpr_read_b32 v251, a1\n"
    "v_mfma_f32_16x16x32_bf16 v[16:19], v[224:227], v[200:203], 0\n"
    "buffer_load_dwordx4 v242, s[20:23], s67 offen offset:0 lds\n"
    "v_mfma_f32_16x16x32_bf16 v[4:7], v[228:231], v[192:195], 0\n"
    "ds_read_b64_tr_b16 v[224:225], v245 offset:128\n"
    "v_add_u32_e64 v246, s52, v243\n"
    "v_mfma_f32_16x16x32_bf16 v[12:15], v[228:231], v[196:199], 0\n"
    "s_add_u32 s52, s52, 16896\n"
    "ds_read_b64_tr_b16 v[226:227], v245 offset:640\n"
    "v_accvgpr_read_b32 v252, a2\n"
    "v_mfma_f32_16x16x32_bf16 v[20:23], v[228:231], v[200:203], 0\n"
    "buffer_load_dwordx4 v242, s[20:23], s68 offen offset:1056 lds\n"
    "v_mfma_f32_16x16x32_bf16 v[24:27], v[232:235], v[192:195], 0\n"
    "s_cmp_eq_u32 s52, s53\n"
    "ds_read_b64_tr_b16 v[228:229], v245 offset:136\n"
    "v_accvgpr_read_b32 v253, a3\n"
    "v_mfma_f32_16x16x32_bf16 v[32:35], v[232:235], v[196:199], 0\n"
    "s_cselect_b32 s52, s79, s52\n"
    "ds_read_b64_tr_b16 v[230:231], v245 offset:648\n"
    "v_mul_f32_e64 v250, v250, v247\n"
    "v_mfma_f32_16x16x32_bf16 v[40:43], v[232:235], v[200:203], 0\n"
    "s_waitcnt lgkmcnt(4)\n"
    "buffer_load_dwordx4 v242, s[20:23], s69 offen offset:2112 lds\n"
    "v_mfma_f32_16x16x32_bf16 v[28:31], v[236:239], v[192:195], 0\n"
    "s_add_u32 s67, s67, 64\n"
    "ds_read_b64_tr_b16 v[232:233], v245 offset:192\n"
    "v_mul_f32_e64 v251, v251, v247\n"
    "v_mfma_f32_16x16x32_bf16 v[36:39], v[236:239], v[196:199], 0\n"
    "s_add_u32 s68, s68, 64\n"
    "ds_read_b64_tr_b16 v[234:235], v245 offset:704\n"
    "v_mul_f32_e64 v252, v252, v247\n"
    "v_mfma_f32_16x16x32_bf16 v[44:47], v[236:239], v[200:203], 0\n"
    "s_waitcnt lgkmcnt(4)\n"
    "v_mul_f32_e64 v253, v253, v247\n"
    "v_cvt_pk_bf16_f32 v250, v250, v251\n"
    "v_mfma_f32_16x16x32_bf16 v[48:51], v[224:227], v[192:195], 0\n"
    "s_add_u32 s69, s69, 64\n"
    "ds_read_b64_tr_b16 v[236:237], v245 offset:200\n"
    "v_cvt_pk_bf16_f32 v252, v252, v253\n"
    "v_mfma_f32_16x16x32_bf16 v[56:59], v[224:227], v[196:199], 0\n"
    "s_add_u32 s49, s49, 16896\n"
    "s_cmp_eq_u32 s49, s50\n"
    "s_cselect_b32 s49, s51, s49\n"
    "s_mov_b32 m0, s49\n"
    "ds_read_b64_tr_b16 v[238:239], v245 offset:712\n"
    "v_accvgpr_write_b32 a192, v250\n"
    "v_mfma_f32_16x16x32_bf16 v[64:67], v[224:227], v[200:203], 0\n"
    "s_waitcnt lgkmcnt(4)\n"
    "buffer_load_dwordx4 v240, s[24:27], s54 offen offset:0 lds\n"
    "v_mfma_f32_16x16x32_bf16 v[52:55], v[228:231], v[192:195], 0\n"
    "s_add_u32 m0, s49, 1032\n"
    "ds_read_b64_tr_b16 v[224:225], v245 offset:256\n"
    "v_accvgpr_write_b32 a193, v252\n"
    "v_mfma_f32_16x16x32_bf16 v[60:63], v[228:231], v[196:199], 0\n"
    "s_add_u32 s75, s54, 8192\n"
    "ds_read_b64_tr_b16 v[226:227], v245 offset:768\n"
    "v_accvgpr_read_b32 v250, a12\n"
    "v_mfma_f32_16x16x32_bf16 v[68:71], v[228:231], v[200:203], 0\n"
    "s_waitcnt lgkmcnt(4)\n"
    "v_accvgpr_read_b32 v251, a13\n"
    "v_accvgpr_read_b32 v252, a14\n"
    "v_mfma_f32_16x16x32_bf16 v[72:75], v[232:235], v[192:195], 0\n"
    "ds_read_b64_tr_b16 v[228:229], v245 offset:264\n"
    "v_accvgpr_read_b32 v253, a15\n"
    "v_mfma_f32_16x16x32_bf16 v[80:83], v[232:235], v[196:199], 0\n"
    "ds_read_b64_tr_b16 v[230:231], v245 offset:776\n"
    "v_mul_f32_e64 v250, v250, v247\n"
    "v_mfma_f32_16x16x32_bf16 v[88:91], v[232:235], v[200:203], 0\n"
    "s_waitcnt lgkmcnt(4)\n"
    "buffer_load_dwordx4 v240, s[24:27], s75 offen offset:0 lds\n"
    "v_mfma_f32_16x16x32_bf16 v[76:79], v[236:239], v[192:195], 0\n"
    "s_add_u32 m0, s49, 2112\n"
    "ds_read_b64_tr_b16 v[232:233], v245 offset:320\n"
    "v_mul_f32_e64 v251, v251, v247\n"
    "v_mfma_f32_16x16x32_bf16 v[84:87], v[236:239], v[196:199], 0\n"
    "s_add_u32 s74, s54, 16384\n"
    "ds_read_b64_tr_b16 v[234:235], v245 offset:832\n"
    "v_mul_f32_e64 v252, v252, v247\n"
    "v_mfma_f32_16x16x32_bf16 v[92:95], v[236:239], v[200:203], 0\n"
    "s_waitcnt lgkmcnt(4)\n"
    "v_mul_f32_e64 v253, v253, v247\n"
    "v_cvt_pk_bf16_f32 v250, v250, v251\n"
    "v_mfma_f32_16x16x32_bf16 v[96:99], v[224:227], v[192:195], 0\n"
    "ds_read_b64_tr_b16 v[236:237], v245 offset:328\n"
    "v_cvt_pk_bf16_f32 v252, v252, v253\n"
    "v_mfma_f32_16x16x32_bf16 v[104:107], v[224:227], v[196:199], 0\n"
    "ds_read_b64_tr_b16 v[238:239], v245 offset:840\n"
    "v_accvgpr_write_b32 a194, v250\n"
    "v_mfma_f32_16x16x32_bf16 v[112:115], v[224:227], v[200:203], 0\n"
    "s_waitcnt lgkmcnt(4)\n"
    "buffer_load_dwordx4 v240, s[24:27], s74 offen offset:0 lds\n"
    "v_mfma_f32_16x16x32_bf16 v[100:103], v[228:231], v[192:195], 0\n"
    "s_add_u32 m0, s49, 3144\n"
    "ds_read_b64_tr_b16 v[224:225], v245 offset:384\n"
    "v_accvgpr_write_b32 a195, v252\n"
    "v_mfma_f32_16x16x32_bf16 v[108:111], v[228:231], v[196:199], 0\n"
    "s_add_u32 s75, s54, 24576\n"
    "ds_read_b64_tr_b16 v[226:227], v245 offset:896\n"
    "v_accvgpr_read_b32 v250, a4\n"
    "v_mfma_f32_16x16x32_bf16 v[116:119], v[228:231], v[200:203], 0\n"
    "s_waitcnt lgkmcnt(4)\n"
    "v_accvgpr_read_b32 v251, a5\n"
    "v_accvgpr_read_b32 v252, a6\n"
    "v_mfma_f32_16x16x32_bf16 v[120:123], v[232:235], v[192:195], 0\n"
    "ds_read_b64_tr_b16 v[228:229], v245 offset:392\n"
    "v_accvgpr_read_b32 v253, a7\n"
    "v_mfma_f32_16x16x32_bf16 v[128:131], v[232:235], v[196:199], 0\n"
    "ds_read_b64_tr_b16 v[230:231], v245 offset:904\n"
    "v_mul_f32_e64 v250, v250, v248\n"
    "v_mfma_f32_16x16x32_bf16 v[136:139], v[232:235], v[200:203], 0\n"
    "s_waitcnt lgkmcnt(4)\n"
    "buffer_load_dwordx4 v240, s[24:27], s75 offen offset:0 lds\n"
    "v_mfma_f32_16x16x32_bf16 v[124:127], v[236:239], v[192:195], 0\n"
    "s_add_u32 s54, s54, 262144\n"
    "ds_read_b64_tr_b16 v[232:233], v245 offset:448\n"
    "v_mul_f32_e64 v251, v251, v248\n"
    "v_mfma_f32_16x16x32_bf16 v[132:135], v[236:239], v[196:199], 0\n"
    "s_add_u32 s49, s49, 16896\n"
    "ds_read_b64_tr_b16 v[234:235], v245 offset:960\n"
    "v_mul_f32_e64 v252, v252, v248\n"
    "v_mfma_f32_16x16x32_bf16 v[140:143], v[236:239], v[200:203], 0\n"
    "s_waitcnt lgkmcnt(4)\n"
    "v_mul_f32_e64 v253, v253, v248\n"
    "v_cvt_pk_bf16_f32 v250, v250, v251\n"
    "v_mfma_f32_16x16x32_bf16 v[144:147], v[224:227], v[192:195], 0\n"
    "s_cmp_eq_u32 s49, s50\n"
    "ds_read_b64_tr_b16 v[236:237], v245 offset:456\n"
    "v_cvt_pk_bf16_f32 v252, v252, v253\n"
    "v_mfma_f32_16x16x32_bf16 v[152:155], v[224:227], v[196:199], 0\n"
    "s_waitcnt vmcnt(14)\n"
    "ds_read_b64_tr_b16 v[238:239], v245 offset:968\n"
    "v_add_u32_e64 v245, s52, v244\n"
    "v_mfma_f32_16x16x32_bf16 v[160:163], v[224:227], v[200:203], 0\n"
    "s_waitcnt lgkmcnt(4)\n"
    "v_accvgpr_write_b32 a196, v250\n"
    "v_accvgpr_write_b32 a197, v252\n"
    "v_mfma_f32_16x16x32_bf16 v[148:151], v[228:231], v[192:195], 0\n"
    "s_barrier\n"
    "ds_read_b64_tr_b16 v[224:225], v245 offset:0\n"
    "ds_read_b128 v[204:207], v246 offset:0\n"
    "v_mfma_f32_16x16x32_bf16 v[156:159], v[228:231], v[196:199], 0\n"
    "s_cselect_b32 s49, s51, s49\n"
    "ds_read_b64_tr_b16 v[226:227], v245 offset:512\n"
    "ds_read_b128 v[208:211], v246 offset:1056\n"
    "v_mfma_f32_16x16x32_bf16 v[164:167], v[228:231], v[200:203], 0\n"
    "s_waitcnt lgkmcnt(6)\n"
    "ds_read_b128 v[212:215], v246 offset:2112\n"
    "v_accvgpr_read_b32 v250, a16\n"
    "v_mfma_f32_16x16x32_bf16 v[168:171], v[232:235], v[192:195], 0\n"
    "s_mov_b32 m0, s49\n"
    "ds_read_b64_tr_b16 v[228:229], v245 offset:8\n"
    "v_accvgpr_read_b32 v251, a17\n"
    "v_mfma_f32_16x16x32_bf16 v[176:179], v[232:235], v[196:199], 0\n"
    "s_add_u32 s52, s52, 16896\n"
    "ds_read_b64_tr_b16 v[230:231], v245 offset:520\n"
    "v_accvgpr_read_b32 v252, a18\n"
    "v_mfma_f32_16x16x32_bf16 v[184:187], v[232:235], v[200:203], 0\n"
    "s_waitcnt lgkmcnt(7)\n"
    "v_accvgpr_read_b32 v253, a19\n"
    "v_mul_f32_e64 v250, v250, v248\n"
    "v_mfma_f32_16x16x32_bf16 v[172:175], v[236:239], v[192:195], 0\n"
    "s_cmp_eq_u32 s52, s53\n"
    "ds_read_b64_tr_b16 v[232:233], v245 offset:64\n"
    "v_mul_f32_e64 v251, v251, v248\n"
    "v_mfma_f32_16x16x32_bf16 v[180:183], v[236:239], v[196:199], 0\n"
    "s_cselect_b32 s52, s79, s52\n"
    "ds_read_b64_tr_b16 v[234:235], v245 offset:576\n"
    "v_mfma_f32_16x16x32_bf16 v[188:191], v[236:239], v[200:203], 0\n"
    "s_waitcnt lgkmcnt(4)\n"
    "v_mfma_f32_16x16x32_bf16 v[0:3], v[224:227], v[204:207], v[0:3]\n"
    "ds_read_b64_tr_b16 v[236:237], v245 offset:72\n"
    "v_mul_f32_e64 v252, v252, v248\n"
    "v_mfma_f32_16x16x32_bf16 v[8:11], v[224:227], v[208:211], v[8:11]\n"
    "ds_read_b64_tr_b16 v[238:239], v245 offset:584\n"
    "v_mul_f32_e64 v253, v253, v248\n"
    "v_mfma_f32_16x16x32_bf16 v[16:19], v[224:227], v[212:215], v[16:19]\n"
    "s_waitcnt lgkmcnt(4)\n"
    "buffer_load_dwordx4 v242, s[20:23], s67 offen offset:0 lds\n"
    "v_mfma_f32_16x16x32_bf16 v[4:7], v[228:231], v[204:207], v[4:7]\n"
    "ds_read_b64_tr_b16 v[224:225], v245 offset:128\n"
    "v_add_u32_e64 v246, s52, v243\n"
    "v_mfma_f32_16x16x32_bf16 v[12:15], v[228:231], v[208:211], v[12:15]\n"
    "s_add_u32 s52, s52, 16896\n"
    "ds_read_b64_tr_b16 v[226:227], v245 offset:640\n"
    "v_cvt_pk_bf16_f32 v250, v250, v251\n"
    "v_mfma_f32_16x16x32_bf16 v[20:23], v[228:231], v[212:215], v[20:23]\n"
    "s_waitcnt lgkmcnt(4)\n"
    "buffer_load_dwordx4 v242, s[20:23], s68 offen offset:1056 lds\n"
    "v_mfma_f32_16x16x32_bf16 v[24:27], v[232:235], v[204:207], v[24:27]\n"
    "s_cmp_eq_u32 s52, s53\n"
    "ds_read_b64_tr_b16 v[228:229], v245 offset:136\n"
    "v_cvt_pk_bf16_f32 v252, v252, v253\n"
    "v_mfma_f32_16x16x32_bf16 v[32:35], v[232:235], v[208:211], v[32:35]\n"
    "s_cselect_b32 s52, s79, s52\n"
    "ds_read_b64_tr_b16 v[230:231], v245 offset:648\n"
    "v_accvgpr_write_b32 a198, v250\n"
    "v_mfma_f32_16x16x32_bf16 v[40:43], v[232:235], v[212:215], v[40:43]\n"
    "s_waitcnt lgkmcnt(4)\n"
    "buffer_load_dwordx4 v242, s[20:23], s69 offen offset:2112 lds\n"
    "v_mfma_f32_16x16x32_bf16 v[28:31], v[236:239], v[204:207], v[28:31]\n"
    "s_add_u32 s67, s67, 64\n"
    "ds_read_b64_tr_b16 v[232:233], v245 offset:192\n"
    "v_accvgpr_write_b32 a199, v252\n"
    "v_mfma_f32_16x16x32_bf16 v[36:39], v[236:239], v[208:211], v[36:39]\n"
    "s_add_u32 s68, s68, 64\n"
    "ds_read_b64_tr_b16 v[234:235], v245 offset:704\n"
    "v_accvgpr_read_b32 v250, a8\n"
    "v_mfma_f32_16x16x32_bf16 v[44:47], v[236:239], v[212:215], v[44:47]\n"
    "s_waitcnt lgkmcnt(4)\n"
    "v_accvgpr_read_b32 v251, a9\n"
    "v_accvgpr_read_b32 v252, a10\n"
    "v_mfma_f32_16x16x32_bf16 v[48:51], v[224:227], v[204:207], v[48:51]\n"
    "s_add_u32 s69, s69, 64\n"
    "ds_read_b64_tr_b16 v[236:237], v245 offset:200\n"
    "v_accvgpr_read_b32 v253, a11\n"
    "v_mfma_f32_16x16x32_bf16 v[56:59], v[224:227], v[208:211], v[56:59]\n"
    "s_add_u32 s49, s49, 16896\n"
    "s_cmp_eq_u32 s49, s50\n"
    "s_cselect_b32 s49, s51, s49\n"
    "s_mov_b32 m0, s49\n"
    "ds_read_b64_tr_b16 v[238:239], v245 offset:712\n"
    "v_mul_f32_e64 v250, v250, v249\n"
    "v_mfma_f32_16x16x32_bf16 v[64:67], v[224:227], v[212:215], v[64:67]\n"
    "s_waitcnt lgkmcnt(4)\n"
    "buffer_load_dwordx4 v240, s[24:27], s54 offen offset:0 lds\n"
    "v_mfma_f32_16x16x32_bf16 v[52:55], v[228:231], v[204:207], v[52:55]\n"
    "s_add_u32 m0, s49, 1032\n"
    "ds_read_b64_tr_b16 v[224:225], v245 offset:256\n"
    "v_mul_f32_e64 v251, v251, v249\n"
    "v_mfma_f32_16x16x32_bf16 v[60:63], v[228:231], v[208:211], v[60:63]\n"
    "s_add_u32 s75, s54, 8192\n"
    "ds_read_b64_tr_b16 v[226:227], v245 offset:768\n"
    "v_mul_f32_e64 v252, v252, v249\n"
    "v_mfma_f32_16x16x32_bf16 v[68:71], v[228:231], v[212:215], v[68:71]\n"
    "s_waitcnt lgkmcnt(4)\n"
    "v_mul_f32_e64 v253, v253, v249\n"
    "v_cvt_pk_bf16_f32 v250, v250, v251\n"
    "v_mfma_f32_16x16x32_bf16 v[72:75], v[232:235], v[204:207], v[72:75]\n"
    "ds_read_b64_tr_b16 v[228:229], v245 offset:264\n"
    "v_cvt_pk_bf16_f32 v252, v252, v253\n"
    "v_mfma_f32_16x16x32_bf16 v[80:83], v[232:235], v[208:211], v[80:83]\n"
    "ds_read_b64_tr_b16 v[230:231], v245 offset:776\n"
    "v_accvgpr_write_b32 a200, v250\n"
    "v_mfma_f32_16x16x32_bf16 v[88:91], v[232:235], v[212:215], v[88:91]\n"
    "s_waitcnt lgkmcnt(4)\n"
    "buffer_load_dwordx4 v240, s[24:27], s75 offen offset:0 lds\n"
    "v_mfma_f32_16x16x32_bf16 v[76:79], v[236:239], v[204:207], v[76:79]\n"
    "s_add_u32 m0, s49, 2112\n"
    "ds_read_b64_tr_b16 v[232:233], v245 offset:320\n"
    "v_accvgpr_write_b32 a201, v252\n"
    "v_mfma_f32_16x16x32_bf16 v[84:87], v[236:239], v[208:211], v[84:87]\n"
    "s_add_u32 s74, s54, 16384\n"
    "ds_read_b64_tr_b16 v[234:235], v245 offset:832\n"
    "v_accvgpr_read_b32 v250, a20\n"
    "v_mfma_f32_16x16x32_bf16 v[92:95], v[236:239], v[212:215], v[92:95]\n"
    "s_waitcnt lgkmcnt(4)\n"
    "v_accvgpr_read_b32 v251, a21\n"
    "v_accvgpr_read_b32 v252, a22\n"
    "v_mfma_f32_16x16x32_bf16 v[96:99], v[224:227], v[204:207], v[96:99]\n"
    "ds_read_b64_tr_b16 v[236:237], v245 offset:328\n"
    "v_accvgpr_read_b32 v253, a23\n"
    "v_mfma_f32_16x16x32_bf16 v[104:107], v[224:227], v[208:211], v[104:107]\n"
    "ds_read_b64_tr_b16 v[238:239], v245 offset:840\n"
    "v_mul_f32_e64 v250, v250, v249\n"
    "v_mfma_f32_16x16x32_bf16 v[112:115], v[224:227], v[212:215], v[112:115]\n"
    "s_waitcnt lgkmcnt(4)\n"
    "buffer_load_dwordx4 v240, s[24:27], s74 offen offset:0 lds\n"
    "v_mfma_f32_16x16x32_bf16 v[100:103], v[228:231], v[204:207], v[100:103]\n"
    "s_add_u32 m0, s49, 3144\n"
    "ds_read_b64_tr_b16 v[224:225], v245 offset:384\n"
    "v_mul_f32_e64 v251, v251, v249\n"
    "v_mfma_f32_16x16x32_bf16 v[108:111], v[228:231], v[208:211], v[108:111]\n"
    "s_add_u32 s75, s54, 24576\n"
    "ds_read_b64_tr_b16 v[226:227], v245 offset:896\n"
    "v_mul_f32_e64 v252, v252, v249\n"
    "v_mfma_f32_16x16x32_bf16 v[116:119], v[228:231], v[212:215], v[116:119]\n"
    "s_waitcnt lgkmcnt(4)\n"
    "v_mul_f32_e64 v253, v253, v249\n"
    "v_cvt_pk_bf16_f32 v250, v250, v251\n"
    "v_mfma_f32_16x16x32_bf16 v[120:123], v[232:235], v[204:207], v[120:123]\n"
    "ds_read_b64_tr_b16 v[228:229], v245 offset:392\n"
    "v_cvt_pk_bf16_f32 v252, v252, v253\n"
    "v_mfma_f32_16x16x32_bf16 v[128:131], v[232:235], v[208:211], v[128:131]\n"
    "ds_read_b64_tr_b16 v[230:231], v245 offset:904\n"
    "v_accvgpr_write_b32 a202, v250\n"
    "v_mfma_f32_16x16x32_bf16 v[136:139], v[232:235], v[212:215], v[136:139]\n"
    "s_waitcnt lgkmcnt(4)\n"
    "buffer_load_dwordx4 v240, s[24:27], s75 offen offset:0 lds\n"
    "v_mfma_f32_16x16x32_bf16 v[124:127], v[236:239], v[204:207], v[124:127]\n"
    "s_add_u32 s54, s54, 262144\n"
    "ds_read_b64_tr_b16 v[232:233], v245 offset:448\n"
    "v_accvgpr_write_b32 a203, v252\n"
    "v_mfma_f32_16x16x32_bf16 v[132:135], v[236:239], v[208:211], v[132:135]\n"
    "s_add_u32 s49, s49, 16896\n"
    "ds_read_b64_tr_b16 v[234:235], v245 offset:960\n"
    "v_accvgpr_read_b32 v250, a24\n"
    "v_mfma_f32_16x16x32_bf16 v[140:143], v[236:239], v[212:215], v[140:143]\n"
    "s_waitcnt lgkmcnt(4)\n"
    "v_accvgpr_read_b32 v251, a25\n"
    "v_accvgpr_read_b32 v252, a26\n"
    "v_mfma_f32_16x16x32_bf16 v[144:147], v[224:227], v[204:207], v[144:147]\n"
    "s_cmp_eq_u32 s49, s50\n"
    "ds_read_b64_tr_b16 v[236:237], v245 offset:456\n"
    "v_accvgpr_read_b32 v253, a27\n"
    "v_mfma_f32_16x16x32_bf16 v[152:155], v[224:227], v[208:211], v[152:155]\n"
    "s_waitcnt vmcnt(14)\n"
    "ds_read_b64_tr_b16 v[238:239], v245 offset:968\n"
    "v_add_u32_e64 v245, s52, v244\n"
    "v_mfma_f32_16x16x32_bf16 v[160:163], v[224:227], v[212:215], v[160:163]\n"
    "s_waitcnt lgkmcnt(4)\n"
    "v_mul_f32_e64 v250, v250, v247\n"
    "v_mul_f32_e64 v251, v251, v247\n"
    "v_mfma_f32_16x16x32_bf16 v[148:151], v[228:231], v[204:207], v[148:151]\n"
    "s_barrier\n"
    "ds_read_b64_tr_b16 v[224:225], v245 offset:0\n"
    "ds_read_b128 a[0:3], v246 offset:0\n"
    "v_mfma_f32_16x16x32_bf16 v[156:159], v[228:231], v[208:211], v[156:159]\n"
    "s_cselect_b32 s49, s51, s49\n"
    "ds_read_b64_tr_b16 v[226:227], v245 offset:512\n"
    "ds_read_b128 a[4:7], v246 offset:1056\n"
    "v_mfma_f32_16x16x32_bf16 v[164:167], v[228:231], v[212:215], v[164:167]\n"
    "s_waitcnt lgkmcnt(6)\n"
    "ds_read_b128 a[8:11], v246 offset:2112\n"
    "v_mul_f32_e64 v252, v252, v247\n"
    "v_mfma_f32_16x16x32_bf16 v[168:171], v[232:235], v[204:207], v[168:171]\n"
    "s_mov_b32 m0, s49\n"
    "ds_read_b64_tr_b16 v[228:229], v245 offset:8\n"
    "v_mul_f32_e64 v253, v253, v247\n"
    "v_mfma_f32_16x16x32_bf16 v[176:179], v[232:235], v[208:211], v[176:179]\n"
    "s_add_u32 s52, s52, 16896\n"
    "ds_read_b64_tr_b16 v[230:231], v245 offset:520\n"
    "v_cvt_pk_bf16_f32 v250, v250, v251\n"
    "v_mfma_f32_16x16x32_bf16 v[184:187], v[232:235], v[212:215], v[184:187]\n"
    "s_waitcnt lgkmcnt(7)\n"
    "v_cvt_pk_bf16_f32 v252, v252, v253\n"
    "v_accvgpr_write_b32 a204, v250\n"
    "v_mfma_f32_16x16x32_bf16 v[172:175], v[236:239], v[204:207], v[172:175]\n"
    "s_cmp_eq_u32 s52, s53\n"
    "ds_read_b64_tr_b16 v[232:233], v245 offset:64\n"
    "v_accvgpr_write_b32 a205, v252\n"
    "v_mfma_f32_16x16x32_bf16 v[180:183], v[236:239], v[208:211], v[180:183]\n"
    "s_cselect_b32 s52, s79, s52\n"
    "ds_read_b64_tr_b16 v[234:235], v245 offset:576\n"
    "v_mfma_f32_16x16x32_bf16 v[188:191], v[236:239], v[212:215], v[188:191]\n"
    "s_waitcnt lgkmcnt(4)\n"
    "v_mfma_f32_16x16x32_bf16 v[0:3], v[224:227], a[0:3], v[0:3]\n"
    "ds_read_b64_tr_b16 v[236:237], v245 offset:72\n"
    "v_accvgpr_read_b32 v250, a36\n"
    "v_mfma_f32_16x16x32_bf16 v[8:11], v[224:227], a[4:7], v[8:11]\n"
    "ds_read_b64_tr_b16 v[238:239], v245 offset:584\n"
    "v_accvgpr_read_b32 v251, a37\n"
    "v_mfma_f32_16x16x32_bf16 v[16:19], v[224:227], a[8:11], v[16:19]\n"
    "s_waitcnt lgkmcnt(4)\n"
    "buffer_load_dwordx4 v242, s[20:23], s67 offen offset:0 lds\n"
    "v_mfma_f32_16x16x32_bf16 v[4:7], v[228:231], a[0:3], v[4:7]\n"
    "ds_read_b64_tr_b16 v[224:225], v245 offset:128\n"
    "v_add_u32_e64 v246, s52, v243\n"
    "v_mfma_f32_16x16x32_bf16 v[12:15], v[228:231], a[4:7], v[12:15]\n"
    "s_add_u32 s52, s52, 16896\n"
    "ds_read_b64_tr_b16 v[226:227], v245 offset:640\n"
    "v_accvgpr_read_b32 v252, a38\n"
    "v_mfma_f32_16x16x32_bf16 v[20:23], v[228:231], a[8:11], v[20:23]\n"
    "s_waitcnt lgkmcnt(4)\n"
    "buffer_load_dwordx4 v242, s[20:23], s68 offen offset:1056 lds\n"
    "v_mfma_f32_16x16x32_bf16 v[24:27], v[232:235], a[0:3], v[24:27]\n"
    "s_cmp_eq_u32 s52, s53\n"
    "ds_read_b64_tr_b16 v[228:229], v245 offset:136\n"
    "v_accvgpr_read_b32 v253, a39\n"
    "v_mfma_f32_16x16x32_bf16 v[32:35], v[232:235], a[4:7], v[32:35]\n"
    "s_cselect_b32 s52, s79, s52\n"
    "ds_read_b64_tr_b16 v[230:231], v245 offset:648\n"
    "v_mul_f32_e64 v250, v250, v247\n"
    "v_mfma_f32_16x16x32_bf16 v[40:43], v[232:235], a[8:11], v[40:43]\n"
    "s_waitcnt lgkmcnt(4)\n"
    "buffer_load_dwordx4 v242, s[20:23], s69 offen offset:2112 lds\n"
    "v_mfma_f32_16x16x32_bf16 v[28:31], v[236:239], a[0:3], v[28:31]\n"
    "s_add_u32 s67, s67, 64\n"
    "ds_read_b64_tr_b16 v[232:233], v245 offset:192\n"
    "v_mul_f32_e64 v251, v251, v247\n"
    "v_mfma_f32_16x16x32_bf16 v[36:39], v[236:239], a[4:7], v[36:39]\n"
    "s_add_u32 s68, s68, 64\n"
    "ds_read_b64_tr_b16 v[234:235], v245 offset:704\n"
    "v_mul_f32_e64 v252, v252, v247\n"
    "v_mfma_f32_16x16x32_bf16 v[44:47], v[236:239], a[8:11], v[44:47]\n"
    "s_waitcnt lgkmcnt(4)\n"
    "v_mul_f32_e64 v253, v253, v247\n"
    "v_cvt_pk_bf16_f32 v250, v250, v251\n"
    "v_mfma_f32_16x16x32_bf16 v[48:51], v[224:227], a[0:3], v[48:51]\n"
    "s_add_u32 s69, s69, 64\n"
    "ds_read_b64_tr_b16 v[236:237], v245 offset:200\n"
    "v_cvt_pk_bf16_f32 v252, v252, v253\n"
    "v_mfma_f32_16x16x32_bf16 v[56:59], v[224:227], a[4:7], v[56:59]\n"
    "s_add_u32 s49, s49, 16896\n"
    "s_cmp_eq_u32 s49, s50\n"
    "s_cselect_b32 s49, s51, s49\n"
    "s_mov_b32 m0, s49\n"
    "ds_read_b64_tr_b16 v[238:239], v245 offset:712\n"
    "v_accvgpr_write_b32 a206, v250\n"
    "v_mfma_f32_16x16x32_bf16 v[64:67], v[224:227], a[8:11], v[64:67]\n"
    "s_waitcnt lgkmcnt(4)\n"
    "buffer_load_dwordx4 v240, s[24:27], s54 offen offset:0 lds\n"
    "v_mfma_f32_16x16x32_bf16 v[52:55], v[228:231], a[0:3], v[52:55]\n"
    "s_add_u32 m0, s49, 1032\n"
    "ds_read_b64_tr_b16 v[224:225], v245 offset:256\n"
    "v_accvgpr_write_b32 a207, v252\n"
    "v_mfma_f32_16x16x32_bf16 v[60:63], v[228:231], a[4:7], v[60:63]\n"
    "s_add_u32 s75, s54, 8192\n"
    "ds_read_b64_tr_b16 v[226:227], v245 offset:768\n"
    "v_accvgpr_read_b32 v250, a28\n"
    "v_mfma_f32_16x16x32_bf16 v[68:71], v[228:231], a[8:11], v[68:71]\n"
    "s_waitcnt lgkmcnt(4)\n"
    "v_accvgpr_read_b32 v251, a29\n"
    "v_accvgpr_read_b32 v252, a30\n"
    "v_mfma_f32_16x16x32_bf16 v[72:75], v[232:235], a[0:3], v[72:75]\n"
    "ds_read_b64_tr_b16 v[228:229], v245 offset:264\n"
    "v_accvgpr_read_b32 v253, a31\n"
    "v_mfma_f32_16x16x32_bf16 v[80:83], v[232:235], a[4:7], v[80:83]\n"
    "ds_read_b64_tr_b16 v[230:231], v245 offset:776\n"
    "v_mul_f32_e64 v250, v250, v248\n"
    "v_mfma_f32_16x16x32_bf16 v[88:91], v[232:235], a[8:11], v[88:91]\n"
    "s_waitcnt lgkmcnt(4)\n"
    "buffer_load_dwordx4 v240, s[24:27], s75 offen offset:0 lds\n"
    "v_mfma_f32_16x16x32_bf16 v[76:79], v[236:239], a[0:3], v[76:79]\n"
    "s_add_u32 m0, s49, 2112\n"
    "ds_read_b64_tr_b16 v[232:233], v245 offset:320\n"
    "v_mul_f32_e64 v251, v251, v248\n"
    "v_mfma_f32_16x16x32_bf16 v[84:87], v[236:239], a[4:7], v[84:87]\n"
    "s_add_u32 s74, s54, 16384\n"
    "ds_read_b64_tr_b16 v[234:235], v245 offset:832\n"
    "v_mul_f32_e64 v252, v252, v248\n"
    "v_mfma_f32_16x16x32_bf16 v[92:95], v[236:239], a[8:11], v[92:95]\n"
    "s_waitcnt lgkmcnt(4)\n"
    "v_mul_f32_e64 v253, v253, v248\n"
    "v_cvt_pk_bf16_f32 v250, v250, v251\n"
    "v_mfma_f32_16x16x32_bf16 v[96:99], v[224:227], a[0:3], v[96:99]\n"
    "ds_read_b64_tr_b16 v[236:237], v245 offset:328\n"
    "v_cvt_pk_bf16_f32 v252, v252, v253\n"
    "v_mfma_f32_16x16x32_bf16 v[104:107], v[224:227], a[4:7], v[104:107]\n"
    "ds_read_b64_tr_b16 v[238:239], v245 offset:840\n"
    "v_accvgpr_write_b32 a208, v250\n"
    "v_mfma_f32_16x16x32_bf16 v[112:115], v[224:227], a[8:11], v[112:115]\n"
    "s_waitcnt lgkmcnt(4)\n"
    "buffer_load_dwordx4 v240, s[24:27], s74 offen offset:0 lds\n"
    "v_mfma_f32_16x16x32_bf16 v[100:103], v[228:231], a[0:3], v[100:103]\n"
    "s_add_u32 m0, s49, 3144\n"
    "ds_read_b64_tr_b16 v[224:225], v245 offset:384\n"
    "v_accvgpr_write_b32 a209, v252\n"
    "v_mfma_f32_16x16x32_bf16 v[108:111], v[228:231], a[4:7], v[108:111]\n"
    "s_add_u32 s75, s54, 24576\n"
    "ds_read_b64_tr_b16 v[226:227], v245 offset:896\n"
    "v_accvgpr_read_b32 v250, a40\n"
    "v_mfma_f32_16x16x32_bf16 v[116:119], v[228:231], a[8:11], v[116:119]\n"
    "s_waitcnt lgkmcnt(4)\n"
    "v_accvgpr_read_b32 v251, a41\n"
    "v_accvgpr_read_b32 v252, a42\n"
    "v_mfma_f32_16x16x32_bf16 v[120:123], v[232:235], a[0:3], v[120:123]\n"
    "ds_read_b64_tr_b16 v[228:229], v245 offset:392\n"
    "v_accvgpr_read_b32 v253, a43\n"
    "v_mfma_f32_16x16x32_bf16 v[128:131], v[232:235], a[4:7], v[128:131]\n"
    "ds_read_b64_tr_b16 v[230:231], v245 offset:904\n"
    "v_mul_f32_e64 v250, v250, v248\n"
    "v_mfma_f32_16x16x32_bf16 v[136:139], v[232:235], a[8:11], v[136:139]\n"
    "s_waitcnt lgkmcnt(4)\n"
    "buffer_load_dwordx4 v240, s[24:27], s75 offen offset:0 lds\n"
    "v_mfma_f32_16x16x32_bf16 v[124:127], v[236:239], a[0:3], v[124:127]\n"
    "s_add_u32 s54, s54, 262144\n"
    "ds_read_b64_tr_b16 v[232:233], v245 offset:448\n"
    "v_mul_f32_e64 v251, v251, v248\n"
    "v_mfma_f32_16x16x32_bf16 v[132:135], v[236:239], a[4:7], v[132:135]\n"
    "s_add_u32 s49, s49, 16896\n"
    "ds_read_b64_tr_b16 v[234:235], v245 offset:960\n"
    "v_mul_f32_e64 v252, v252, v248\n"
    "v_mfma_f32_16x16x32_bf16 v[140:143], v[236:239], a[8:11], v[140:143]\n"
    "s_waitcnt lgkmcnt(4)\n"
    "v_mul_f32_e64 v253, v253, v248\n"
    "v_cvt_pk_bf16_f32 v250, v250, v251\n"
    "v_mfma_f32_16x16x32_bf16 v[144:147], v[224:227], a[0:3], v[144:147]\n"
    "s_cmp_eq_u32 s49, s50\n"
    "ds_read_b64_tr_b16 v[236:237], v245 offset:456\n"
    "v_cvt_pk_bf16_f32 v252, v252, v253\n"
    "v_mfma_f32_16x16x32_bf16 v[152:155], v[224:227], a[4:7], v[152:155]\n"
    "s_waitcnt vmcnt(14)\n"
    "ds_read_b64_tr_b16 v[238:239], v245 offset:968\n"
    "v_add_u32_e64 v245, s52, v244\n"
    "v_mfma_f32_16x16x32_bf16 v[160:163], v[224:227], a[8:11], v[160:163]\n"
    "s_waitcnt lgkmcnt(4)\n"
    "v_accvgpr_write_b32 a210, v250\n"
    "v_accvgpr_write_b32 a211, v252\n"
    "v_mfma_f32_16x16x32_bf16 v[148:151], v[228:231], a[0:3], v[148:151]\n"
    "s_barrier\n"
    "ds_read_b64_tr_b16 v[224:225], v245 offset:0\n"
    "ds_read_b128 a[12:15], v246 offset:0\n"
    "v_mfma_f32_16x16x32_bf16 v[156:159], v[228:231], a[4:7], v[156:159]\n"
    "s_cselect_b32 s49, s51, s49\n"
    "ds_read_b64_tr_b16 v[226:227], v245 offset:512\n"
    "ds_read_b128 a[16:19], v246 offset:1056\n"
    "v_mfma_f32_16x16x32_bf16 v[164:167], v[228:231], a[8:11], v[164:167]\n"
    "s_waitcnt lgkmcnt(6)\n"
    "ds_read_b128 a[20:23], v246 offset:2112\n"
    "v_accvgpr_read_b32 v250, a32\n"
    "v_mfma_f32_16x16x32_bf16 v[168:171], v[232:235], a[0:3], v[168:171]\n"
    "s_mov_b32 m0, s49\n"
    "ds_read_b64_tr_b16 v[228:229], v245 offset:8\n"
    "v_accvgpr_read_b32 v251, a33\n"
    "v_mfma_f32_16x16x32_bf16 v[176:179], v[232:235], a[4:7], v[176:179]\n"
    "s_add_u32 s52, s52, 16896\n"
    "ds_read_b64_tr_b16 v[230:231], v245 offset:520\n"
    "v_accvgpr_read_b32 v252, a34\n"
    "v_mfma_f32_16x16x32_bf16 v[184:187], v[232:235], a[8:11], v[184:187]\n"
    "s_waitcnt lgkmcnt(7)\n"
    "v_accvgpr_read_b32 v253, a35\n"
    "v_mul_f32_e64 v250, v250, v249\n"
    "v_mfma_f32_16x16x32_bf16 v[172:175], v[236:239], a[0:3], v[172:175]\n"
    "s_cmp_eq_u32 s52, s53\n"
    "ds_read_b64_tr_b16 v[232:233], v245 offset:64\n"
    "v_mul_f32_e64 v251, v251, v249\n"
    "v_mfma_f32_16x16x32_bf16 v[180:183], v[236:239], a[4:7], v[180:183]\n"
    "s_cselect_b32 s52, s79, s52\n"
    "ds_read_b64_tr_b16 v[234:235], v245 offset:576\n"
    "v_mfma_f32_16x16x32_bf16 v[188:191], v[236:239], a[8:11], v[188:191]\n"
    "s_waitcnt lgkmcnt(4)\n"
    "v_mfma_f32_16x16x32_bf16 v[0:3], v[224:227], a[12:15], v[0:3]\n"
    "ds_read_b64_tr_b16 v[236:237], v245 offset:72\n"
    "v_mul_f32_e64 v252, v252, v249\n"
    "v_mfma_f32_16x16x32_bf16 v[8:11], v[224:227], a[16:19], v[8:11]\n"
    "ds_read_b64_tr_b16 v[238:239], v245 offset:584\n"
    "v_mul_f32_e64 v253, v253, v249\n"
    "v_mfma_f32_16x16x32_bf16 v[16:19], v[224:227], a[20:23], v[16:19]\n"
    "s_waitcnt lgkmcnt(4)\n"
    "buffer_load_dwordx4 v242, s[20:23], s67 offen offset:0 lds\n"
    "v_mfma_f32_16x16x32_bf16 v[4:7], v[228:231], a[12:15], v[4:7]\n"
    "ds_read_b64_tr_b16 v[224:225], v245 offset:128\n"
    "v_add_u32_e64 v246, s52, v243\n"
    "v_mfma_f32_16x16x32_bf16 v[12:15], v[228:231], a[16:19], v[12:15]\n"
    "s_add_u32 s52, s52, 16896\n"
    "ds_read_b64_tr_b16 v[226:227], v245 offset:640\n"
    "v_cvt_pk_bf16_f32 v250, v250, v251\n"
    "v_mfma_f32_16x16x32_bf16 v[20:23], v[228:231], a[20:23], v[20:23]\n"
    "s_waitcnt lgkmcnt(4)\n"
    "buffer_load_dwordx4 v242, s[20:23], s68 offen offset:1056 lds\n"
    "v_mfma_f32_16x16x32_bf16 v[24:27], v[232:235], a[12:15], v[24:27]\n"
    "s_cmp_eq_u32 s52, s53\n"
    "ds_read_b64_tr_b16 v[228:229], v245 offset:136\n"
    "v_cvt_pk_bf16_f32 v252, v252, v253\n"
    "v_mfma_f32_16x16x32_bf16 v[32:35], v[232:235], a[16:19], v[32:35]\n"
    "s_cselect_b32 s52, s79, s52\n"
    "ds_read_b64_tr_b16 v[230:231], v245 offset:648\n"
    "v_accvgpr_write_b32 a212, v250\n"
    "v_mfma_f32_16x16x32_bf16 v[40:43], v[232:235], a[20:23], v[40:43]\n"
    "s_waitcnt lgkmcnt(4)\n"
    "buffer_load_dwordx4 v242, s[20:23], s69 offen offset:2112 lds\n"
    "v_mfma_f32_16x16x32_bf16 v[28:31], v[236:239], a[12:15], v[28:31]\n"
    "s_add_u32 s67, s67, 64\n"
    "ds_read_b64_tr_b16 v[232:233], v245 offset:192\n"
    "v_accvgpr_write_b32 a213, v252\n"
    "v_mfma_f32_16x16x32_bf16 v[36:39], v[236:239], a[16:19], v[36:39]\n"
    "s_add_u32 s68, s68, 64\n"
    "ds_read_b64_tr_b16 v[234:235], v245 offset:704\n"
    "v_accvgpr_read_b32 v250, a44\n"
    "v_mfma_f32_16x16x32_bf16 v[44:47], v[236:239], a[20:23], v[44:47]\n"
    "s_waitcnt lgkmcnt(4)\n"
    "v_accvgpr_read_b32 v251, a45\n"
    "v_accvgpr_read_b32 v252, a46\n"
    "v_mfma_f32_16x16x32_bf16 v[48:51], v[224:227], a[12:15], v[48:51]\n"
    "s_add_u32 s69, s69, 64\n"
    "ds_read_b64_tr_b16 v[236:237], v245 offset:200\n"
    "v_accvgpr_read_b32 v253, a47\n"
    "v_mfma_f32_16x16x32_bf16 v[56:59], v[224:227], a[16:19], v[56:59]\n"
    "s_add_u32 s49, s49, 16896\n"
    "s_cmp_eq_u32 s49, s50\n"
    "s_cselect_b32 s49, s51, s49\n"
    "s_mov_b32 m0, s49\n"
    "ds_read_b64_tr_b16 v[238:239], v245 offset:712\n"
    "v_mul_f32_e64 v250, v250, v249\n"
    "v_mfma_f32_16x16x32_bf16 v[64:67], v[224:227], a[20:23], v[64:67]\n"
    "s_waitcnt lgkmcnt(4)\n"
    "buffer_load_dwordx4 v240, s[24:27], s54 offen offset:0 lds\n"
    "v_mfma_f32_16x16x32_bf16 v[52:55], v[228:231], a[12:15], v[52:55]\n"
    "s_add_u32 m0, s49, 1032\n"
    "ds_read_b64_tr_b16 v[224:225], v245 offset:256\n"
    "v_mul_f32_e64 v251, v251, v249\n"
    "v_mfma_f32_16x16x32_bf16 v[60:63], v[228:231], a[16:19], v[60:63]\n"
    "s_add_u32 s75, s54, 8192\n"
    "ds_read_b64_tr_b16 v[226:227], v245 offset:768\n"
    "v_mul_f32_e64 v252, v252, v249\n"
    "v_mfma_f32_16x16x32_bf16 v[68:71], v[228:231], a[20:23], v[68:71]\n"
    "s_waitcnt lgkmcnt(4)\n"
    "v_mul_f32_e64 v253, v253, v249\n"
    "v_cvt_pk_bf16_f32 v250, v250, v251\n"
    "v_mfma_f32_16x16x32_bf16 v[72:75], v[232:235], a[12:15], v[72:75]\n"
    "ds_read_b64_tr_b16 v[228:229], v245 offset:264\n"
    "v_cvt_pk_bf16_f32 v252, v252, v253\n"
    "v_mfma_f32_16x16x32_bf16 v[80:83], v[232:235], a[16:19], v[80:83]\n"
    "ds_read_b64_tr_b16 v[230:231], v245 offset:776\n"
    "v_accvgpr_write_b32 a214, v250\n"
    "v_mfma_f32_16x16x32_bf16 v[88:91], v[232:235], a[20:23], v[88:91]\n"
    "s_waitcnt lgkmcnt(4)\n"
    "buffer_load_dwordx4 v240, s[24:27], s75 offen offset:0 lds\n"
    "v_mfma_f32_16x16x32_bf16 v[76:79], v[236:239], a[12:15], v[76:79]\n"
    "s_add_u32 m0, s49, 2112\n"
    "ds_read_b64_tr_b16 v[232:233], v245 offset:320\n"
    "v_accvgpr_write_b32 a215, v252\n"
    "v_mfma_f32_16x16x32_bf16 v[84:87], v[236:239], a[16:19], v[84:87]\n"
    "s_add_u32 s74, s54, 16384\n"
    "ds_read_b64_tr_b16 v[234:235], v245 offset:832\n"
    "v_accvgpr_read_b32 v250, a48\n"
    "v_mfma_f32_16x16x32_bf16 v[92:95], v[236:239], a[20:23], v[92:95]\n"
    "s_waitcnt lgkmcnt(4)\n"
    "v_accvgpr_read_b32 v251, a49\n"
    "v_accvgpr_read_b32 v252, a50\n"
    "v_mfma_f32_16x16x32_bf16 v[96:99], v[224:227], a[12:15], v[96:99]\n"
    "ds_read_b64_tr_b16 v[236:237], v245 offset:328\n"
    "v_accvgpr_read_b32 v253, a51\n"
    "v_mfma_f32_16x16x32_bf16 v[104:107], v[224:227], a[16:19], v[104:107]\n"
    "ds_read_b64_tr_b16 v[238:239], v245 offset:840\n"
    "v_mul_f32_e64 v250, v250, v247\n"
    "v_mfma_f32_16x16x32_bf16 v[112:115], v[224:227], a[20:23], v[112:115]\n"
    "s_waitcnt lgkmcnt(4)\n"
    "buffer_load_dwordx4 v240, s[24:27], s74 offen offset:0 lds\n"
    "v_mfma_f32_16x16x32_bf16 v[100:103], v[228:231], a[12:15], v[100:103]\n"
    "s_add_u32 m0, s49, 3144\n"
    "ds_read_b64_tr_b16 v[224:225], v245 offset:384\n"
    "v_mul_f32_e64 v251, v251, v247\n"
    "v_mfma_f32_16x16x32_bf16 v[108:111], v[228:231], a[16:19], v[108:111]\n"
    "s_add_u32 s75, s54, 24576\n"
    "ds_read_b64_tr_b16 v[226:227], v245 offset:896\n"
    "v_mul_f32_e64 v252, v252, v247\n"
    "v_mfma_f32_16x16x32_bf16 v[116:119], v[228:231], a[20:23], v[116:119]\n"
    "s_waitcnt lgkmcnt(4)\n"
    "v_mul_f32_e64 v253, v253, v247\n"
    "v_cvt_pk_bf16_f32 v250, v250, v251\n"
    "v_mfma_f32_16x16x32_bf16 v[120:123], v[232:235], a[12:15], v[120:123]\n"
    "ds_read_b64_tr_b16 v[228:229], v245 offset:392\n"
    "v_cvt_pk_bf16_f32 v252, v252, v253\n"
    "v_mfma_f32_16x16x32_bf16 v[128:131], v[232:235], a[16:19], v[128:131]\n"
    "ds_read_b64_tr_b16 v[230:231], v245 offset:904\n"
    "v_accvgpr_write_b32 a216, v250\n"
    "v_mfma_f32_16x16x32_bf16 v[136:139], v[232:235], a[20:23], v[136:139]\n"
    "s_waitcnt lgkmcnt(4)\n"
    "buffer_load_dwordx4 v240, s[24:27], s75 offen offset:0 lds\n"
    "v_mfma_f32_16x16x32_bf16 v[124:127], v[236:239], a[12:15], v[124:127]\n"
    "s_add_u32 s54, s54, 262144\n"
    "ds_read_b64_tr_b16 v[232:233], v245 offset:448\n"
    "v_accvgpr_write_b32 a217, v252\n"
    "v_mfma_f32_16x16x32_bf16 v[132:135], v[236:239], a[16:19], v[132:135]\n"
    "s_add_u32 s49, s49, 16896\n"
    "ds_read_b64_tr_b16 v[234:235], v245 offset:960\n"
    "v_accvgpr_read_b32 v250, a60\n"
    "v_mfma_f32_16x16x32_bf16 v[140:143], v[236:239], a[20:23], v[140:143]\n"
    "s_waitcnt lgkmcnt(4)\n"
    "v_accvgpr_read_b32 v251, a61\n"
    "v_accvgpr_read_b32 v252, a62\n"
    "v_mfma_f32_16x16x32_bf16 v[144:147], v[224:227], a[12:15], v[144:147]\n"
    "s_cmp_eq_u32 s49, s50\n"
    "ds_read_b64_tr_b16 v[236:237], v245 offset:456\n"
    "v_accvgpr_read_b32 v253, a63\n"
    "v_mfma_f32_16x16x32_bf16 v[152:155], v[224:227], a[16:19], v[152:155]\n"
    "s_waitcnt vmcnt(14)\n"
    "ds_read_b64_tr_b16 v[238:239], v245 offset:968\n"
    "v_add_u32_e64 v245, s52, v244\n"
    "v_mfma_f32_16x16x32_bf16 v[160:163], v[224:227], a[20:23], v[160:163]\n"
    "s_waitcnt lgkmcnt(4)\n"
    "v_mul_f32_e64 v250, v250, v247\n"
    "v_mul_f32_e64 v251, v251, v247\n"
    "v_mfma_f32_16x16x32_bf16 v[148:151], v[228:231], a[12:15], v[148:151]\n"
    "s_barrier\n"
    "ds_read_b64_tr_b16 v[224:225], v245 offset:0\n"
    "ds_read_b128 a[0:3], v246 offset:0\n"
    "v_mfma_f32_16x16x32_bf16 v[156:159], v[228:231], a[16:19], v[156:159]\n"
    "s_cselect_b32 s49, s51, s49\n"
    "ds_read_b64_tr_b16 v[226:227], v245 offset:512\n"
    "ds_read_b128 a[4:7], v246 offset:1056\n"
    "v_mfma_f32_16x16x32_bf16 v[164:167], v[228:231], a[20:23], v[164:167]\n"
    "s_waitcnt lgkmcnt(6)\n"
    "ds_read_b128 a[8:11], v246 offset:2112\n"
    "v_mul_f32_e64 v252, v252, v247\n"
    "v_mfma_f32_16x16x32_bf16 v[168:171], v[232:235], a[12:15], v[168:171]\n"
    "s_mov_b32 m0, s49\n"
    "ds_read_b64_tr_b16 v[228:229], v245 offset:8\n"
    "v_mul_f32_e64 v253, v253, v247\n"
    "v_mfma_f32_16x16x32_bf16 v[176:179], v[232:235], a[16:19], v[176:179]\n"
    "s_add_u32 s52, s52, 16896\n"
    "ds_read_b64_tr_b16 v[230:231], v245 offset:520\n"
    "v_cvt_pk_bf16_f32 v250, v250, v251\n"
    "v_mfma_f32_16x16x32_bf16 v[184:187], v[232:235], a[20:23], v[184:187]\n"
    "s_waitcnt lgkmcnt(7)\n"
    "v_cvt_pk_bf16_f32 v252, v252, v253\n"
    "v_accvgpr_write_b32 a218, v250\n"
    "v_mfma_f32_16x16x32_bf16 v[172:175], v[236:239], a[12:15], v[172:175]\n"
    "s_cmp_eq_u32 s52, s53\n"
    "ds_read_b64_tr_b16 v[232:233], v245 offset:64\n"
    "v_accvgpr_write_b32 a219, v252\n"
    "v_mfma_f32_16x16x32_bf16 v[180:183], v[236:239], a[16:19], v[180:183]\n"
    "s_cselect_b32 s52, s79, s52\n"
    "ds_read_b64_tr_b16 v[234:235], v245 offset:576\n"
    "v_mfma_f32_16x16x32_bf16 v[188:191], v[236:239], a[20:23], v[188:191]\n"
    "s_waitcnt lgkmcnt(4)\n"
    "v_mfma_f32_16x16x32_bf16 v[0:3], v[224:227], a[0:3], v[0:3]\n"
    "ds_read_b64_tr_b16 v[236:237], v245 offset:72\n"
    "v_accvgpr_read_b32 v250, a52\n"
    "v_mfma_f32_16x16x32_bf16 v[8:11], v[224:227], a[4:7], v[8:11]\n"
    "ds_read_b64_tr_b16 v[238:239], v245 offset:584\n"
    "v_accvgpr_read_b32 v251, a53\n"
    "v_mfma_f32_16x16x32_bf16 v[16:19], v[224:227], a[8:11], v[16:19]\n"
    "s_waitcnt lgkmcnt(4)\n"
    "buffer_load_dwordx4 v242, s[20:23], s67 offen offset:0 lds\n"
    "v_mfma_f32_16x16x32_bf16 v[4:7], v[228:231], a[0:3], v[4:7]\n"
    "ds_read_b64_tr_b16 v[224:225], v245 offset:128\n"
    "v_add_u32_e64 v246, s52, v243\n"
    "v_mfma_f32_16x16x32_bf16 v[12:15], v[228:231], a[4:7], v[12:15]\n"
    "s_add_u32 s52, s52, 16896\n"
    "ds_read_b64_tr_b16 v[226:227], v245 offset:640\n"
    "v_accvgpr_read_b32 v252, a54\n"
    "v_mfma_f32_16x16x32_bf16 v[20:23], v[228:231], a[8:11], v[20:23]\n"
    "s_waitcnt lgkmcnt(4)\n"
    "buffer_load_dwordx4 v242, s[20:23], s68 offen offset:1056 lds\n"
    "v_mfma_f32_16x16x32_bf16 v[24:27], v[232:235], a[0:3], v[24:27]\n"
    "s_cmp_eq_u32 s52, s53\n"
    "ds_read_b64_tr_b16 v[228:229], v245 offset:136\n"
    "v_accvgpr_read_b32 v253, a55\n"
    "v_mfma_f32_16x16x32_bf16 v[32:35], v[232:235], a[4:7], v[32:35]\n"
    "s_cselect_b32 s52, s79, s52\n"
    "ds_read_b64_tr_b16 v[230:231], v245 offset:648\n"
    "v_mul_f32_e64 v250, v250, v248\n"
    "v_mfma_f32_16x16x32_bf16 v[40:43], v[232:235], a[8:11], v[40:43]\n"
    "s_waitcnt lgkmcnt(4)\n"
    "buffer_load_dwordx4 v242, s[20:23], s69 offen offset:2112 lds\n"
    "v_mfma_f32_16x16x32_bf16 v[28:31], v[236:239], a[0:3], v[28:31]\n"
    "s_add_u32 s67, s67, 64\n"
    "ds_read_b64_tr_b16 v[232:233], v245 offset:192\n"
    "v_mul_f32_e64 v251, v251, v248\n"
    "v_mfma_f32_16x16x32_bf16 v[36:39], v[236:239], a[4:7], v[36:39]\n"
    "s_add_u32 s68, s68, 64\n"
    "ds_read_b64_tr_b16 v[234:235], v245 offset:704\n"
    "v_mul_f32_e64 v252, v252, v248\n"
    "v_mfma_f32_16x16x32_bf16 v[44:47], v[236:239], a[8:11], v[44:47]\n"
    "s_waitcnt lgkmcnt(4)\n"
    "v_mul_f32_e64 v253, v253, v248\n"
    "v_cvt_pk_bf16_f32 v250, v250, v251\n"
    "v_mfma_f32_16x16x32_bf16 v[48:51], v[224:227], a[0:3], v[48:51]\n"
    "s_add_u32 s69, s69, 64\n"
    "ds_read_b64_tr_b16 v[236:237], v245 offset:200\n"
    "v_cvt_pk_bf16_f32 v252, v252, v253\n"
    "v_mfma_f32_16x16x32_bf16 v[56:59], v[224:227], a[4:7], v[56:59]\n"
    "s_add_u32 s49, s49, 16896\n"
    "s_cmp_eq_u32 s49, s50\n"
    "s_cselect_b32 s49, s51, s49\n"
    "s_mov_b32 m0, s49\n"
    "ds_read_b64_tr_b16 v[238:239], v245 offset:712\n"
    "v_accvgpr_write_b32 a220, v250\n"
    "v_mfma_f32_16x16x32_bf16 v[64:67], v[224:227], a[8:11], v[64:67]\n"
    "s_waitcnt lgkmcnt(4)\n"
    "buffer_load_dwordx4 v240, s[24:27], s54 offen offset:0 lds\n"
    "v_mfma_f32_16x16x32_bf16 v[52:55], v[228:231], a[0:3], v[52:55]\n"
    "s_add_u32 m0, s49, 1032\n"
    "ds_read_b64_tr_b16 v[224:225], v245 offset:256\n"
    "v_accvgpr_write_b32 a221, v252\n"
    "v_mfma_f32_16x16x32_bf16 v[60:63], v[228:231], a[4:7], v[60:63]\n"
    "s_add_u32 s75, s54, 8192\n"
    "ds_read_b64_tr_b16 v[226:227], v245 offset:768\n"
    "v_accvgpr_read_b32 v250, a64\n"
    "v_mfma_f32_16x16x32_bf16 v[68:71], v[228:231], a[8:11], v[68:71]\n"
    "s_waitcnt lgkmcnt(4)\n"
    "v_accvgpr_read_b32 v251, a65\n"
    "v_accvgpr_read_b32 v252, a66\n"
    "v_mfma_f32_16x16x32_bf16 v[72:75], v[232:235], a[0:3], v[72:75]\n"
    "ds_read_b64_tr_b16 v[228:229], v245 offset:264\n"
    "v_accvgpr_read_b32 v253, a67\n"
    "v_mfma_f32_16x16x32_bf16 v[80:83], v[232:235], a[4:7], v[80:83]\n"
    "ds_read_b64_tr_b16 v[230:231], v245 offset:776\n"
    "v_mul_f32_e64 v250, v250, v248\n"
    "v_mfma_f32_16x16x32_bf16 v[88:91], v[232:235], a[8:11], v[88:91]\n"
    "s_waitcnt lgkmcnt(4)\n"
    "buffer_load_dwordx4 v240, s[24:27], s75 offen offset:0 lds\n"
    "v_mfma_f32_16x16x32_bf16 v[76:79], v[236:239], a[0:3], v[76:79]\n"
    "s_add_u32 m0, s49, 2112\n"
    "ds_read_b64_tr_b16 v[232:233], v245 offset:320\n"
    "v_mul_f32_e64 v251, v251, v248\n"
    "v_mfma_f32_16x16x32_bf16 v[84:87], v[236:239], a[4:7], v[84:87]\n"
    "s_add_u32 s74, s54, 16384\n"
    "ds_read_b64_tr_b16 v[234:235], v245 offset:832\n"
    "v_mul_f32_e64 v252, v252, v248\n"
    "v_mfma_f32_16x16x32_bf16 v[92:95], v[236:239], a[8:11], v[92:95]\n"
    "s_waitcnt lgkmcnt(4)\n"
    "v_mul_f32_e64 v253, v253, v248\n"
    "v_cvt_pk_bf16_f32 v250, v250, v251\n"
    "v_mfma_f32_16x16x32_bf16 v[96:99], v[224:227], a[0:3], v[96:99]\n"
    "ds_read_b64_tr_b16 v[236:237], v245 offset:328\n"
    "v_cvt_pk_bf16_f32 v252, v252, v253\n"
    "v_mfma_f32_16x16x32_bf16 v[104:107], v[224:227], a[4:7], v[104:107]\n"
    "ds_read_b64_tr_b16 v[238:239], v245 offset:840\n"
    "v_accvgpr_write_b32 a222, v250\n"
    "v_mfma_f32_16x16x32_bf16 v[112:115], v[224:227], a[8:11], v[112:115]\n"
    "s_waitcnt lgkmcnt(4)\n"
    "buffer_load_dwordx4 v240, s[24:27], s74 offen offset:0 lds\n"
    "v_mfma_f32_16x16x32_bf16 v[100:103], v[228:231], a[0:3], v[100:103]\n"
    "s_add_u32 m0, s49, 3144\n"
    "ds_read_b64_tr_b16 v[224:225], v245 offset:384\n"
    "v_accvgpr_write_b32 a223, v252\n"
    "v_mfma_f32_16x16x32_bf16 v[108:111], v[228:231], a[4:7], v[108:111]\n"
    "s_add_u32 s75, s54, 24576\n"
    "ds_read_b64_tr_b16 v[226:227], v245 offset:896\n"
    "v_accvgpr_read_b32 v250, a56\n"
    "v_mfma_f32_16x16x32_bf16 v[116:119], v[228:231], a[8:11], v[116:119]\n"
    "s_waitcnt lgkmcnt(4)\n"
    "v_accvgpr_read_b32 v251, a57\n"
    "v_accvgpr_read_b32 v252, a58\n"
    "v_mfma_f32_16x16x32_bf16 v[120:123], v[232:235], a[0:3], v[120:123]\n"
    "ds_read_b64_tr_b16 v[228:229], v245 offset:392\n"
    "v_accvgpr_read_b32 v253, a59\n"
    "v_mfma_f32_16x16x32_bf16 v[128:131], v[232:235], a[4:7], v[128:131]\n"
    "ds_read_b64_tr_b16 v[230:231], v245 offset:904\n"
    "v_mul_f32_e64 v250, v250, v249\n"
    "v_mfma_f32_16x16x32_bf16 v[136:139], v[232:235], a[8:11], v[136:139]\n"
    "s_waitcnt lgkmcnt(4)\n"
    "buffer_load_dwordx4 v240, s[24:27], s75 offen offset:0 lds\n"
    "v_mfma_f32_16x16x32_bf16 v[124:127], v[236:239], a[0:3], v[124:127]\n"
    "s_add_u32 s54, s54, 262144\n"
    "ds_read_b64_tr_b16 v[232:233], v245 offset:448\n"
    "v_mul_f32_e64 v251, v251, v249\n"
    "v_mfma_f32_16x16x32_bf16 v[132:135], v[236:239], a[4:7], v[132:135]\n"
    "s_add_u32 s49, s49, 16896\n"
    "ds_read_b64_tr_b16 v[234:235], v245 offset:960\n"
    "v_mul_f32_e64 v252, v252, v249\n"
    "v_mfma_f32_16x16x32_bf16 v[140:143], v[236:239], a[8:11], v[140:143]\n"
    "s_waitcnt lgkmcnt(4)\n"
    "v_mul_f32_e64 v253, v253, v249\n"
    "v_cvt_pk_bf16_f32 v250, v250, v251\n"
    "v_mfma_f32_16x16x32_bf16 v[144:147], v[224:227], a[0:3], v[144:147]\n"
    "s_cmp_eq_u32 s49, s50\n"
    "ds_read_b64_tr_b16 v[236:237], v245 offset:456\n"
    "v_cvt_pk_bf16_f32 v252, v252, v253\n"
    "v_mfma_f32_16x16x32_bf16 v[152:155], v[224:227], a[4:7], v[152:155]\n"
    "s_waitcnt vmcnt(14)\n"
    "ds_read_b64_tr_b16 v[238:239], v245 offset:968\n"
    "v_add_u32_e64 v245, s52, v244\n"
    "v_mfma_f32_16x16x32_bf16 v[160:163], v[224:227], a[8:11], v[160:163]\n"
    "s_waitcnt lgkmcnt(4)\n"
    "v_accvgpr_write_b32 a224, v250\n"
    "v_accvgpr_write_b32 a225, v252\n"
    "v_mfma_f32_16x16x32_bf16 v[148:151], v[228:231], a[0:3], v[148:151]\n"
    "s_barrier\n"
    "ds_read_b64_tr_b16 v[224:225], v245 offset:0\n"
    "ds_read_b128 a[12:15], v246 offset:0\n"
    "v_mfma_f32_16x16x32_bf16 v[156:159], v[228:231], a[4:7], v[156:159]\n"
    "s_cselect_b32 s49, s51, s49\n"
    "ds_read_b64_tr_b16 v[226:227], v245 offset:512\n"
    "ds_read_b128 a[16:19], v246 offset:1056\n"
    "v_mfma_f32_16x16x32_bf16 v[164:167], v[228:231], a[8:11], v[164:167]\n"
    "s_waitcnt lgkmcnt(6)\n"
    "ds_read_b128 a[20:23], v246 offset:2112\n"
    "v_accvgpr_read_b32 v250, a68\n"
    "v_mfma_f32_16x16x32_bf16 v[168:171], v[232:235], a[0:3], v[168:171]\n"
    "s_mov_b32 m0, s49\n"
    "ds_read_b64_tr_b16 v[228:229], v245 offset:8\n"
    "v_accvgpr_read_b32 v251, a69\n"
    "v_mfma_f32_16x16x32_bf16 v[176:179], v[232:235], a[4:7], v[176:179]\n"
    "s_add_u32 s52, s52, 16896\n"
    "ds_read_b64_tr_b16 v[230:231], v245 offset:520\n"
    "v_accvgpr_read_b32 v252, a70\n"
    "v_mfma_f32_16x16x32_bf16 v[184:187], v[232:235], a[8:11], v[184:187]\n"
    "s_waitcnt lgkmcnt(7)\n"
    "v_accvgpr_read_b32 v253, a71\n"
    "v_mul_f32_e64 v250, v250, v249\n"
    "v_mfma_f32_16x16x32_bf16 v[172:175], v[236:239], a[0:3], v[172:175]\n"
    "s_cmp_eq_u32 s52, s53\n"
    "ds_read_b64_tr_b16 v[232:233], v245 offset:64\n"
    "v_mul_f32_e64 v251, v251, v249\n"
    "v_mfma_f32_16x16x32_bf16 v[180:183], v[236:239], a[4:7], v[180:183]\n"
    "s_cselect_b32 s52, s79, s52\n"
    "ds_read_b64_tr_b16 v[234:235], v245 offset:576\n"
    "v_mfma_f32_16x16x32_bf16 v[188:191], v[236:239], a[8:11], v[188:191]\n"
    "s_waitcnt lgkmcnt(4)\n"
    "v_mfma_f32_16x16x32_bf16 v[0:3], v[224:227], a[12:15], v[0:3]\n"
    "ds_read_b64_tr_b16 v[236:237], v245 offset:72\n"
    "v_mul_f32_e64 v252, v252, v249\n"
    "v_mfma_f32_16x16x32_bf16 v[8:11], v[224:227], a[16:19], v[8:11]\n"
    "ds_read_b64_tr_b16 v[238:239], v245 offset:584\n"
    "v_mul_f32_e64 v253, v253, v249\n"
    "v_mfma_f32_16x16x32_bf16 v[16:19], v[224:227], a[20:23], v[16:19]\n"
    "s_waitcnt lgkmcnt(4)\n"
    "buffer_load_dwordx4 v242, s[20:23], s67 offen offset:0 lds\n"
    "v_mfma_f32_16x16x32_bf16 v[4:7], v[228:231], a[12:15], v[4:7]\n"
    "ds_read_b64_tr_b16 v[224:225], v245 offset:128\n"
    "v_add_u32_e64 v246, s52, v243\n"
    "v_mfma_f32_16x16x32_bf16 v[12:15], v[228:231], a[16:19], v[12:15]\n"
    "s_add_u32 s52, s52, 16896\n"
    "ds_read_b64_tr_b16 v[226:227], v245 offset:640\n"
    "v_cvt_pk_bf16_f32 v250, v250, v251\n"
    "v_mfma_f32_16x16x32_bf16 v[20:23], v[228:231], a[20:23], v[20:23]\n"
    "s_waitcnt lgkmcnt(4)\n"
    "buffer_load_dwordx4 v242, s[20:23], s68 offen offset:1056 lds\n"
    "v_mfma_f32_16x16x32_bf16 v[24:27], v[232:235], a[12:15], v[24:27]\n"
    "s_cmp_eq_u32 s52, s53\n"
    "ds_read_b64_tr_b16 v[228:229], v245 offset:136\n"
    "v_cvt_pk_bf16_f32 v252, v252, v253\n"
    "v_mfma_f32_16x16x32_bf16 v[32:35], v[232:235], a[16:19], v[32:35]\n"
    "s_cselect_b32 s52, s79, s52\n"
    "ds_read_b64_tr_b16 v[230:231], v245 offset:648\n"
    "v_accvgpr_write_b32 a226, v250\n"
    "v_mfma_f32_16x16x32_bf16 v[40:43], v[232:235], a[20:23], v[40:43]\n"
    "s_waitcnt lgkmcnt(4)\n"
    "buffer_load_dwordx4 v242, s[20:23], s69 offen offset:2112 lds\n"
    "v_mfma_f32_16x16x32_bf16 v[28:31], v[236:239], a[12:15], v[28:31]\n"
    "s_add_u32 s67, s67, 64\n"
    "ds_read_b64_tr_b16 v[232:233], v245 offset:192\n"
    "v_accvgpr_write_b32 a227, v252\n"
    "v_mfma_f32_16x16x32_bf16 v[36:39], v[236:239], a[16:19], v[36:39]\n"
    "s_add_u32 s68, s68, 64\n"
    "ds_read_b64_tr_b16 v[234:235], v245 offset:704\n"
    "v_accvgpr_read_b32 v250, a72\n"
    "v_mfma_f32_16x16x32_bf16 v[44:47], v[236:239], a[20:23], v[44:47]\n"
    "s_waitcnt lgkmcnt(4)\n"
    "v_accvgpr_read_b32 v251, a73\n"
    "v_accvgpr_read_b32 v252, a74\n"
    "v_mfma_f32_16x16x32_bf16 v[48:51], v[224:227], a[12:15], v[48:51]\n"
    "s_add_u32 s69, s69, 64\n"
    "ds_read_b64_tr_b16 v[236:237], v245 offset:200\n"
    "v_accvgpr_read_b32 v253, a75\n"
    "v_mfma_f32_16x16x32_bf16 v[56:59], v[224:227], a[16:19], v[56:59]\n"
    "s_add_u32 s49, s49, 16896\n"
    "s_cmp_eq_u32 s49, s50\n"
    "s_cselect_b32 s49, s51, s49\n"
    "s_mov_b32 m0, s49\n"
    "ds_read_b64_tr_b16 v[238:239], v245 offset:712\n"
    "v_mul_f32_e64 v250, v250, v247\n"
    "v_mfma_f32_16x16x32_bf16 v[64:67], v[224:227], a[20:23], v[64:67]\n"
    "s_waitcnt lgkmcnt(4)\n"
    "buffer_load_dwordx4 v240, s[24:27], s54 offen offset:0 lds\n"
    "v_mfma_f32_16x16x32_bf16 v[52:55], v[228:231], a[12:15], v[52:55]\n"
    "s_add_u32 m0, s49, 1032\n"
    "ds_read_b64_tr_b16 v[224:225], v245 offset:256\n"
    "v_mul_f32_e64 v251, v251, v247\n"
    "v_mfma_f32_16x16x32_bf16 v[60:63], v[228:231], a[16:19], v[60:63]\n"
    "s_add_u32 s75, s54, 8192\n"
    "ds_read_b64_tr_b16 v[226:227], v245 offset:768\n"
    "v_mul_f32_e64 v252, v252, v247\n"
    "v_mfma_f32_16x16x32_bf16 v[68:71], v[228:231], a[20:23], v[68:71]\n"
    "s_waitcnt lgkmcnt(4)\n"
    "v_mul_f32_e64 v253, v253, v247\n"
    "v_cvt_pk_bf16_f32 v250, v250, v251\n"
    "v_mfma_f32_16x16x32_bf16 v[72:75], v[232:235], a[12:15], v[72:75]\n"
    "ds_read_b64_tr_b16 v[228:229], v245 offset:264\n"
    "v_cvt_pk_bf16_f32 v252, v252, v253\n"
    "v_mfma_f32_16x16x32_bf16 v[80:83], v[232:235], a[16:19], v[80:83]\n"
    "ds_read_b64_tr_b16 v[230:231], v245 offset:776\n"
    "v_accvgpr_write_b32 a228, v250\n"
    "v_mfma_f32_16x16x32_bf16 v[88:91], v[232:235], a[20:23], v[88:91]\n"
    "s_waitcnt lgkmcnt(4)\n"
    "buffer_load_dwordx4 v240, s[24:27], s75 offen offset:0 lds\n"
    "v_mfma_f32_16x16x32_bf16 v[76:79], v[236:239], a[12:15], v[76:79]\n"
    "s_add_u32 m0, s49, 2112\n"
    "ds_read_b64_tr_b16 v[232:233], v245 offset:320\n"
    "v_accvgpr_write_b32 a229, v252\n"
    "v_mfma_f32_16x16x32_bf16 v[84:87], v[236:239], a[16:19], v[84:87]\n"
    "s_add_u32 s74, s54, 16384\n"
    "ds_read_b64_tr_b16 v[234:235], v245 offset:832\n"
    "v_accvgpr_read_b32 v250, a84\n"
    "v_mfma_f32_16x16x32_bf16 v[92:95], v[236:239], a[20:23], v[92:95]\n"
    "s_waitcnt lgkmcnt(4)\n"
    "v_accvgpr_read_b32 v251, a85\n"
    "v_accvgpr_read_b32 v252, a86\n"
    "v_mfma_f32_16x16x32_bf16 v[96:99], v[224:227], a[12:15], v[96:99]\n"
    "ds_read_b64_tr_b16 v[236:237], v245 offset:328\n"
    "v_accvgpr_read_b32 v253, a87\n"
    "v_mfma_f32_16x16x32_bf16 v[104:107], v[224:227], a[16:19], v[104:107]\n"
    "ds_read_b64_tr_b16 v[238:239], v245 offset:840\n"
    "v_mul_f32_e64 v250, v250, v247\n"
    "v_mfma_f32_16x16x32_bf16 v[112:115], v[224:227], a[20:23], v[112:115]\n"
    "s_waitcnt lgkmcnt(4)\n"
    "buffer_load_dwordx4 v240, s[24:27], s74 offen offset:0 lds\n"
    "v_mfma_f32_16x16x32_bf16 v[100:103], v[228:231], a[12:15], v[100:103]\n"
    "s_add_u32 m0, s49, 3144\n"
    "ds_read_b64_tr_b16 v[224:225], v245 offset:384\n"
    "v_mul_f32_e64 v251, v251, v247\n"
    "v_mfma_f32_16x16x32_bf16 v[108:111], v[228:231], a[16:19], v[108:111]\n"
    "s_add_u32 s75, s54, 24576\n"
    "ds_read_b64_tr_b16 v[226:227], v245 offset:896\n"
    "v_mul_f32_e64 v252, v252, v247\n"
    "v_mfma_f32_16x16x32_bf16 v[116:119], v[228:231], a[20:23], v[116:119]\n"
    "s_waitcnt lgkmcnt(4)\n"
    "v_mul_f32_e64 v253, v253, v247\n"
    "v_cvt_pk_bf16_f32 v250, v250, v251\n"
    "v_mfma_f32_16x16x32_bf16 v[120:123], v[232:235], a[12:15], v[120:123]\n"
    "ds_read_b64_tr_b16 v[228:229], v245 offset:392\n"
    "v_cvt_pk_bf16_f32 v252, v252, v253\n"
    "v_mfma_f32_16x16x32_bf16 v[128:131], v[232:235], a[16:19], v[128:131]\n"
    "ds_read_b64_tr_b16 v[230:231], v245 offset:904\n"
    "v_accvgpr_write_b32 a230, v250\n"
    "v_mfma_f32_16x16x32_bf16 v[136:139], v[232:235], a[20:23], v[136:139]\n"
    "s_waitcnt lgkmcnt(4)\n"
    "buffer_load_dwordx4 v240, s[24:27], s75 offen offset:0 lds\n"
    "v_mfma_f32_16x16x32_bf16 v[124:127], v[236:239], a[12:15], v[124:127]\n"
    "s_add_u32 s54, s54, 262144\n"
    "ds_read_b64_tr_b16 v[232:233], v245 offset:448\n"
    "v_accvgpr_write_b32 a231, v252\n"
    "v_mfma_f32_16x16x32_bf16 v[132:135], v[236:239], a[16:19], v[132:135]\n"
    "s_add_u32 s49, s49, 16896\n"
    "ds_read_b64_tr_b16 v[234:235], v245 offset:960\n"
    "v_accvgpr_read_b32 v250, a76\n"
    "v_mfma_f32_16x16x32_bf16 v[140:143], v[236:239], a[20:23], v[140:143]\n"
    "s_waitcnt lgkmcnt(4)\n"
    "v_accvgpr_read_b32 v251, a77\n"
    "v_accvgpr_read_b32 v252, a78\n"
    "v_mfma_f32_16x16x32_bf16 v[144:147], v[224:227], a[12:15], v[144:147]\n"
    "s_cmp_eq_u32 s49, s50\n"
    "ds_read_b64_tr_b16 v[236:237], v245 offset:456\n"
    "v_accvgpr_read_b32 v253, a79\n"
    "v_mfma_f32_16x16x32_bf16 v[152:155], v[224:227], a[16:19], v[152:155]\n"
    "s_waitcnt vmcnt(14)\n"
    "ds_read_b64_tr_b16 v[238:239], v245 offset:968\n"
    "v_add_u32_e64 v245, s52, v244\n"
    "v_mfma_f32_16x16x32_bf16 v[160:163], v[224:227], a[20:23], v[160:163]\n"
    "s_waitcnt lgkmcnt(4)\n"
    "v_mul_f32_e64 v250, v250, v248\n"
    "v_mul_f32_e64 v251, v251, v248\n"
    "v_mfma_f32_16x16x32_bf16 v[148:151], v[228:231], a[12:15], v[148:151]\n"
    "s_barrier\n"
    "ds_read_b64_tr_b16 v[224:225], v245 offset:0\n"
    "ds_read_b128 a[0:3], v246 offset:0\n"
    "v_mfma_f32_16x16x32_bf16 v[156:159], v[228:231], a[16:19], v[156:159]\n"
    "s_cselect_b32 s49, s51, s49\n"
    "ds_read_b64_tr_b16 v[226:227], v245 offset:512\n"
    "ds_read_b128 a[4:7], v246 offset:1056\n"
    "v_mfma_f32_16x16x32_bf16 v[164:167], v[228:231], a[20:23], v[164:167]\n"
    "s_waitcnt lgkmcnt(6)\n"
    "ds_read_b128 a[8:11], v246 offset:2112\n"
    "v_mul_f32_e64 v252, v252, v248\n"
    "v_mfma_f32_16x16x32_bf16 v[168:171], v[232:235], a[12:15], v[168:171]\n"
    "s_mov_b32 m0, s49\n"
    "ds_read_b64_tr_b16 v[228:229], v245 offset:8\n"
    "v_mul_f32_e64 v253, v253, v248\n"
    "v_mfma_f32_16x16x32_bf16 v[176:179], v[232:235], a[16:19], v[176:179]\n"
    "s_add_u32 s52, s52, 16896\n"
    "ds_read_b64_tr_b16 v[230:231], v245 offset:520\n"
    "v_cvt_pk_bf16_f32 v250, v250, v251\n"
    "v_mfma_f32_16x16x32_bf16 v[184:187], v[232:235], a[20:23], v[184:187]\n"
    "s_waitcnt lgkmcnt(7)\n"
    "v_cvt_pk_bf16_f32 v252, v252, v253\n"
    "v_accvgpr_write_b32 a232, v250\n"
    "v_mfma_f32_16x16x32_bf16 v[172:175], v[236:239], a[12:15], v[172:175]\n"
    "s_cmp_eq_u32 s52, s53\n"
    "ds_read_b64_tr_b16 v[232:233], v245 offset:64\n"
    "v_accvgpr_write_b32 a233, v252\n"
    "v_mfma_f32_16x16x32_bf16 v[180:183], v[236:239], a[16:19], v[180:183]\n"
    "s_cselect_b32 s52, s79, s52\n"
    "ds_read_b64_tr_b16 v[234:235], v245 offset:576\n"
    "v_mfma_f32_16x16x32_bf16 v[188:191], v[236:239], a[20:23], v[188:191]\n"
    "s_waitcnt lgkmcnt(4)\n"
    "v_mfma_f32_16x16x32_bf16 v[0:3], v[224:227], a[0:3], v[0:3]\n"
    "ds_read_b64_tr_b16 v[236:237], v245 offset:72\n"
    "v_accvgpr_read_b32 v250, a88\n"
    "v_mfma_f32_16x16x32_bf16 v[8:11], v[224:227], a[4:7], v[8:11]\n"
    "ds_read_b64_tr_b16 v[238:239], v245 offset:584\n"
    "v_accvgpr_read_b32 v251, a89\n"
    "v_mfma_f32_16x16x32_bf16 v[16:19], v[224:227], a[8:11], v[16:19]\n"
    "s_waitcnt lgkmcnt(4)\n"
    "buffer_load_dwordx4 v242, s[20:23], s67 offen offset:0 lds\n"
    "v_mfma_f32_16x16x32_bf16 v[4:7], v[228:231], a[0:3], v[4:7]\n"
    "ds_read_b64_tr_b16 v[224:225], v245 offset:128\n"
    "v_add_u32_e64 v246, s52, v243\n"
    "v_mfma_f32_16x16x32_bf16 v[12:15], v[228:231], a[4:7], v[12:15]\n"
    "s_add_u32 s52, s52, 16896\n"
    "ds_read_b64_tr_b16 v[226:227], v245 offset:640\n"
    "v_accvgpr_read_b32 v252, a90\n"
    "v_mfma_f32_16x16x32_bf16 v[20:23], v[228:231], a[8:11], v[20:23]\n"
    "s_waitcnt lgkmcnt(4)\n"
    "buffer_load_dwordx4 v242, s[20:23], s68 offen offset:1056 lds\n"
    "v_mfma_f32_16x16x32_bf16 v[24:27], v[232:235], a[0:3], v[24:27]\n"
    "s_cmp_eq_u32 s52, s53\n"
    "ds_read_b64_tr_b16 v[228:229], v245 offset:136\n"
    "v_accvgpr_read_b32 v253, a91\n"
    "v_mfma_f32_16x16x32_bf16 v[32:35], v[232:235], a[4:7], v[32:35]\n"
    "s_cselect_b32 s52, s79, s52\n"
    "ds_read_b64_tr_b16 v[230:231], v245 offset:648\n"
    "v_mul_f32_e64 v250, v250, v248\n"
    "v_mfma_f32_16x16x32_bf16 v[40:43], v[232:235], a[8:11], v[40:43]\n"
    "s_waitcnt lgkmcnt(4)\n"
    "buffer_load_dwordx4 v242, s[20:23], s69 offen offset:2112 lds\n"
    "v_mfma_f32_16x16x32_bf16 v[28:31], v[236:239], a[0:3], v[28:31]\n"
    "s_add_u32 s67, s67, 64\n"
    "ds_read_b64_tr_b16 v[232:233], v245 offset:192\n"
    "v_mul_f32_e64 v251, v251, v248\n"
    "v_mfma_f32_16x16x32_bf16 v[36:39], v[236:239], a[4:7], v[36:39]\n"
    "s_add_u32 s68, s68, 64\n"
    "ds_read_b64_tr_b16 v[234:235], v245 offset:704\n"
    "v_mul_f32_e64 v252, v252, v248\n"
    "v_mfma_f32_16x16x32_bf16 v[44:47], v[236:239], a[8:11], v[44:47]\n"
    "s_waitcnt lgkmcnt(4)\n"
    "v_mul_f32_e64 v253, v253, v248\n"
    "v_cvt_pk_bf16_f32 v250, v250, v251\n"
    "v_mfma_f32_16x16x32_bf16 v[48:51], v[224:227], a[0:3], v[48:51]\n"
    "s_add_u32 s69, s69, 64\n"
    "ds_read_b64_tr_b16 v[236:237], v245 offset:200\n"
    "v_cvt_pk_bf16_f32 v252, v252, v253\n"
    "v_mfma_f32_16x16x32_bf16 v[56:59], v[224:227], a[4:7], v[56:59]\n"
    "s_add_u32 s49, s49, 16896\n"
    "s_cmp_eq_u32 s49, s50\n"
    "s_cselect_b32 s49, s51, s49\n"
    "s_mov_b32 m0, s49\n"
    "ds_read_b64_tr_b16 v[238:239], v245 offset:712\n"
    "v_accvgpr_write_b32 a234, v250\n"
    "v_mfma_f32_16x16x32_bf16 v[64:67], v[224:227], a[8:11], v[64:67]\n"
    "s_waitcnt lgkmcnt(4)\n"
    "buffer_load_dwordx4 v240, s[24:27], s54 offen offset:0 lds\n"
    "v_mfma_f32_16x16x32_bf16 v[52:55], v[228:231], a[0:3], v[52:55]\n"
    "s_add_u32 m0, s49, 1032\n"
    "ds_read_b64_tr_b16 v[224:225], v245 offset:256\n"
    "v_accvgpr_write_b32 a235, v252\n"
    "v_mfma_f32_16x16x32_bf16 v[60:63], v[228:231], a[4:7], v[60:63]\n"
    "s_add_u32 s75, s54, 8192\n"
    "ds_read_b64_tr_b16 v[226:227], v245 offset:768\n"
    "v_accvgpr_read_b32 v250, a80\n"
    "v_mfma_f32_16x16x32_bf16 v[68:71], v[228:231], a[8:11], v[68:71]\n"
    "s_waitcnt lgkmcnt(4)\n"
    "v_accvgpr_read_b32 v251, a81\n"
    "v_accvgpr_read_b32 v252, a82\n"
    "v_mfma_f32_16x16x32_bf16 v[72:75], v[232:235], a[0:3], v[72:75]\n"
    "ds_read_b64_tr_b16 v[228:229], v245 offset:264\n"
    "v_accvgpr_read_b32 v253, a83\n"
    "v_mfma_f32_16x16x32_bf16 v[80:83], v[232:235], a[4:7], v[80:83]\n"
    "ds_read_b64_tr_b16 v[230:231], v245 offset:776\n"
    "v_mul_f32_e64 v250, v250, v249\n"
    "v_mfma_f32_16x16x32_bf16 v[88:91], v[232:235], a[8:11], v[88:91]\n"
    "s_waitcnt lgkmcnt(4)\n"
    "buffer_load_dwordx4 v240, s[24:27], s75 offen offset:0 lds\n"
    "v_mfma_f32_16x16x32_bf16 v[76:79], v[236:239], a[0:3], v[76:79]\n"
    "s_add_u32 m0, s49, 2112\n"
    "ds_read_b64_tr_b16 v[232:233], v245 offset:320\n"
    "v_mul_f32_e64 v251, v251, v249\n"
    "v_mfma_f32_16x16x32_bf16 v[84:87], v[236:239], a[4:7], v[84:87]\n"
    "s_add_u32 s74, s54, 16384\n"
    "ds_read_b64_tr_b16 v[234:235], v245 offset:832\n"
    "v_mul_f32_e64 v252, v252, v249\n"
    "v_mfma_f32_16x16x32_bf16 v[92:95], v[236:239], a[8:11], v[92:95]\n"
    "s_waitcnt lgkmcnt(4)\n"
    "v_mul_f32_e64 v253, v253, v249\n"
    "v_cvt_pk_bf16_f32 v250, v250, v251\n"
    "v_mfma_f32_16x16x32_bf16 v[96:99], v[224:227], a[0:3], v[96:99]\n"
    "ds_read_b64_tr_b16 v[236:237], v245 offset:328\n"
    "v_cvt_pk_bf16_f32 v252, v252, v253\n"
    "v_mfma_f32_16x16x32_bf16 v[104:107], v[224:227], a[4:7], v[104:107]\n"
    "ds_read_b64_tr_b16 v[238:239], v245 offset:840\n"
    "v_accvgpr_write_b32 a236, v250\n"
    "v_mfma_f32_16x16x32_bf16 v[112:115], v[224:227], a[8:11], v[112:115]\n"
    "s_waitcnt lgkmcnt(4)\n"
    "buffer_load_dwordx4 v240, s[24:27], s74 offen offset:0 lds\n"
    "v_mfma_f32_16x16x32_bf16 v[100:103], v[228:231], a[0:3], v[100:103]\n"
    "s_add_u32 m0, s49, 3144\n"
    "ds_read_b64_tr_b16 v[224:225], v245 offset:384\n"
    "v_accvgpr_write_b32 a237, v252\n"
    "v_mfma_f32_16x16x32_bf16 v[108:111], v[228:231], a[4:7], v[108:111]\n"
    "s_add_u32 s75, s54, 24576\n"
    "ds_read_b64_tr_b16 v[226:227], v245 offset:896\n"
    "v_accvgpr_read_b32 v250, a92\n"
    "v_mfma_f32_16x16x32_bf16 v[116:119], v[228:231], a[8:11], v[116:119]\n"
    "s_waitcnt lgkmcnt(4)\n"
    "v_accvgpr_read_b32 v251, a93\n"
    "v_accvgpr_read_b32 v252, a94\n"
    "v_mfma_f32_16x16x32_bf16 v[120:123], v[232:235], a[0:3], v[120:123]\n"
    "ds_read_b64_tr_b16 v[228:229], v245 offset:392\n"
    "v_accvgpr_read_b32 v253, a95\n"
    "v_mfma_f32_16x16x32_bf16 v[128:131], v[232:235], a[4:7], v[128:131]\n"
    "ds_read_b64_tr_b16 v[230:231], v245 offset:904\n"
    "v_mul_f32_e64 v250, v250, v249\n"
    "v_mfma_f32_16x16x32_bf16 v[136:139], v[232:235], a[8:11], v[136:139]\n"
    "s_waitcnt lgkmcnt(4)\n"
    "buffer_load_dwordx4 v240, s[24:27], s75 offen offset:0 lds\n"
    "v_mfma_f32_16x16x32_bf16 v[124:127], v[236:239], a[0:3], v[124:127]\n"
    "s_add_u32 s54, s54, 262144\n"
    "ds_read_b64_tr_b16 v[232:233], v245 offset:448\n"
    "v_mul_f32_e64 v251, v251, v249\n"
    "v_mfma_f32_16x16x32_bf16 v[132:135], v[236:239], a[4:7], v[132:135]\n"
    "s_add_u32 s49, s49, 16896\n"
    "ds_read_b64_tr_b16 v[234:235], v245 offset:960\n"
    "v_mul_f32_e64 v252, v252, v249\n"
    "v_mfma_f32_16x16x32_bf16 v[140:143], v[236:239], a[8:11], v[140:143]\n"
    "s_waitcnt lgkmcnt(4)\n"
    "v_mul_f32_e64 v253, v253, v249\n"
    "v_cvt_pk_bf16_f32 v250, v250, v251\n"
    "v_mfma_f32_16x16x32_bf16 v[144:147], v[224:227], a[0:3], v[144:147]\n"
    "s_cmp_eq_u32 s49, s50\n"
    "ds_read_b64_tr_b16 v[236:237], v245 offset:456\n"
    "v_cvt_pk_bf16_f32 v252, v252, v253\n"
    "v_mfma_f32_16x16x32_bf16 v[152:155], v[224:227], a[4:7], v[152:155]\n"
    "s_waitcnt vmcnt(14)\n"
    "ds_read_b64_tr_b16 v[238:239], v245 offset:968\n"
    "v_add_u32_e64 v245, s52, v244\n"
    "v_mfma_f32_16x16x32_bf16 v[160:163], v[224:227], a[8:11], v[160:163]\n"
    "s_waitcnt lgkmcnt(4)\n"
    "v_accvgpr_write_b32 a238, v250\n"
    "v_accvgpr_write_b32 a239, v252\n"
    "v_mfma_f32_16x16x32_bf16 v[148:151], v[228:231], a[0:3], v[148:151]\n"
    "s_barrier\n"
    "ds_read_b64_tr_b16 v[224:225], v245 offset:0\n"
    "ds_read_b128 a[12:15], v246 offset:0\n"
    "v_mfma_f32_16x16x32_bf16 v[156:159], v[228:231], a[4:7], v[156:159]\n"
    "s_cselect_b32 s49, s51, s49\n"
    "ds_read_b64_tr_b16 v[226:227], v245 offset:512\n"
    "ds_read_b128 a[16:19], v246 offset:1056\n"
    "v_mfma_f32_16x16x32_bf16 v[164:167], v[228:231], a[8:11], v[164:167]\n"
    "s_waitcnt lgkmcnt(6)\n"
    "ds_read_b128 a[20:23], v246 offset:2112\n"
    "v_accvgpr_read_b32 v250, a96\n"
    "v_mfma_f32_16x16x32_bf16 v[168:171], v[232:235], a[0:3], v[168:171]\n"
    "s_mov_b32 m0, s49\n"
    "ds_read_b64_tr_b16 v[228:229], v245 offset:8\n"
    "v_accvgpr_read_b32 v251, a97\n"
    "v_mfma_f32_16x16x32_bf16 v[176:179], v[232:235], a[4:7], v[176:179]\n"
    "s_add_u32 s52, s52, 16896\n"
    "ds_read_b64_tr_b16 v[230:231], v245 offset:520\n"
    "v_accvgpr_read_b32 v252, a98\n"
    "v_mfma_f32_16x16x32_bf16 v[184:187], v[232:235], a[8:11], v[184:187]\n"
    "s_waitcnt lgkmcnt(7)\n"
    "v_accvgpr_read_b32 v253, a99\n"
    "v_mul_f32_e64 v250, v250, v247\n"
    "v_mfma_f32_16x16x32_bf16 v[172:175], v[236:239], a[0:3], v[172:175]\n"
    "s_cmp_eq_u32 s52, s53\n"
    "ds_read_b64_tr_b16 v[232:233], v245 offset:64\n"
    "v_mul_f32_e64 v251, v251, v247\n"
    "v_mfma_f32_16x16x32_bf16 v[180:183], v[236:239], a[4:7], v[180:183]\n"
    "s_cselect_b32 s52, s79, s52\n"
    "ds_read_b64_tr_b16 v[234:235], v245 offset:576\n"
    "v_mfma_f32_16x16x32_bf16 v[188:191], v[236:239], a[8:11], v[188:191]\n"
    "s_waitcnt lgkmcnt(4)\n"
    "v_mfma_f32_16x16x32_bf16 v[0:3], v[224:227], a[12:15], v[0:3]\n"
    "ds_read_b64_tr_b16 v[236:237], v245 offset:72\n"
    "v_mul_f32_e64 v252, v252, v247\n"
    "v_mfma_f32_16x16x32_bf16 v[8:11], v[224:227], a[16:19], v[8:11]\n"
    "ds_read_b64_tr_b16 v[238:239], v245 offset:584\n"
    "v_mul_f32_e64 v253, v253, v247\n"
    "v_mfma_f32_16x16x32_bf16 v[16:19], v[224:227], a[20:23], v[16:19]\n"
    "s_waitcnt lgkmcnt(4)\n"
    "buffer_load_dwordx4 v242, s[20:23], s67 offen offset:0 lds\n"
    "v_mfma_f32_16x16x32_bf16 v[4:7], v[228:231], a[12:15], v[4:7]\n"
    "ds_read_b64_tr_b16 v[224:225], v245 offset:128\n"
    "v_add_u32_e64 v246, s52, v243\n"
    "v_mfma_f32_16x16x32_bf16 v[12:15], v[228:231], a[16:19], v[12:15]\n"
    "s_add_u32 s52, s52, 16896\n"
    "ds_read_b64_tr_b16 v[226:227], v245 offset:640\n"
    "v_cvt_pk_bf16_f32 v250, v250, v251\n"
    "v_mfma_f32_16x16x32_bf16 v[20:23], v[228:231], a[20:23], v[20:23]\n"
    "s_waitcnt lgkmcnt(4)\n"
    "buffer_load_dwordx4 v242, s[20:23], s68 offen offset:1056 lds\n"
    "v_mfma_f32_16x16x32_bf16 v[24:27], v[232:235], a[12:15], v[24:27]\n"
    "s_cmp_eq_u32 s52, s53\n"
    "ds_read_b64_tr_b16 v[228:229], v245 offset:136\n"
    "v_cvt_pk_bf16_f32 v252, v252, v253\n"
    "v_mfma_f32_16x16x32_bf16 v[32:35], v[232:235], a[16:19], v[32:35]\n"
    "s_cselect_b32 s52, s79, s52\n"
    "ds_read_b64_tr_b16 v[230:231], v245 offset:648\n"
    "v_accvgpr_write_b32 a240, v250\n"
    "v_mfma_f32_16x16x32_bf16 v[40:43], v[232:235], a[20:23], v[40:43]\n"
    "s_waitcnt lgkmcnt(4)\n"
    "buffer_load_dwordx4 v242, s[20:23], s69 offen offset:2112 lds\n"
    "v_mfma_f32_16x16x32_bf16 v[28:31], v[236:239], a[12:15], v[28:31]\n"
    "s_add_u32 s67, s67, 64\n"
    "ds_read_b64_tr_b16 v[232:233], v245 offset:192\n"
    "v_accvgpr_write_b32 a241, v252\n"
    "v_mfma_f32_16x16x32_bf16 v[36:39], v[236:239], a[16:19], v[36:39]\n"
    "s_add_u32 s68, s68, 64\n"
    "ds_read_b64_tr_b16 v[234:235], v245 offset:704\n"
    "v_accvgpr_read_b32 v250, a108\n"
    "v_mfma_f32_16x16x32_bf16 v[44:47], v[236:239], a[20:23], v[44:47]\n"
    "s_waitcnt lgkmcnt(4)\n"
    "v_accvgpr_read_b32 v251, a109\n"
    "v_accvgpr_read_b32 v252, a110\n"
    "v_mfma_f32_16x16x32_bf16 v[48:51], v[224:227], a[12:15], v[48:51]\n"
    "s_add_u32 s69, s69, 64\n"
    "ds_read_b64_tr_b16 v[236:237], v245 offset:200\n"
    "v_accvgpr_read_b32 v253, a111\n"
    "v_mfma_f32_16x16x32_bf16 v[56:59], v[224:227], a[16:19], v[56:59]\n"
    "s_add_u32 s49, s49, 16896\n"
    "s_cmp_eq_u32 s49, s50\n"
    "s_cselect_b32 s49, s51, s49\n"
    "s_mov_b32 m0, s49\n"
    "ds_read_b64_tr_b16 v[238:239], v245 offset:712\n"
    "v_mul_f32_e64 v250, v250, v247\n"
    "v_mfma_f32_16x16x32_bf16 v[64:67], v[224:227], a[20:23], v[64:67]\n"
    "s_waitcnt lgkmcnt(4)\n"
    "buffer_load_dwordx4 v240, s[24:27], s54 offen offset:0 lds\n"
    "v_mfma_f32_16x16x32_bf16 v[52:55], v[228:231], a[12:15], v[52:55]\n"
    "s_add_u32 m0, s49, 1032\n"
    "ds_read_b64_tr_b16 v[224:225], v245 offset:256\n"
    "v_mul_f32_e64 v251, v251, v247\n"
    "v_mfma_f32_16x16x32_bf16 v[60:63], v[228:231], a[16:19], v[60:63]\n"
    "s_add_u32 s75, s54, 8192\n"
    "ds_read_b64_tr_b16 v[226:227], v245 offset:768\n"
    "v_mul_f32_e64 v252, v252, v247\n"
    "v_mfma_f32_16x16x32_bf16 v[68:71], v[228:231], a[20:23], v[68:71]\n"
    "s_waitcnt lgkmcnt(4)\n"
    "v_mul_f32_e64 v253, v253, v247\n"
    "v_cvt_pk_bf16_f32 v250, v250, v251\n"
    "v_mfma_f32_16x16x32_bf16 v[72:75], v[232:235], a[12:15], v[72:75]\n"
    "ds_read_b64_tr_b16 v[228:229], v245 offset:264\n"
    "v_cvt_pk_bf16_f32 v252, v252, v253\n"
    "v_mfma_f32_16x16x32_bf16 v[80:83], v[232:235], a[16:19], v[80:83]\n"
    "ds_read_b64_tr_b16 v[230:231], v245 offset:776\n"
    "v_accvgpr_write_b32 a242, v250\n"
    "v_mfma_f32_16x16x32_bf16 v[88:91], v[232:235], a[20:23], v[88:91]\n"
    "s_waitcnt lgkmcnt(4)\n"
    "buffer_load_dwordx4 v240, s[24:27], s75 offen offset:0 lds\n"
    "v_mfma_f32_16x16x32_bf16 v[76:79], v[236:239], a[12:15], v[76:79]\n"
    "s_add_u32 m0, s49, 2112\n"
    "ds_read_b64_tr_b16 v[232:233], v245 offset:320\n"
    "v_accvgpr_write_b32 a243, v252\n"
    "v_mfma_f32_16x16x32_bf16 v[84:87], v[236:239], a[16:19], v[84:87]\n"
    "s_add_u32 s74, s54, 16384\n"
    "ds_read_b64_tr_b16 v[234:235], v245 offset:832\n"
    "v_accvgpr_read_b32 v250, a100\n"
    "v_mfma_f32_16x16x32_bf16 v[92:95], v[236:239], a[20:23], v[92:95]\n"
    "s_waitcnt lgkmcnt(4)\n"
    "v_accvgpr_read_b32 v251, a101\n"
    "v_accvgpr_read_b32 v252, a102\n"
    "v_mfma_f32_16x16x32_bf16 v[96:99], v[224:227], a[12:15], v[96:99]\n"
    "ds_read_b64_tr_b16 v[236:237], v245 offset:328\n"
    "v_accvgpr_read_b32 v253, a103\n"
    "v_mfma_f32_16x16x32_bf16 v[104:107], v[224:227], a[16:19], v[104:107]\n"
    "ds_read_b64_tr_b16 v[238:239], v245 offset:840\n"
    "v_mul_f32_e64 v250, v250, v248\n"
    "v_mfma_f32_16x16x32_bf16 v[112:115], v[224:227], a[20:23], v[112:115]\n"
    "s_waitcnt lgkmcnt(4)\n"
    "buffer_load_dwordx4 v240, s[24:27], s74 offen offset:0 lds\n"
    "v_mfma_f32_16x16x32_bf16 v[100:103], v[228:231], a[12:15], v[100:103]\n"
    "s_add_u32 m0, s49, 3144\n"
    "ds_read_b64_tr_b16 v[224:225], v245 offset:384\n"
    "v_mul_f32_e64 v251, v251, v248\n"
    "v_mfma_f32_16x16x32_bf16 v[108:111], v[228:231], a[16:19], v[108:111]\n"
    "s_add_u32 s75, s54, 24576\n"
    "ds_read_b64_tr_b16 v[226:227], v245 offset:896\n"
    "v_mul_f32_e64 v252, v252, v248\n"
    "v_mfma_f32_16x16x32_bf16 v[116:119], v[228:231], a[20:23], v[116:119]\n"
    "s_waitcnt lgkmcnt(4)\n"
    "v_mul_f32_e64 v253, v253, v248\n"
    "v_cvt_pk_bf16_f32 v250, v250, v251\n"
    "v_mfma_f32_16x16x32_bf16 v[120:123], v[232:235], a[12:15], v[120:123]\n"
    "ds_read_b64_tr_b16 v[228:229], v245 offset:392\n"
    "v_cvt_pk_bf16_f32 v252, v252, v253\n"
    "v_mfma_f32_16x16x32_bf16 v[128:131], v[232:235], a[16:19], v[128:131]\n"
    "ds_read_b64_tr_b16 v[230:231], v245 offset:904\n"
    "v_accvgpr_write_b32 a244, v250\n"
    "v_mfma_f32_16x16x32_bf16 v[136:139], v[232:235], a[20:23], v[136:139]\n"
    "s_waitcnt lgkmcnt(4)\n"
    "buffer_load_dwordx4 v240, s[24:27], s75 offen offset:0 lds\n"
    "v_mfma_f32_16x16x32_bf16 v[124:127], v[236:239], a[12:15], v[124:127]\n"
    "s_add_u32 s54, s54, 262144\n"
    "ds_read_b64_tr_b16 v[232:233], v245 offset:448\n"
    "v_accvgpr_write_b32 a245, v252\n"
    "v_mfma_f32_16x16x32_bf16 v[132:135], v[236:239], a[16:19], v[132:135]\n"
    "s_add_u32 s49, s49, 16896\n"
    "ds_read_b64_tr_b16 v[234:235], v245 offset:960\n"
    "v_accvgpr_read_b32 v250, a112\n"
    "v_mfma_f32_16x16x32_bf16 v[140:143], v[236:239], a[20:23], v[140:143]\n"
    "s_waitcnt lgkmcnt(4)\n"
    "v_accvgpr_read_b32 v251, a113\n"
    "v_accvgpr_read_b32 v252, a114\n"
    "v_mfma_f32_16x16x32_bf16 v[144:147], v[224:227], a[12:15], v[144:147]\n"
    "s_cmp_eq_u32 s49, s50\n"
    "ds_read_b64_tr_b16 v[236:237], v245 offset:456\n"
    "v_accvgpr_read_b32 v253, a115\n"
    "v_mfma_f32_16x16x32_bf16 v[152:155], v[224:227], a[16:19], v[152:155]\n"
    "s_waitcnt vmcnt(14)\n"
    "ds_read_b64_tr_b16 v[238:239], v245 offset:968\n"
    "v_add_u32_e64 v245, s52, v244\n"
    "v_mfma_f32_16x16x32_bf16 v[160:163], v[224:227], a[20:23], v[160:163]\n"
    "s_waitcnt lgkmcnt(4)\n"
    "v_mul_f32_e64 v250, v250, v248\n"
    "v_mul_f32_e64 v251, v251, v248\n"
    "v_mfma_f32_16x16x32_bf16 v[148:151], v[228:231], a[12:15], v[148:151]\n"
    "s_barrier\n"
    "ds_read_b64_tr_b16 v[224:225], v245 offset:0\n"
    "ds_read_b128 a[0:3], v246 offset:0\n"
    "v_mfma_f32_16x16x32_bf16 v[156:159], v[228:231], a[16:19], v[156:159]\n"
    "s_cselect_b32 s49, s51, s49\n"
    "ds_read_b64_tr_b16 v[226:227], v245 offset:512\n"
    "ds_read_b128 a[4:7], v246 offset:1056\n"
    "v_mfma_f32_16x16x32_bf16 v[164:167], v[228:231], a[20:23], v[164:167]\n"
    "s_waitcnt lgkmcnt(6)\n"
    "ds_read_b128 a[8:11], v246 offset:2112\n"
    "v_mul_f32_e64 v252, v252, v248\n"
    "v_mfma_f32_16x16x32_bf16 v[168:171], v[232:235], a[12:15], v[168:171]\n"
    "s_mov_b32 m0, s49\n"
    "ds_read_b64_tr_b16 v[228:229], v245 offset:8\n"
    "v_mul_f32_e64 v253, v253, v248\n"
    "v_mfma_f32_16x16x32_bf16 v[176:179], v[232:235], a[16:19], v[176:179]\n"
    "s_add_u32 s52, s52, 16896\n"
    "ds_read_b64_tr_b16 v[230:231], v245 offset:520\n"
    "v_cvt_pk_bf16_f32 v250, v250, v251\n"
    "v_mfma_f32_16x16x32_bf16 v[184:187], v[232:235], a[20:23], v[184:187]\n"
    "s_waitcnt lgkmcnt(7)\n"
    "v_cvt_pk_bf16_f32 v252, v252, v253\n"
    "v_accvgpr_write_b32 a246, v250\n"
    "v_mfma_f32_16x16x32_bf16 v[172:175], v[236:239], a[12:15], v[172:175]\n"
    "s_cmp_eq_u32 s52, s53\n"
    "ds_read_b64_tr_b16 v[232:233], v245 offset:64\n"
    "v_accvgpr_write_b32 a247, v252\n"
    "v_mfma_f32_16x16x32_bf16 v[180:183], v[236:239], a[16:19], v[180:183]\n"
    "s_cselect_b32 s52, s79, s52\n"
    "ds_read_b64_tr_b16 v[234:235], v245 offset:576\n"
    "v_mfma_f32_16x16x32_bf16 v[188:191], v[236:239], a[20:23], v[188:191]\n"
    "s_waitcnt lgkmcnt(4)\n"
    "v_mfma_f32_16x16x32_bf16 v[0:3], v[224:227], a[0:3], v[0:3]\n"
    "ds_read_b64_tr_b16 v[236:237], v245 offset:72\n"
    "v_accvgpr_read_b32 v250, a104\n"
    "v_mfma_f32_16x16x32_bf16 v[8:11], v[224:227], a[4:7], v[8:11]\n"
    "ds_read_b64_tr_b16 v[238:239], v245 offset:584\n"
    "v_accvgpr_read_b32 v251, a105\n"
    "v_mfma_f32_16x16x32_bf16 v[16:19], v[224:227], a[8:11], v[16:19]\n"
    "s_waitcnt lgkmcnt(4)\n"
    "buffer_load_dwordx4 v242, s[20:23], s67 offen offset:0 lds\n"
    "v_mfma_f32_16x16x32_bf16 v[4:7], v[228:231], a[0:3], v[4:7]\n"
    "ds_read_b64_tr_b16 v[224:225], v245 offset:128\n"
    "v_add_u32_e64 v246, s52, v243\n"
    "v_mfma_f32_16x16x32_bf16 v[12:15], v[228:231], a[4:7], v[12:15]\n"
    "s_add_u32 s52, s52, 16896\n"
    "ds_read_b64_tr_b16 v[226:227], v245 offset:640\n"
    "v_accvgpr_read_b32 v252, a106\n"
    "v_mfma_f32_16x16x32_bf16 v[20:23], v[228:231], a[8:11], v[20:23]\n"
    "s_waitcnt lgkmcnt(4)\n"
    "buffer_load_dwordx4 v242, s[20:23], s68 offen offset:1056 lds\n"
    "v_mfma_f32_16x16x32_bf16 v[24:27], v[232:235], a[0:3], v[24:27]\n"
    "s_cmp_eq_u32 s52, s53\n"
    "ds_read_b64_tr_b16 v[228:229], v245 offset:136\n"
    "v_accvgpr_read_b32 v253, a107\n"
    "v_mfma_f32_16x16x32_bf16 v[32:35], v[232:235], a[4:7], v[32:35]\n"
    "s_cselect_b32 s52, s79, s52\n"
    "ds_read_b64_tr_b16 v[230:231], v245 offset:648\n"
    "v_mul_f32_e64 v250, v250, v249\n"
    "v_mfma_f32_16x16x32_bf16 v[40:43], v[232:235], a[8:11], v[40:43]\n"
    "s_waitcnt lgkmcnt(4)\n"
    "buffer_load_dwordx4 v242, s[20:23], s69 offen offset:2112 lds\n"
    "v_mfma_f32_16x16x32_bf16 v[28:31], v[236:239], a[0:3], v[28:31]\n"
    "s_add_u32 s67, s67, 64\n"
    "ds_read_b64_tr_b16 v[232:233], v245 offset:192\n"
    "v_mul_f32_e64 v251, v251, v249\n"
    "v_mfma_f32_16x16x32_bf16 v[36:39], v[236:239], a[4:7], v[36:39]\n"
    "s_add_u32 s68, s68, 64\n"
    "ds_read_b64_tr_b16 v[234:235], v245 offset:704\n"
    "v_mul_f32_e64 v252, v252, v249\n"
    "v_mfma_f32_16x16x32_bf16 v[44:47], v[236:239], a[8:11], v[44:47]\n"
    "s_waitcnt lgkmcnt(4)\n"
    "v_mul_f32_e64 v253, v253, v249\n"
    "v_cvt_pk_bf16_f32 v250, v250, v251\n"
    "v_mfma_f32_16x16x32_bf16 v[48:51], v[224:227], a[0:3], v[48:51]\n"
    "s_add_u32 s69, s69, 64\n"
    "ds_read_b64_tr_b16 v[236:237], v245 offset:200\n"
    "v_cvt_pk_bf16_f32 v252, v252, v253\n"
    "v_mfma_f32_16x16x32_bf16 v[56:59], v[224:227], a[4:7], v[56:59]\n"
    "s_add_u32 s49, s49, 16896\n"
    "s_cmp_eq_u32 s49, s50\n"
    "s_cselect_b32 s49, s51, s49\n"
    "s_mov_b32 m0, s49\n"
    "ds_read_b64_tr_b16 v[238:239], v245 offset:712\n"
    "v_accvgpr_write_b32 a248, v250\n"
    "v_mfma_f32_16x16x32_bf16 v[64:67], v[224:227], a[8:11], v[64:67]\n"
    "s_waitcnt lgkmcnt(4)\n"
    "buffer_load_dwordx4 v240, s[24:27], s54 offen offset:0 lds\n"
    "v_mfma_f32_16x16x32_bf16 v[52:55], v[228:231], a[0:3], v[52:55]\n"
    "s_add_u32 m0, s49, 1032\n"
    "ds_read_b64_tr_b16 v[224:225], v245 offset:256\n"
    "v_accvgpr_write_b32 a249, v252\n"
    "v_mfma_f32_16x16x32_bf16 v[60:63], v[228:231], a[4:7], v[60:63]\n"
    "s_add_u32 s75, s54, 8192\n"
    "ds_read_b64_tr_b16 v[226:227], v245 offset:768\n"
    "v_accvgpr_read_b32 v250, a116\n"
    "v_mfma_f32_16x16x32_bf16 v[68:71], v[228:231], a[8:11], v[68:71]\n"
    "s_waitcnt lgkmcnt(4)\n"
    "v_accvgpr_read_b32 v251, a117\n"
    "v_accvgpr_read_b32 v252, a118\n"
    "v_mfma_f32_16x16x32_bf16 v[72:75], v[232:235], a[0:3], v[72:75]\n"
    "ds_read_b64_tr_b16 v[228:229], v245 offset:264\n"
    "v_accvgpr_read_b32 v253, a119\n"
    "v_mfma_f32_16x16x32_bf16 v[80:83], v[232:235], a[4:7], v[80:83]\n"
    "ds_read_b64_tr_b16 v[230:231], v245 offset:776\n"
    "v_mul_f32_e64 v250, v250, v249\n"
    "v_mfma_f32_16x16x32_bf16 v[88:91], v[232:235], a[8:11], v[88:91]\n"
    "s_waitcnt lgkmcnt(4)\n"
    "buffer_load_dwordx4 v240, s[24:27], s75 offen offset:0 lds\n"
    "v_mfma_f32_16x16x32_bf16 v[76:79], v[236:239], a[0:3], v[76:79]\n"
    "s_add_u32 m0, s49, 2112\n"
    "ds_read_b64_tr_b16 v[232:233], v245 offset:320\n"
    "v_mul_f32_e64 v251, v251, v249\n"
    "v_mfma_f32_16x16x32_bf16 v[84:87], v[236:239], a[4:7], v[84:87]\n"
    "s_add_u32 s74, s54, 16384\n"
    "ds_read_b64_tr_b16 v[234:235], v245 offset:832\n"
    "v_mul_f32_e64 v252, v252, v249\n"
    "v_mfma_f32_16x16x32_bf16 v[92:95], v[236:239], a[8:11], v[92:95]\n"
    "s_waitcnt lgkmcnt(4)\n"
    "v_mul_f32_e64 v253, v253, v249\n"
    "v_cvt_pk_bf16_f32 v250, v250, v251\n"
    "v_mfma_f32_16x16x32_bf16 v[96:99], v[224:227], a[0:3], v[96:99]\n"
    "ds_read_b64_tr_b16 v[236:237], v245 offset:328\n"
    "v_cvt_pk_bf16_f32 v252, v252, v253\n"
    "v_mfma_f32_16x16x32_bf16 v[104:107], v[224:227], a[4:7], v[104:107]\n"
    "ds_read_b64_tr_b16 v[238:239], v245 offset:840\n"
    "v_accvgpr_write_b32 a250, v250\n"
    "v_mfma_f32_16x16x32_bf16 v[112:115], v[224:227], a[8:11], v[112:115]\n"
    "s_waitcnt lgkmcnt(4)\n"
    "buffer_load_dwordx4 v240, s[24:27], s74 offen offset:0 lds\n"
    "v_mfma_f32_16x16x32_bf16 v[100:103], v[228:231], a[0:3], v[100:103]\n"
    "s_add_u32 m0, s49, 3144\n"
    "ds_read_b64_tr_b16 v[224:225], v245 offset:384\n"
    "v_accvgpr_write_b32 a251, v252\n"
    "v_mfma_f32_16x16x32_bf16 v[108:111], v[228:231], a[4:7], v[108:111]\n"
    "s_add_u32 s75, s54, 24576\n"
    "ds_read_b64_tr_b16 v[226:227], v245 offset:896\n"
    "v_accvgpr_read_b32 v250, a120\n"
    "v_mfma_f32_16x16x32_bf16 v[116:119], v[228:231], a[8:11], v[116:119]\n"
    "s_waitcnt lgkmcnt(4)\n"
    "v_accvgpr_read_b32 v251, a121\n"
    "v_accvgpr_read_b32 v252, a122\n"
    "v_mfma_f32_16x16x32_bf16 v[120:123], v[232:235], a[0:3], v[120:123]\n"
    "ds_read_b64_tr_b16 v[228:229], v245 offset:392\n"
    "v_accvgpr_read_b32 v253, a123\n"
    "v_mfma_f32_16x16x32_bf16 v[128:131], v[232:235], a[4:7], v[128:131]\n"
    "ds_read_b64_tr_b16 v[230:231], v245 offset:904\n"
    "v_mul_f32_e64 v250, v250, v247\n"
    "v_mfma_f32_16x16x32_bf16 v[136:139], v[232:235], a[8:11], v[136:139]\n"
    "s_waitcnt lgkmcnt(4)\n"
    "buffer_load_dwordx4 v240, s[24:27], s75 offen offset:0 lds\n"
    "v_mfma_f32_16x16x32_bf16 v[124:127], v[236:239], a[0:3], v[124:127]\n"
    "s_add_u32 s54, s54, 262144\n"
    "ds_read_b64_tr_b16 v[232:233], v245 offset:448\n"
    "v_mul_f32_e64 v251, v251, v247\n"
    "v_mfma_f32_16x16x32_bf16 v[132:135], v[236:239], a[4:7], v[132:135]\n"
    "s_add_u32 s49, s49, 16896\n"
    "ds_read_b64_tr_b16 v[234:235], v245 offset:960\n"
    "v_mul_f32_e64 v252, v252, v247\n"
    "v_mfma_f32_16x16x32_bf16 v[140:143], v[236:239], a[8:11], v[140:143]\n"
    "s_waitcnt lgkmcnt(4)\n"
    "v_mul_f32_e64 v253, v253, v247\n"
    "v_cvt_pk_bf16_f32 v250, v250, v251\n"
    "v_mfma_f32_16x16x32_bf16 v[144:147], v[224:227], a[0:3], v[144:147]\n"
    "s_cmp_eq_u32 s49, s50\n"
    "ds_read_b64_tr_b16 v[236:237], v245 offset:456\n"
    "v_cvt_pk_bf16_f32 v252, v252, v253\n"
    "v_mfma_f32_16x16x32_bf16 v[152:155], v[224:227], a[4:7], v[152:155]\n"
    "s_waitcnt vmcnt(14)\n"
    "ds_read_b64_tr_b16 v[238:239], v245 offset:968\n"
    "v_add_u32_e64 v245, s52, v244\n"
    "v_mfma_f32_16x16x32_bf16 v[160:163], v[224:227], a[8:11], v[160:163]\n"
    "s_waitcnt lgkmcnt(4)\n"
    "v_accvgpr_write_b32 a252, v250\n"
    "v_accvgpr_write_b32 a253, v252\n"
    "v_mfma_f32_16x16x32_bf16 v[148:151], v[228:231], a[0:3], v[148:151]\n"
    "s_barrier\n"
    "ds_read_b64_tr_b16 v[224:225], v245 offset:0\n"
    "ds_read_b128 a[12:15], v246 offset:0\n"
    "v_mfma_f32_16x16x32_bf16 v[156:159], v[228:231], a[4:7], v[156:159]\n"
    "s_cselect_b32 s49, s51, s49\n"
    "ds_read_b64_tr_b16 v[226:227], v245 offset:512\n"
    "ds_read_b128 a[16:19], v246 offset:1056\n"
    "v_mfma_f32_16x16x32_bf16 v[164:167], v[228:231], a[8:11], v[164:167]\n"
    "s_waitcnt lgkmcnt(6)\n"
    "ds_read_b128 a[20:23], v246 offset:2112\n"
    "v_accvgpr_read_b32 v250, a132\n"
    "v_mfma_f32_16x16x32_bf16 v[168:171], v[232:235], a[0:3], v[168:171]\n"
    "s_mov_b32 m0, s49\n"
    "ds_read_b64_tr_b16 v[228:229], v245 offset:8\n"
    "v_accvgpr_read_b32 v251, a133\n"
    "v_mfma_f32_16x16x32_bf16 v[176:179], v[232:235], a[4:7], v[176:179]\n"
    "s_add_u32 s52, s52, 16896\n"
    "ds_read_b64_tr_b16 v[230:231], v245 offset:520\n"
    "v_accvgpr_read_b32 v252, a134\n"
    "v_mfma_f32_16x16x32_bf16 v[184:187], v[232:235], a[8:11], v[184:187]\n"
    "s_waitcnt lgkmcnt(7)\n"
    "v_accvgpr_read_b32 v253, a135\n"
    "v_mul_f32_e64 v250, v250, v247\n"
    "v_mfma_f32_16x16x32_bf16 v[172:175], v[236:239], a[0:3], v[172:175]\n"
    "s_cmp_eq_u32 s52, s53\n"
    "ds_read_b64_tr_b16 v[232:233], v245 offset:64\n"
    "v_mul_f32_e64 v251, v251, v247\n"
    "v_mfma_f32_16x16x32_bf16 v[180:183], v[236:239], a[4:7], v[180:183]\n"
    "s_cselect_b32 s52, s79, s52\n"
    "ds_read_b64_tr_b16 v[234:235], v245 offset:576\n"
    "v_mfma_f32_16x16x32_bf16 v[188:191], v[236:239], a[8:11], v[188:191]\n"
    "s_waitcnt lgkmcnt(4)\n"
    "v_mfma_f32_16x16x32_bf16 v[0:3], v[224:227], a[12:15], v[0:3]\n"
    "ds_read_b64_tr_b16 v[236:237], v245 offset:72\n"
    "v_mul_f32_e64 v252, v252, v247\n"
    "v_mfma_f32_16x16x32_bf16 v[8:11], v[224:227], a[16:19], v[8:11]\n"
    "ds_read_b64_tr_b16 v[238:239], v245 offset:584\n"
    "v_mul_f32_e64 v253, v253, v247\n"
    "v_mfma_f32_16x16x32_bf16 v[16:19], v[224:227], a[20:23], v[16:19]\n"
    "s_waitcnt lgkmcnt(4)\n"
    "buffer_load_dwordx4 v242, s[20:23], s67 offen offset:0 lds\n"
    "v_mfma_f32_16x16x32_bf16 v[4:7], v[228:231], a[12:15], v[4:7]\n"
    "ds_read_b64_tr_b16 v[224:225], v245 offset:128\n"
    "v_add_u32_e64 v246, s52, v243\n"
    "v_mfma_f32_16x16x32_bf16 v[12:15], v[228:231], a[16:19], v[12:15]\n"
    "s_add_u32 s52, s52, 16896\n"
    "ds_read_b64_tr_b16 v[226:227], v245 offset:640\n"
    "v_cvt_pk_bf16_f32 v250, v250, v251\n"
    "v_mfma_f32_16x16x32_bf16 v[20:23], v[228:231], a[20:23], v[20:23]\n"
    "s_waitcnt lgkmcnt(4)\n"
    "buffer_load_dwordx4 v242, s[20:23], s68 offen offset:1056 lds\n"
    "v_mfma_f32_16x16x32_bf16 v[24:27], v[232:235], a[12:15], v[24:27]\n"
    "s_cmp_eq_u32 s52, s53\n"
    "ds_read_b64_tr_b16 v[228:229], v245 offset:136\n"
    "v_cvt_pk_bf16_f32 v252, v252, v253\n"
    "v_mfma_f32_16x16x32_bf16 v[32:35], v[232:235], a[16:19], v[32:35]\n"
    "s_cselect_b32 s52, s79, s52\n"
    "ds_read_b64_tr_b16 v[230:231], v245 offset:648\n"
    "v_accvgpr_write_b32 a254, v250\n"
    "v_mfma_f32_16x16x32_bf16 v[40:43], v[232:235], a[20:23], v[40:43]\n"
    "s_waitcnt lgkmcnt(4)\n"
    "buffer_load_dwordx4 v242, s[20:23], s69 offen offset:2112 lds\n"
    "v_mfma_f32_16x16x32_bf16 v[28:31], v[236:239], a[12:15], v[28:31]\n"
    "s_add_u32 s67, s67, 64\n"
    "ds_read_b64_tr_b16 v[232:233], v245 offset:192\n"
    "v_accvgpr_write_b32 a255, v252\n"
    "v_mfma_f32_16x16x32_bf16 v[36:39], v[236:239], a[16:19], v[36:39]\n"
    "s_add_u32 s68, s68, 64\n"
    "ds_read_b64_tr_b16 v[234:235], v245 offset:704\n"
    "v_accvgpr_read_b32 v250, a124\n"
    "v_mfma_f32_16x16x32_bf16 v[44:47], v[236:239], a[20:23], v[44:47]\n"
    "s_waitcnt lgkmcnt(4)\n"
    "v_accvgpr_read_b32 v251, a125\n"
    "v_accvgpr_read_b32 v252, a126\n"
    "v_mfma_f32_16x16x32_bf16 v[48:51], v[224:227], a[12:15], v[48:51]\n"
    "s_add_u32 s69, s69, 64\n"
    "ds_read_b64_tr_b16 v[236:237], v245 offset:200\n"
    "v_accvgpr_read_b32 v253, a127\n"
    "v_mfma_f32_16x16x32_bf16 v[56:59], v[224:227], a[16:19], v[56:59]\n"
    "s_add_u32 s49, s49, 16896\n"
    "s_cmp_eq_u32 s49, s50\n"
    "s_cselect_b32 s49, s51, s49\n"
    "s_mov_b32 m0, s49\n"
    "ds_read_b64_tr_b16 v[238:239], v245 offset:712\n"
    "v_mul_f32_e64 v250, v250, v248\n"
    "v_mfma_f32_16x16x32_bf16 v[64:67], v[224:227], a[20:23], v[64:67]\n"
    "s_waitcnt lgkmcnt(4)\n"
    "buffer_load_dwordx4 v240, s[24:27], s54 offen offset:0 lds\n"
    "v_mfma_f32_16x16x32_bf16 v[52:55], v[228:231], a[12:15], v[52:55]\n"
    "s_add_u32 m0, s49, 1032\n"
    "ds_read_b64_tr_b16 v[224:225], v245 offset:256\n"
    "v_mul_f32_e64 v251, v251, v248\n"
    "v_mfma_f32_16x16x32_bf16 v[60:63], v[228:231], a[16:19], v[60:63]\n"
    "s_add_u32 s75, s54, 8192\n"
    "ds_read_b64_tr_b16 v[226:227], v245 offset:768\n"
    "v_mul_f32_e64 v252, v252, v248\n"
    "v_mfma_f32_16x16x32_bf16 v[68:71], v[228:231], a[20:23], v[68:71]\n"
    "s_waitcnt lgkmcnt(4)\n"
    "v_mul_f32_e64 v253, v253, v248\n"
    "v_cvt_pk_bf16_f32 v192, v250, v251\n"
    "v_mfma_f32_16x16x32_bf16 v[72:75], v[232:235], a[12:15], v[72:75]\n"
    "ds_read_b64_tr_b16 v[228:229], v245 offset:264\n"
    "v_cvt_pk_bf16_f32 v193, v252, v253\n"
    "v_mfma_f32_16x16x32_bf16 v[80:83], v[232:235], a[16:19], v[80:83]\n"
    "ds_read_b64_tr_b16 v[230:231], v245 offset:776\n"
    "v_accvgpr_read_b32 v250, a136\n"
    "v_mfma_f32_16x16x32_bf16 v[88:91], v[232:235], a[20:23], v[88:91]\n"
    "s_waitcnt lgkmcnt(4)\n"
    "buffer_load_dwordx4 v240, s[24:27], s75 offen offset:0 lds\n"
    "v_mfma_f32_16x16x32_bf16 v[76:79], v[236:239], a[12:15], v[76:79]\n"
    "s_add_u32 m0, s49, 2112\n"
    "ds_read_b64_tr_b16 v[232:233], v245 offset:320\n"
    "v_accvgpr_read_b32 v251, a137\n"
    "v_mfma_f32_16x16x32_bf16 v[84:87], v[236:239], a[16:19], v[84:87]\n"
    "s_add_u32 s74, s54, 16384\n"
    "ds_read_b64_tr_b16 v[234:235], v245 offset:832\n"
    "v_accvgpr_read_b32 v252, a138\n"
    "v_mfma_f32_16x16x32_bf16 v[92:95], v[236:239], a[20:23], v[92:95]\n"
    "s_waitcnt lgkmcnt(4)\n"
    "v_accvgpr_read_b32 v253, a139\n"
    "v_mul_f32_e64 v250, v250, v248\n"
    "v_mfma_f32_16x16x32_bf16 v[96:99], v[224:227], a[12:15], v[96:99]\n"
    "ds_read_b64_tr_b16 v[236:237], v245 offset:328\n"
    "v_mul_f32_e64 v251, v251, v248\n"
    "v_mfma_f32_16x16x32_bf16 v[104:107], v[224:227], a[16:19], v[104:107]\n"
    "ds_read_b64_tr_b16 v[238:239], v245 offset:840\n"
    "v_mul_f32_e64 v252, v252, v248\n"
    "v_mfma_f32_16x16x32_bf16 v[112:115], v[224:227], a[20:23], v[112:115]\n"
    "s_waitcnt lgkmcnt(4)\n"
    "buffer_load_dwordx4 v240, s[24:27], s74 offen offset:0 lds\n"
    "v_mfma_f32_16x16x32_bf16 v[100:103], v[228:231], a[12:15], v[100:103]\n"
    "s_add_u32 m0, s49, 3144\n"
    "ds_read_b64_tr_b16 v[224:225], v245 offset:384\n"
    "v_mul_f32_e64 v253, v253, v248\n"
    "v_mfma_f32_16x16x32_bf16 v[108:111], v[228:231], a[16:19], v[108:111]\n"
    "s_add_u32 s75, s54, 24576\n"
    "ds_read_b64_tr_b16 v[226:227], v245 offset:896\n"
    "v_cvt_pk_bf16_f32 v194, v250, v251\n"
    "v_mfma_f32_16x16x32_bf16 v[116:119], v[228:231], a[20:23], v[116:119]\n"
    "s_waitcnt lgkmcnt(4)\n"
    "v_cvt_pk_bf16_f32 v195, v252, v253\n"
    "v_accvgpr_read_b32 v250, a128\n"
    "v_mfma_f32_16x16x32_bf16 v[120:123], v[232:235], a[12:15], v[120:123]\n"
    "ds_read_b64_tr_b16 v[228:229], v245 offset:392\n"
    "v_accvgpr_read_b32 v251, a129\n"
    "v_mfma_f32_16x16x32_bf16 v[128:131], v[232:235], a[16:19], v[128:131]\n"
    "ds_read_b64_tr_b16 v[230:231], v245 offset:904\n"
    "v_accvgpr_read_b32 v252, a130\n"
    "v_mfma_f32_16x16x32_bf16 v[136:139], v[232:235], a[20:23], v[136:139]\n"
    "s_waitcnt lgkmcnt(4)\n"
    "buffer_load_dwordx4 v240, s[24:27], s75 offen offset:0 lds\n"
    "v_mfma_f32_16x16x32_bf16 v[124:127], v[236:239], a[12:15], v[124:127]\n"
    "s_add_u32 s54, s54, 262144\n"
    "ds_read_b64_tr_b16 v[232:233], v245 offset:448\n"
    "v_accvgpr_read_b32 v253, a131\n"
    "v_mfma_f32_16x16x32_bf16 v[132:135], v[236:239], a[16:19], v[132:135]\n"
    "s_add_u32 s49, s49, 16896\n"
    "ds_read_b64_tr_b16 v[234:235], v245 offset:960\n"
    "v_mul_f32_e64 v250, v250, v249\n"
    "v_mfma_f32_16x16x32_bf16 v[140:143], v[236:239], a[20:23], v[140:143]\n"
    "s_waitcnt lgkmcnt(4)\n"
    "v_mul_f32_e64 v251, v251, v249\n"
    "v_mul_f32_e64 v252, v252, v249\n"
    "v_mfma_f32_16x16x32_bf16 v[144:147], v[224:227], a[12:15], v[144:147]\n"
    "s_cmp_eq_u32 s49, s50\n"
    "ds_read_b64_tr_b16 v[236:237], v245 offset:456\n"
    "v_mul_f32_e64 v253, v253, v249\n"
    "v_mfma_f32_16x16x32_bf16 v[152:155], v[224:227], a[16:19], v[152:155]\n"
    "s_waitcnt vmcnt(14)\n"
    "ds_read_b64_tr_b16 v[238:239], v245 offset:968\n"
    "v_add_u32_e64 v245, s52, v244\n"
    "v_mfma_f32_16x16x32_bf16 v[160:163], v[224:227], a[20:23], v[160:163]\n"
    "s_waitcnt lgkmcnt(4)\n"
    "v_cvt_pk_bf16_f32 v196, v250, v251\n"
    "v_cvt_pk_bf16_f32 v197, v252, v253\n"
    "v_mfma_f32_16x16x32_bf16 v[148:151], v[228:231], a[12:15], v[148:151]\n"
    "s_barrier\n"
    "ds_read_b64_tr_b16 v[224:225], v245 offset:0\n"
    "ds_read_b128 a[0:3], v246 offset:0\n"
    "v_mfma_f32_16x16x32_bf16 v[156:159], v[228:231], a[16:19], v[156:159]\n"
    "s_cselect_b32 s49, s51, s49\n"
    "ds_read_b64_tr_b16 v[226:227], v245 offset:512\n"
    "ds_read_b128 a[4:7], v246 offset:1056\n"
    "v_mfma_f32_16x16x32_bf16 v[164:167], v[228:231], a[20:23], v[164:167]\n"
    "s_waitcnt lgkmcnt(6)\n"
    "ds_read_b128 a[8:11], v246 offset:2112\n"
    "v_accvgpr_read_b32 v250, a140\n"
    "v_mfma_f32_16x16x32_bf16 v[168:171], v[232:235], a[12:15], v[168:171]\n"
    "s_mov_b32 m0, s49\n"
    "ds_read_b64_tr_b16 v[228:229], v245 offset:8\n"
    "v_accvgpr_read_b32 v251, a141\n"
    "v_mfma_f32_16x16x32_bf16 v[176:179], v[232:235], a[16:19], v[176:179]\n"
    "s_add_u32 s52, s52, 16896\n"
    "ds_read_b64_tr_b16 v[230:231], v245 offset:520\n"
    "v_accvgpr_read_b32 v252, a142\n"
    "v_mfma_f32_16x16x32_bf16 v[184:187], v[232:235], a[20:23], v[184:187]\n"
    "s_waitcnt lgkmcnt(7)\n"
    "v_accvgpr_read_b32 v253, a143\n"
    "v_mul_f32_e64 v250, v250, v249\n"
    "v_mfma_f32_16x16x32_bf16 v[172:175], v[236:239], a[12:15], v[172:175]\n"
    "s_cmp_eq_u32 s52, s53\n"
    "ds_read_b64_tr_b16 v[232:233], v245 offset:64\n"
    "v_mul_f32_e64 v251, v251, v249\n"
    "v_mfma_f32_16x16x32_bf16 v[180:183], v[236:239], a[16:19], v[180:183]\n"
    "s_cselect_b32 s52, s79, s52\n"
    "ds_read_b64_tr_b16 v[234:235], v245 offset:576\n"
    "v_mfma_f32_16x16x32_bf16 v[188:191], v[236:239], a[20:23], v[188:191]\n"
    "s_waitcnt lgkmcnt(4)\n"
    "v_mfma_f32_16x16x32_bf16 v[0:3], v[224:227], a[0:3], v[0:3]\n"
    "ds_read_b64_tr_b16 v[236:237], v245 offset:72\n"
    "v_mul_f32_e64 v252, v252, v249\n"
    "v_mfma_f32_16x16x32_bf16 v[8:11], v[224:227], a[4:7], v[8:11]\n"
    "ds_read_b64_tr_b16 v[238:239], v245 offset:584\n"
    "v_mul_f32_e64 v253, v253, v249\n"
    "v_mfma_f32_16x16x32_bf16 v[16:19], v[224:227], a[8:11], v[16:19]\n"
    "s_waitcnt lgkmcnt(4)\n"
    "buffer_load_dwordx4 v242, s[20:23], s67 offen offset:0 lds\n"
    "v_mfma_f32_16x16x32_bf16 v[4:7], v[228:231], a[0:3], v[4:7]\n"
    "ds_read_b64_tr_b16 v[224:225], v245 offset:128\n"
    "v_add_u32_e64 v246, s52, v243\n"
    "v_mfma_f32_16x16x32_bf16 v[12:15], v[228:231], a[4:7], v[12:15]\n"
    "s_add_u32 s52, s52, 16896\n"
    "ds_read_b64_tr_b16 v[226:227], v245 offset:640\n"
    "v_cvt_pk_bf16_f32 v198, v250, v251\n"
    "v_mfma_f32_16x16x32_bf16 v[20:23], v[228:231], a[8:11], v[20:23]\n"
    "s_waitcnt lgkmcnt(4)\n"
    "buffer_load_dwordx4 v242, s[20:23], s68 offen offset:1056 lds\n"
    "v_mfma_f32_16x16x32_bf16 v[24:27], v[232:235], a[0:3], v[24:27]\n"
    "s_cmp_eq_u32 s52, s53\n"
    "ds_read_b64_tr_b16 v[228:229], v245 offset:136\n"
    "v_cvt_pk_bf16_f32 v199, v252, v253\n"
    "v_mfma_f32_16x16x32_bf16 v[32:35], v[232:235], a[4:7], v[32:35]\n"
    "s_cselect_b32 s52, s79, s52\n"
    "ds_read_b64_tr_b16 v[230:231], v245 offset:648\n"
    "v_accvgpr_read_b32 v250, a144\n"
    "v_mfma_f32_16x16x32_bf16 v[40:43], v[232:235], a[8:11], v[40:43]\n"
    "s_waitcnt lgkmcnt(4)\n"
    "buffer_load_dwordx4 v242, s[20:23], s69 offen offset:2112 lds\n"
    "v_mfma_f32_16x16x32_bf16 v[28:31], v[236:239], a[0:3], v[28:31]\n"
    "s_add_u32 s67, s67, 64\n"
    "ds_read_b64_tr_b16 v[232:233], v245 offset:192\n"
    "v_accvgpr_read_b32 v251, a145\n"
    "v_mfma_f32_16x16x32_bf16 v[36:39], v[236:239], a[4:7], v[36:39]\n"
    "s_add_u32 s68, s68, 64\n"
    "ds_read_b64_tr_b16 v[234:235], v245 offset:704\n"
    "v_accvgpr_read_b32 v252, a146\n"
    "v_mfma_f32_16x16x32_bf16 v[44:47], v[236:239], a[8:11], v[44:47]\n"
    "s_waitcnt lgkmcnt(4)\n"
    "v_accvgpr_read_b32 v253, a147\n"
    "v_mul_f32_e64 v250, v250, v247\n"
    "v_mfma_f32_16x16x32_bf16 v[48:51], v[224:227], a[0:3], v[48:51]\n"
    "s_add_u32 s69, s69, 64\n"
    "ds_read_b64_tr_b16 v[236:237], v245 offset:200\n"
    "v_mul_f32_e64 v251, v251, v247\n"
    "v_mfma_f32_16x16x32_bf16 v[56:59], v[224:227], a[4:7], v[56:59]\n"
    "s_add_u32 s49, s49, 16896\n"
    "s_cmp_eq_u32 s49, s50\n"
    "s_cselect_b32 s49, s51, s49\n"
    "s_mov_b32 m0, s49\n"
    "ds_read_b64_tr_b16 v[238:239], v245 offset:712\n"
    "v_mul_f32_e64 v252, v252, v247\n"
    "v_mfma_f32_16x16x32_bf16 v[64:67], v[224:227], a[8:11], v[64:67]\n"
    "s_waitcnt lgkmcnt(4)\n"
    "buffer_load_dwordx4 v240, s[24:27], s54 offen offset:0 lds\n"
    "v_mfma_f32_16x16x32_bf16 v[52:55], v[228:231], a[0:3], v[52:55]\n"
    "s_add_u32 m0, s49, 1032\n"
    "ds_read_b64_tr_b16 v[224:225], v245 offset:256\n"
    "v_mul_f32_e64 v253, v253, v247\n"
    "v_mfma_f32_16x16x32_bf16 v[60:63], v[228:231], a[4:7], v[60:63]\n"
    "s_add_u32 s75, s54, 8192\n"
    "ds_read_b64_tr_b16 v[226:227], v245 offset:768\n"
    "v_cvt_pk_bf16_f32 v200, v250, v251\n"
    "v_mfma_f32_16x16x32_bf16 v[68:71], v[228:231], a[8:11], v[68:71]\n"
    "s_waitcnt lgkmcnt(4)\n"
    "v_cvt_pk_bf16_f32 v201, v252, v253\n"
    "v_accvgpr_read_b32 v250, a156\n"
    "v_mfma_f32_16x16x32_bf16 v[72:75], v[232:235], a[0:3], v[72:75]\n"
    "ds_read_b64_tr_b16 v[228:229], v245 offset:264\n"
    "v_accvgpr_read_b32 v251, a157\n"
    "v_mfma_f32_16x16x32_bf16 v[80:83], v[232:235], a[4:7], v[80:83]\n"
    "ds_read_b64_tr_b16 v[230:231], v245 offset:776\n"
    "v_accvgpr_read_b32 v252, a158\n"
    "v_mfma_f32_16x16x32_bf16 v[88:91], v[232:235], a[8:11], v[88:91]\n"
    "s_waitcnt lgkmcnt(4)\n"
    "buffer_load_dwordx4 v240, s[24:27], s75 offen offset:0 lds\n"
    "v_mfma_f32_16x16x32_bf16 v[76:79], v[236:239], a[0:3], v[76:79]\n"
    "s_add_u32 m0, s49, 2112\n"
    "ds_read_b64_tr_b16 v[232:233], v245 offset:320\n"
    "v_accvgpr_read_b32 v253, a159\n"
    "v_mfma_f32_16x16x32_bf16 v[84:87], v[236:239], a[4:7], v[84:87]\n"
    "s_add_u32 s74, s54, 16384\n"
    "ds_read_b64_tr_b16 v[234:235], v245 offset:832\n"
    "v_mul_f32_e64 v250, v250, v247\n"
    "v_mfma_f32_16x16x32_bf16 v[92:95], v[236:239], a[8:11], v[92:95]\n"
    "s_waitcnt lgkmcnt(4)\n"
    "v_mul_f32_e64 v251, v251, v247\n"
    "v_mul_f32_e64 v252, v252, v247\n"
    "v_mfma_f32_16x16x32_bf16 v[96:99], v[224:227], a[0:3], v[96:99]\n"
    "ds_read_b64_tr_b16 v[236:237], v245 offset:328\n"
    "v_mul_f32_e64 v253, v253, v247\n"
    "v_mfma_f32_16x16x32_bf16 v[104:107], v[224:227], a[4:7], v[104:107]\n"
    "ds_read_b64_tr_b16 v[238:239], v245 offset:840\n"
    "v_cvt_pk_bf16_f32 v202, v250, v251\n"
    "v_mfma_f32_16x16x32_bf16 v[112:115], v[224:227], a[8:11], v[112:115]\n"
    "s_waitcnt lgkmcnt(4)\n"
    "buffer_load_dwordx4 v240, s[24:27], s74 offen offset:0 lds\n"
    "v_mfma_f32_16x16x32_bf16 v[100:103], v[228:231], a[0:3], v[100:103]\n"
    "s_add_u32 m0, s49, 3144\n"
    "ds_read_b64_tr_b16 v[224:225], v245 offset:384\n"
    "v_cvt_pk_bf16_f32 v203, v252, v253\n"
    "v_mfma_f32_16x16x32_bf16 v[108:111], v[228:231], a[4:7], v[108:111]\n"
    "s_add_u32 s75, s54, 24576\n"
    "ds_read_b64_tr_b16 v[226:227], v245 offset:896\n"
    "v_accvgpr_read_b32 v250, a148\n"
    "v_mfma_f32_16x16x32_bf16 v[116:119], v[228:231], a[8:11], v[116:119]\n"
    "s_waitcnt lgkmcnt(4)\n"
    "v_accvgpr_read_b32 v251, a149\n"
    "v_accvgpr_read_b32 v252, a150\n"
    "v_mfma_f32_16x16x32_bf16 v[120:123], v[232:235], a[0:3], v[120:123]\n"
    "ds_read_b64_tr_b16 v[228:229], v245 offset:392\n"
    "v_accvgpr_read_b32 v253, a151\n"
    "v_mfma_f32_16x16x32_bf16 v[128:131], v[232:235], a[4:7], v[128:131]\n"
    "ds_read_b64_tr_b16 v[230:231], v245 offset:904\n"
    "v_mul_f32_e64 v250, v250, v248\n"
    "v_mfma_f32_16x16x32_bf16 v[136:139], v[232:235], a[8:11], v[136:139]\n"
    "s_waitcnt lgkmcnt(4)\n"
    "buffer_load_dwordx4 v240, s[24:27], s75 offen offset:0 lds\n"
    "v_mfma_f32_16x16x32_bf16 v[124:127], v[236:239], a[0:3], v[124:127]\n"
    "s_add_u32 s54, s54, 262144\n"
    "ds_read_b64_tr_b16 v[232:233], v245 offset:448\n"
    "v_mul_f32_e64 v251, v251, v248\n"
    "v_mfma_f32_16x16x32_bf16 v[132:135], v[236:239], a[4:7], v[132:135]\n"
    "s_add_u32 s49, s49, 16896\n"
    "ds_read_b64_tr_b16 v[234:235], v245 offset:960\n"
    "v_mul_f32_e64 v252, v252, v248\n"
    "v_mfma_f32_16x16x32_bf16 v[140:143], v[236:239], a[8:11], v[140:143]\n"
    "s_waitcnt lgkmcnt(4)\n"
    "v_mul_f32_e64 v253, v253, v248\n"
    "v_cvt_pk_bf16_f32 v204, v250, v251\n"
    "v_mfma_f32_16x16x32_bf16 v[144:147], v[224:227], a[0:3], v[144:147]\n"
    "s_cmp_eq_u32 s49, s50\n"
    "ds_read_b64_tr_b16 v[236:237], v245 offset:456\n"
    "v_cvt_pk_bf16_f32 v205, v252, v253\n"
    "v_mfma_f32_16x16x32_bf16 v[152:155], v[224:227], a[4:7], v[152:155]\n"
    "s_waitcnt vmcnt(14)\n"
    "ds_read_b64_tr_b16 v[238:239], v245 offset:968\n"
    "v_add_u32_e64 v245, s52, v244\n"
    "v_mfma_f32_16x16x32_bf16 v[160:163], v[224:227], a[8:11], v[160:163]\n"
    "s_waitcnt lgkmcnt(4)\n"
    "v_accvgpr_read_b32 v250, a160\n"
    "v_accvgpr_read_b32 v251, a161\n"
    "v_mfma_f32_16x16x32_bf16 v[148:151], v[228:231], a[0:3], v[148:151]\n"
    "s_barrier\n"
    "ds_read_b64_tr_b16 v[224:225], v245 offset:0\n"
    "ds_read_b128 a[12:15], v246 offset:0\n"
    "v_mfma_f32_16x16x32_bf16 v[156:159], v[228:231], a[4:7], v[156:159]\n"
    "s_cselect_b32 s49, s51, s49\n"
    "ds_read_b64_tr_b16 v[226:227], v245 offset:512\n"
    "ds_read_b128 a[16:19], v246 offset:1056\n"
    "v_mfma_f32_16x16x32_bf16 v[164:167], v[228:231], a[8:11], v[164:167]\n"
    "s_waitcnt lgkmcnt(6)\n"
    "ds_read_b128 a[20:23], v246 offset:2112\n"
    "v_accvgpr_read_b32 v252, a162\n"
    "v_mfma_f32_16x16x32_bf16 v[168:171], v[232:235], a[0:3], v[168:171]\n"
    "s_mov_b32 m0, s49\n"
    "ds_read_b64_tr_b16 v[228:229], v245 offset:8\n"
    "v_accvgpr_read_b32 v253, a163\n"
    "v_mfma_f32_16x16x32_bf16 v[176:179], v[232:235], a[4:7], v[176:179]\n"
    "s_add_u32 s52, s52, 16896\n"
    "ds_read_b64_tr_b16 v[230:231], v245 offset:520\n"
    "v_mul_f32_e64 v250, v250, v248\n"
    "v_mfma_f32_16x16x32_bf16 v[184:187], v[232:235], a[8:11], v[184:187]\n"
    "s_waitcnt lgkmcnt(7)\n"
    "v_mul_f32_e64 v251, v251, v248\n"
    "v_mul_f32_e64 v252, v252, v248\n"
    "v_mfma_f32_16x16x32_bf16 v[172:175], v[236:239], a[0:3], v[172:175]\n"
    "s_cmp_eq_u32 s52, s53\n"
    "ds_read_b64_tr_b16 v[232:233], v245 offset:64\n"
    "v_mul_f32_e64 v253, v253, v248\n"
    "v_mfma_f32_16x16x32_bf16 v[180:183], v[236:239], a[4:7], v[180:183]\n"
    "s_cselect_b32 s52, s79, s52\n"
    "ds_read_b64_tr_b16 v[234:235], v245 offset:576\n"
    "v_mfma_f32_16x16x32_bf16 v[188:191], v[236:239], a[8:11], v[188:191]\n"
    "s_waitcnt lgkmcnt(4)\n"
    "v_mfma_f32_16x16x32_bf16 v[0:3], v[224:227], a[12:15], v[0:3]\n"
    "ds_read_b64_tr_b16 v[236:237], v245 offset:72\n"
    "v_cvt_pk_bf16_f32 v206, v250, v251\n"
    "v_mfma_f32_16x16x32_bf16 v[8:11], v[224:227], a[16:19], v[8:11]\n"
    "ds_read_b64_tr_b16 v[238:239], v245 offset:584\n"
    "v_cvt_pk_bf16_f32 v207, v252, v253\n"
    "v_mfma_f32_16x16x32_bf16 v[16:19], v[224:227], a[20:23], v[16:19]\n"
    "s_waitcnt lgkmcnt(4)\n"
    "buffer_load_dwordx4 v241, s[28:31], s55 offen offset:0 nt lds\n"
    "v_mfma_f32_16x16x32_bf16 v[4:7], v[228:231], a[12:15], v[4:7]\n"
    "s_add_u32 m0, s49, 544\n"
    "ds_read_b64_tr_b16 v[224:225], v245 offset:128\n"
    "v_add_u32_e64 v246, s52, v243\n"
    "v_mfma_f32_16x16x32_bf16 v[12:15], v[228:231], a[16:19], v[12:15]\n"
    "s_add_u32 s52, s52, 16896\n"
    "ds_read_b64_tr_b16 v[226:227], v245 offset:640\n"
    "v_accvgpr_read_b32 v250, a152\n"
    "v_mfma_f32_16x16x32_bf16 v[20:23], v[228:231], a[20:23], v[20:23]\n"
    "s_waitcnt lgkmcnt(4)\n"
    "buffer_load_dwordx4 v241, s[28:31], s55 offen offset:512 nt lds\n"
    "v_mfma_f32_16x16x32_bf16 v[24:27], v[232:235], a[12:15], v[24:27]\n"
    "s_add_u32 m0, s49, 1088\n"
    "ds_read_b64_tr_b16 v[228:229], v245 offset:136\n"
    "v_accvgpr_read_b32 v251, a153\n"
    "v_mfma_f32_16x16x32_bf16 v[32:35], v[232:235], a[16:19], v[32:35]\n"
    "s_cmp_eq_u32 s52, s53\n"
    "ds_read_b64_tr_b16 v[230:231], v245 offset:648\n"
    "v_accvgpr_read_b32 v252, a154\n"
    "v_mfma_f32_16x16x32_bf16 v[40:43], v[232:235], a[20:23], v[40:43]\n"
    "s_waitcnt lgkmcnt(4)\n"
    "v_accvgpr_read_b32 v253, a155\n"
    "v_mul_f32_e64 v250, v250, v249\n"
    "v_mfma_f32_16x16x32_bf16 v[28:31], v[236:239], a[12:15], v[28:31]\n"
    "s_cselect_b32 s52, s79, s52\n"
    "ds_read_b64_tr_b16 v[232:233], v245 offset:192\n"
    "v_mul_f32_e64 v251, v251, v249\n"
    "v_mfma_f32_16x16x32_bf16 v[36:39], v[236:239], a[16:19], v[36:39]\n"
    "ds_read_b64_tr_b16 v[234:235], v245 offset:704\n"
    "v_mul_f32_e64 v252, v252, v249\n"
    "v_mfma_f32_16x16x32_bf16 v[44:47], v[236:239], a[20:23], v[44:47]\n"
    "s_waitcnt lgkmcnt(4)\n"
    "buffer_load_dwordx4 v241, s[28:31], s55 offen offset:1024 nt lds\n"
    "v_mfma_f32_16x16x32_bf16 v[48:51], v[224:227], a[12:15], v[48:51]\n"
    "s_add_u32 m0, s49, 1632\n"
    "ds_read_b64_tr_b16 v[236:237], v245 offset:200\n"
    "v_mul_f32_e64 v253, v253, v249\n"
    "v_mfma_f32_16x16x32_bf16 v[56:59], v[224:227], a[16:19], v[56:59]\n"
    "ds_read_b64_tr_b16 v[238:239], v245 offset:712\n"
    "v_cvt_pk_bf16_f32 v208, v250, v251\n"
    "v_mfma_f32_16x16x32_bf16 v[64:67], v[224:227], a[20:23], v[64:67]\n"
    "s_waitcnt lgkmcnt(4)\n"
    "buffer_load_dwordx4 v241, s[28:31], s55 offen offset:1536 nt lds\n"
    "v_mfma_f32_16x16x32_bf16 v[52:55], v[228:231], a[12:15], v[52:55]\n"
    "s_add_u32 s55, s55, 16384\n"
    "ds_read_b64_tr_b16 v[224:225], v245 offset:256\n"
    "v_cvt_pk_bf16_f32 v209, v252, v253\n"
    "v_mfma_f32_16x16x32_bf16 v[60:63], v[228:231], a[16:19], v[60:63]\n"
    "s_and_b32 s55, s55, 262143\n"
    "ds_read_b64_tr_b16 v[226:227], v245 offset:768\n"
    "v_accvgpr_read_b32 v250, a164\n"
    "v_mfma_f32_16x16x32_bf16 v[68:71], v[228:231], a[20:23], v[68:71]\n"
    "s_waitcnt lgkmcnt(4)\n"
    "v_accvgpr_read_b32 v251, a165\n"
    "v_accvgpr_read_b32 v252, a166\n"
    "v_mfma_f32_16x16x32_bf16 v[72:75], v[232:235], a[12:15], v[72:75]\n"
    "s_add_u32 s49, s49, 16896\n"
    "s_cmp_eq_u32 s49, s50\n"
    "s_cselect_b32 s49, s51, s49\n"
    "ds_read_b64_tr_b16 v[228:229], v245 offset:264\n"
    "v_accvgpr_read_b32 v253, a167\n"
    "v_mfma_f32_16x16x32_bf16 v[80:83], v[232:235], a[16:19], v[80:83]\n"
    "s_mov_b32 m0, s49\n"
    "ds_read_b64_tr_b16 v[230:231], v245 offset:776\n"
    "v_mul_f32_e64 v250, v250, v249\n"
    "v_mfma_f32_16x16x32_bf16 v[88:91], v[232:235], a[20:23], v[88:91]\n"
    "s_waitcnt lgkmcnt(4)\n"
    "buffer_load_dwordx4 v241, s[28:31], s55 offen offset:0 nt lds\n"
    "v_mfma_f32_16x16x32_bf16 v[76:79], v[236:239], a[12:15], v[76:79]\n"
    "s_add_u32 m0, s49, 544\n"
    "ds_read_b64_tr_b16 v[232:233], v245 offset:320\n"
    "v_mul_f32_e64 v251, v251, v249\n"
    "v_mfma_f32_16x16x32_bf16 v[84:87], v[236:239], a[16:19], v[84:87]\n"
    "ds_read_b64_tr_b16 v[234:235], v245 offset:832\n"
    "v_mul_f32_e64 v252, v252, v249\n"
    "v_mfma_f32_16x16x32_bf16 v[92:95], v[236:239], a[20:23], v[92:95]\n"
    "s_waitcnt lgkmcnt(4)\n"
    "buffer_load_dwordx4 v241, s[28:31], s55 offen offset:512 nt lds\n"
    "v_mfma_f32_16x16x32_bf16 v[96:99], v[224:227], a[12:15], v[96:99]\n"
    "s_add_u32 m0, s49, 1088\n"
    "ds_read_b64_tr_b16 v[236:237], v245 offset:328\n"
    "v_mul_f32_e64 v253, v253, v249\n"
    "v_mfma_f32_16x16x32_bf16 v[104:107], v[224:227], a[16:19], v[104:107]\n"
    "ds_read_b64_tr_b16 v[238:239], v245 offset:840\n"
    "v_cvt_pk_bf16_f32 v210, v250, v251\n"
    "v_mfma_f32_16x16x32_bf16 v[112:115], v[224:227], a[20:23], v[112:115]\n"
    "s_waitcnt lgkmcnt(4)\n"
    "v_cvt_pk_bf16_f32 v211, v252, v253\n"
    "v_accvgpr_read_b32 v250, a168\n"
    "v_mfma_f32_16x16x32_bf16 v[100:103], v[228:231], a[12:15], v[100:103]\n"
    "ds_read_b64_tr_b16 v[224:225], v245 offset:384\n"
    "v_accvgpr_read_b32 v251, a169\n"
    "v_mfma_f32_16x16x32_bf16 v[108:111], v[228:231], a[16:19], v[108:111]\n"
    "ds_read_b64_tr_b16 v[226:227], v245 offset:896\n"
    "v_accvgpr_read_b32 v252, a170\n"
    "v_mfma_f32_16x16x32_bf16 v[116:119], v[228:231], a[20:23], v[116:119]\n"
    "s_waitcnt lgkmcnt(4)\n"
    "buffer_load_dwordx4 v241, s[28:31], s55 offen offset:1024 nt lds\n"
    "v_mfma_f32_16x16x32_bf16 v[120:123], v[232:235], a[12:15], v[120:123]\n"
    "s_add_u32 m0, s49, 1632\n"
    "ds_read_b64_tr_b16 v[228:229], v245 offset:392\n"
    "v_accvgpr_read_b32 v253, a171\n"
    "v_mfma_f32_16x16x32_bf16 v[128:131], v[232:235], a[16:19], v[128:131]\n"
    "ds_read_b64_tr_b16 v[230:231], v245 offset:904\n"
    "v_mul_f32_e64 v250, v250, v247\n"
    "v_mfma_f32_16x16x32_bf16 v[136:139], v[232:235], a[20:23], v[136:139]\n"
    "s_waitcnt lgkmcnt(4)\n"
    "buffer_load_dwordx4 v241, s[28:31], s55 offen offset:1536 nt lds\n"
    "v_mfma_f32_16x16x32_bf16 v[124:127], v[236:239], a[12:15], v[124:127]\n"
    "s_add_u32 s55, s55, 16384\n"
    "ds_read_b64_tr_b16 v[232:233], v245 offset:448\n"
    "v_mul_f32_e64 v251, v251, v247\n"
    "v_mfma_f32_16x16x32_bf16 v[132:135], v[236:239], a[16:19], v[132:135]\n"
    "s_and_b32 s55, s55, 262143\n"
    "ds_read_b64_tr_b16 v[234:235], v245 offset:960\n"
    "v_mul_f32_e64 v252, v252, v247\n"
    "v_mfma_f32_16x16x32_bf16 v[140:143], v[236:239], a[20:23], v[140:143]\n"
    "s_waitcnt lgkmcnt(4)\n"
    "v_mul_f32_e64 v253, v253, v247\n"
    "v_cvt_pk_bf16_f32 v212, v250, v251\n"
    "v_mfma_f32_16x16x32_bf16 v[144:147], v[224:227], a[12:15], v[144:147]\n"
    "s_add_u32 s49, s49, 16896\n"
    "ds_read_b64_tr_b16 v[236:237], v245 offset:456\n"
    "v_cvt_pk_bf16_f32 v213, v252, v253\n"
    "v_mfma_f32_16x16x32_bf16 v[152:155], v[224:227], a[16:19], v[152:155]\n"
    "s_waitcnt vmcnt(15)\n"
    "ds_read_b64_tr_b16 v[238:239], v245 offset:968\n"
    "v_add_u32_e64 v245, s52, v244\n"
    "v_mfma_f32_16x16x32_bf16 v[160:163], v[224:227], a[20:23], v[160:163]\n"
    "s_waitcnt lgkmcnt(4)\n"
    "v_accvgpr_read_b32 v250, a180\n"
    "v_accvgpr_read_b32 v251, a181\n"
    "v_mfma_f32_16x16x32_bf16 v[148:151], v[228:231], a[12:15], v[148:151]\n"
    "s_barrier\n"
    "ds_read_b64_tr_b16 v[224:225], v245 offset:0\n"
    "ds_read_b128 a[0:3], v246 offset:0\n"
    "v_mfma_f32_16x16x32_bf16 v[156:159], v[228:231], a[16:19], v[156:159]\n"
    "s_cmp_eq_u32 s49, s50\n"
    "ds_read_b64_tr_b16 v[226:227], v245 offset:512\n"
    "ds_read_b128 a[4:7], v246 offset:1056\n"
    "v_mfma_f32_16x16x32_bf16 v[164:167], v[228:231], a[20:23], v[164:167]\n"
    "s_waitcnt lgkmcnt(6)\n"
    "ds_read_b128 a[8:11], v246 offset:2112\n"
    "v_accvgpr_read_b32 v252, a182\n"
    "v_mfma_f32_16x16x32_bf16 v[168:171], v[232:235], a[12:15], v[168:171]\n"
    "s_cselect_b32 s49, s51, s49\n"
    "ds_read_b64_tr_b16 v[228:229], v245 offset:8\n"
    "v_accvgpr_read_b32 v253, a183\n"
    "v_mfma_f32_16x16x32_bf16 v[176:179], v[232:235], a[16:19], v[176:179]\n"
    "s_mov_b32 m0, s49\n"
    "ds_read_b64_tr_b16 v[230:231], v245 offset:520\n"
    "v_mul_f32_e64 v250, v250, v247\n"
    "v_mfma_f32_16x16x32_bf16 v[184:187], v[232:235], a[20:23], v[184:187]\n"
    "s_waitcnt lgkmcnt(7)\n"
    "v_mul_f32_e64 v251, v251, v247\n"
    "v_mul_f32_e64 v252, v252, v247\n"
    "v_mfma_f32_16x16x32_bf16 v[172:175], v[236:239], a[12:15], v[172:175]\n"
    "s_add_u32 s52, s52, 16896\n"
    "ds_read_b64_tr_b16 v[232:233], v245 offset:64\n"
    "v_mul_f32_e64 v253, v253, v247\n"
    "v_mfma_f32_16x16x32_bf16 v[180:183], v[236:239], a[16:19], v[180:183]\n"
    "s_cmp_eq_u32 s52, s53\n"
    "ds_read_b64_tr_b16 v[234:235], v245 offset:576\n"
    "v_cvt_pk_bf16_f32 v214, v250, v251\n"
    "v_mfma_f32_16x16x32_bf16 v[188:191], v[236:239], a[20:23], v[188:191]\n"
    "s_waitcnt lgkmcnt(4)\n"
    "v_cvt_pk_bf16_f32 v215, v252, v253\n"
    "v_mfma_f32_16x16x32_bf16 v[0:3], v[224:227], a[0:3], v[0:3]\n"
    "s_cselect_b32 s52, s79, s52\n"
    "ds_read_b64_tr_b16 v[236:237], v245 offset:72\n"
    "v_accvgpr_read_b32 v250, a172\n"
    "v_mfma_f32_16x16x32_bf16 v[8:11], v[224:227], a[4:7], v[8:11]\n"
    "s_sub_u32 s67, s67, 704\n"
    "ds_read_b64_tr_b16 v[238:239], v245 offset:584\n"
    "v_accvgpr_read_b32 v251, a173\n"
    "v_mfma_f32_16x16x32_bf16 v[16:19], v[224:227], a[8:11], v[16:19]\n"
    "s_waitcnt lgkmcnt(4)\n"
    "buffer_load_dwordx4 v241, s[32:35], s56 offen offset:0 nt lds\n"
    "v_mfma_f32_16x16x32_bf16 v[4:7], v[228:231], a[0:3], v[4:7]\n"
    "s_add_u32 m0, s49, 544\n"
    "ds_read_b64_tr_b16 v[224:225], v245 offset:128\n"
    "v_add_u32_e64 v246, s52, v243\n"
    "v_mfma_f32_16x16x32_bf16 v[12:15], v[228:231], a[4:7], v[12:15]\n"
    "s_add_u32 s52, s52, 16896\n"
    "ds_read_b64_tr_b16 v[226:227], v245 offset:640\n"
    "v_accvgpr_read_b32 v252, a174\n"
    "v_mfma_f32_16x16x32_bf16 v[20:23], v[228:231], a[8:11], v[20:23]\n"
    "s_waitcnt lgkmcnt(4)\n"
    "buffer_load_dwordx4 v241, s[32:35], s56 offen offset:512 nt lds\n"
    "v_mfma_f32_16x16x32_bf16 v[24:27], v[232:235], a[0:3], v[24:27]\n"
    "s_add_u32 m0, s49, 1088\n"
    "ds_read_b64_tr_b16 v[228:229], v245 offset:136\n"
    "v_accvgpr_read_b32 v253, a175\n"
    "v_mfma_f32_16x16x32_bf16 v[32:35], v[232:235], a[4:7], v[32:35]\n"
    "s_cmp_eq_u32 s52, s53\n"
    "ds_read_b64_tr_b16 v[230:231], v245 offset:648\n"
    "v_mul_f32_e64 v250, v250, v248\n"
    "v_mfma_f32_16x16x32_bf16 v[40:43], v[232:235], a[8:11], v[40:43]\n"
    "s_waitcnt lgkmcnt(4)\n"
    "v_mul_f32_e64 v251, v251, v248\n"
    "v_mul_f32_e64 v252, v252, v248\n"
    "v_mfma_f32_16x16x32_bf16 v[28:31], v[236:239], a[0:3], v[28:31]\n"
    "s_cselect_b32 s52, s79, s52\n"
    "ds_read_b64_tr_b16 v[232:233], v245 offset:192\n"
    "v_mul_f32_e64 v253, v253, v248\n"
    "v_mfma_f32_16x16x32_bf16 v[36:39], v[236:239], a[4:7], v[36:39]\n"
    "s_sub_u32 s68, s68, 704\n"
    "ds_read_b64_tr_b16 v[234:235], v245 offset:704\n"
    "v_cvt_pk_bf16_f32 v216, v250, v251\n"
    "v_mfma_f32_16x16x32_bf16 v[44:47], v[236:239], a[8:11], v[44:47]\n"
    "s_waitcnt lgkmcnt(4)\n"
    "buffer_load_dwordx4 v241, s[32:35], s56 offen offset:1024 nt lds\n"
    "v_mfma_f32_16x16x32_bf16 v[48:51], v[224:227], a[0:3], v[48:51]\n"
    "s_add_u32 m0, s49, 1632\n"
    "ds_read_b64_tr_b16 v[236:237], v245 offset:200\n"
    "v_cvt_pk_bf16_f32 v217, v252, v253\n"
    "v_mfma_f32_16x16x32_bf16 v[56:59], v[224:227], a[4:7], v[56:59]\n"
    "s_sub_u32 s69, s69, 704\n"
    "ds_read_b64_tr_b16 v[238:239], v245 offset:712\n"
    "v_accvgpr_read_b32 v250, a184\n"
    "v_mfma_f32_16x16x32_bf16 v[64:67], v[224:227], a[8:11], v[64:67]\n"
    "s_waitcnt lgkmcnt(4)\n"
    "buffer_load_dwordx4 v241, s[32:35], s56 offen offset:1536 nt lds\n"
    "v_mfma_f32_16x16x32_bf16 v[52:55], v[228:231], a[0:3], v[52:55]\n"
    "s_add_u32 s56, s56, 16384\n"
    "ds_read_b64_tr_b16 v[224:225], v245 offset:256\n"
    "v_accvgpr_read_b32 v251, a185\n"
    "v_mfma_f32_16x16x32_bf16 v[60:63], v[228:231], a[4:7], v[60:63]\n"
    "s_and_b32 s56, s56, 262143\n"
    "ds_read_b64_tr_b16 v[226:227], v245 offset:768\n"
    "v_accvgpr_read_b32 v252, a186\n"
    "v_mfma_f32_16x16x32_bf16 v[68:71], v[228:231], a[8:11], v[68:71]\n"
    "s_waitcnt lgkmcnt(4)\n"
    "v_accvgpr_read_b32 v253, a187\n"
    "v_mul_f32_e64 v250, v250, v248\n"
    "v_mfma_f32_16x16x32_bf16 v[72:75], v[232:235], a[0:3], v[72:75]\n"
    "s_add_u32 s49, s49, 16896\n"
    "s_cmp_eq_u32 s49, s50\n"
    "s_cselect_b32 s49, s51, s49\n"
    "ds_read_b64_tr_b16 v[228:229], v245 offset:264\n"
    "v_mul_f32_e64 v251, v251, v248\n"
    "v_mfma_f32_16x16x32_bf16 v[80:83], v[232:235], a[4:7], v[80:83]\n"
    "s_mov_b32 m0, s49\n"
    "ds_read_b64_tr_b16 v[230:231], v245 offset:776\n"
    "v_mul_f32_e64 v252, v252, v248\n"
    "v_mfma_f32_16x16x32_bf16 v[88:91], v[232:235], a[8:11], v[88:91]\n"
    "s_waitcnt lgkmcnt(4)\n"
    "buffer_load_dwordx4 v241, s[28:31], s55 offen offset:0 nt lds\n"
    "v_mfma_f32_16x16x32_bf16 v[76:79], v[236:239], a[0:3], v[76:79]\n"
    "s_add_u32 m0, s49, 544\n"
    "ds_read_b64_tr_b16 v[232:233], v245 offset:320\n"
    "v_mul_f32_e64 v253, v253, v248\n"
    "v_mfma_f32_16x16x32_bf16 v[84:87], v[236:239], a[4:7], v[84:87]\n"
    "ds_read_b64_tr_b16 v[234:235], v245 offset:832\n"
    "v_cvt_pk_bf16_f32 v218, v250, v251\n"
    "v_mfma_f32_16x16x32_bf16 v[92:95], v[236:239], a[8:11], v[92:95]\n"
    "s_waitcnt lgkmcnt(4)\n"
    "buffer_load_dwordx4 v241, s[28:31], s55 offen offset:512 nt lds\n"
    "v_mfma_f32_16x16x32_bf16 v[96:99], v[224:227], a[0:3], v[96:99]\n"
    "s_add_u32 m0, s49, 1088\n"
    "ds_read_b64_tr_b16 v[236:237], v245 offset:328\n"
    "v_cvt_pk_bf16_f32 v219, v252, v253\n"
    "v_mfma_f32_16x16x32_bf16 v[104:107], v[224:227], a[4:7], v[104:107]\n"
    "ds_read_b64_tr_b16 v[238:239], v245 offset:840\n"
    "v_accvgpr_read_b32 v250, a176\n"
    "v_mfma_f32_16x16x32_bf16 v[112:115], v[224:227], a[8:11], v[112:115]\n"
    "s_waitcnt lgkmcnt(4)\n"
    "v_accvgpr_read_b32 v251, a177\n"
    "v_accvgpr_read_b32 v252, a178\n"
    "v_mfma_f32_16x16x32_bf16 v[100:103], v[228:231], a[0:3], v[100:103]\n"
    "ds_read_b64_tr_b16 v[224:225], v245 offset:384\n"
    "v_accvgpr_read_b32 v253, a179\n"
    "v_mfma_f32_16x16x32_bf16 v[108:111], v[228:231], a[4:7], v[108:111]\n"
    "ds_read_b64_tr_b16 v[226:227], v245 offset:896\n"
    "v_mul_f32_e64 v250, v250, v249\n"
    "v_mfma_f32_16x16x32_bf16 v[116:119], v[228:231], a[8:11], v[116:119]\n"
    "s_waitcnt lgkmcnt(4)\n"
    "buffer_load_dwordx4 v241, s[28:31], s55 offen offset:1024 nt lds\n"
    "v_mfma_f32_16x16x32_bf16 v[120:123], v[232:235], a[0:3], v[120:123]\n"
    "s_add_u32 m0, s49, 1632\n"
    "ds_read_b64_tr_b16 v[228:229], v245 offset:392\n"
    "v_mul_f32_e64 v251, v251, v249\n"
    "v_mfma_f32_16x16x32_bf16 v[128:131], v[232:235], a[4:7], v[128:131]\n"
    "ds_read_b64_tr_b16 v[230:231], v245 offset:904\n"
    "v_mul_f32_e64 v252, v252, v249\n"
    "v_mfma_f32_16x16x32_bf16 v[136:139], v[232:235], a[8:11], v[136:139]\n"
    "s_waitcnt lgkmcnt(4)\n"
    "buffer_load_dwordx4 v241, s[28:31], s55 offen offset:1536 nt lds\n"
    "v_mfma_f32_16x16x32_bf16 v[124:127], v[236:239], a[0:3], v[124:127]\n"
    "s_add_u32 s55, s55, 16384\n"
    "ds_read_b64_tr_b16 v[232:233], v245 offset:448\n"
    "v_mul_f32_e64 v253, v253, v249\n"
    "v_mfma_f32_16x16x32_bf16 v[132:135], v[236:239], a[4:7], v[132:135]\n"
    "s_and_b32 s55, s55, 262143\n"
    "ds_read_b64_tr_b16 v[234:235], v245 offset:960\n"
    "v_cvt_pk_bf16_f32 v220, v250, v251\n"
    "v_mfma_f32_16x16x32_bf16 v[140:143], v[236:239], a[8:11], v[140:143]\n"
    "s_waitcnt lgkmcnt(4)\n"
    "v_cvt_pk_bf16_f32 v221, v252, v253\n"
    "v_accvgpr_read_b32 v250, a188\n"
    "v_mfma_f32_16x16x32_bf16 v[144:147], v[224:227], a[0:3], v[144:147]\n"
    "s_add_u32 s49, s49, 16896\n"
    "ds_read_b64_tr_b16 v[236:237], v245 offset:456\n"
    "v_accvgpr_read_b32 v251, a189\n"
    "v_mfma_f32_16x16x32_bf16 v[152:155], v[224:227], a[4:7], v[152:155]\n"
    "s_waitcnt vmcnt(16)\n"
    "ds_read_b64_tr_b16 v[238:239], v245 offset:968\n"
    "v_add_u32_e64 v245, s52, v244\n"
    "v_mfma_f32_16x16x32_bf16 v[160:163], v[224:227], a[8:11], v[160:163]\n"
    "s_waitcnt lgkmcnt(4)\n"
    "v_accvgpr_read_b32 v252, a190\n"
    "v_accvgpr_read_b32 v253, a191\n"
    "v_mfma_f32_16x16x32_bf16 v[148:151], v[228:231], a[0:3], v[148:151]\n"
    "s_barrier\n"
    "ds_read_b64_tr_b16 v[224:225], v245 offset:0\n"
    "ds_read_b128 a[12:15], v246 offset:0\n"
    "v_mfma_f32_16x16x32_bf16 v[156:159], v[228:231], a[4:7], v[156:159]\n"
    "s_cmp_eq_u32 s49, s50\n"
    "ds_read_b64_tr_b16 v[226:227], v245 offset:512\n"
    "ds_read_b128 a[16:19], v246 offset:1056\n"
    "v_mfma_f32_16x16x32_bf16 v[164:167], v[228:231], a[8:11], v[164:167]\n"
    "s_waitcnt lgkmcnt(6)\n"
    "ds_read_b128 a[20:23], v246 offset:2112\n"
    "v_mul_f32_e64 v250, v250, v249\n"
    "v_mfma_f32_16x16x32_bf16 v[168:171], v[232:235], a[0:3], v[168:171]\n"
    "s_cselect_b32 s49, s51, s49\n"
    "ds_read_b64_tr_b16 v[228:229], v245 offset:8\n"
    "v_mul_f32_e64 v251, v251, v249\n"
    "v_mfma_f32_16x16x32_bf16 v[176:179], v[232:235], a[4:7], v[176:179]\n"
    "s_mov_b32 m0, s49\n"
    "ds_read_b64_tr_b16 v[230:231], v245 offset:520\n"
    "v_mul_f32_e64 v252, v252, v249\n"
    "v_mfma_f32_16x16x32_bf16 v[184:187], v[232:235], a[8:11], v[184:187]\n"
    "s_waitcnt lgkmcnt(7)\n"
    "v_mul_f32_e64 v253, v253, v249\n"
    "v_cvt_pk_bf16_f32 v222, v250, v251\n"
    "v_mfma_f32_16x16x32_bf16 v[172:175], v[236:239], a[0:3], v[172:175]\n"
    "s_add_u32 s52, s52, 16896\n"
    "ds_read_b64_tr_b16 v[232:233], v245 offset:64\n"
    "v_cvt_pk_bf16_f32 v223, v252, v253\n"
    "v_mfma_f32_16x16x32_bf16 v[180:183], v[236:239], a[4:7], v[180:183]\n"
    "s_cmp_eq_u32 s52, s53\n"
    "ds_read_b64_tr_b16 v[234:235], v245 offset:576\n"
    "v_mfma_f32_16x16x32_bf16 v[188:191], v[236:239], a[8:11], v[188:191]\n"
    "s_waitcnt lgkmcnt(4)\n"
    "v_mfma_f32_16x16x32_bf16 v[0:3], v[224:227], a[12:15], v[0:3]\n"
    "s_cselect_b32 s52, s79, s52\n"
    "ds_read_b64_tr_b16 v[236:237], v245 offset:72\n"
    "v_mfma_f32_16x16x32_bf16 v[8:11], v[224:227], a[16:19], v[8:11]\n"
    "ds_read_b64_tr_b16 v[238:239], v245 offset:584\n"
    "v_mfma_f32_16x16x32_bf16 v[16:19], v[224:227], a[20:23], v[16:19]\n"
    "s_waitcnt lgkmcnt(4)\n"
    "buffer_load_dwordx4 v241, s[32:35], s56 offen offset:0 nt lds\n"
    "v_mfma_f32_16x16x32_bf16 v[4:7], v[228:231], a[12:15], v[4:7]\n"
    "s_add_u32 m0, s49, 544\n"
    "ds_read_b64_tr_b16 v[224:225], v245 offset:128\n"
    "v_mfma_f32_16x16x32_bf16 v[12:15], v[228:231], a[16:19], v[12:15]\n"
    "ds_read_b64_tr_b16 v[226:227], v245 offset:640\n"
    "v_mfma_f32_16x16x32_bf16 v[20:23], v[228:231], a[20:23], v[20:23]\n"
    "s_waitcnt lgkmcnt(4)\n"
    "buffer_load_dwordx4 v241, s[32:35], s56 offen offset:512 nt lds\n"
    "v_mfma_f32_16x16x32_bf16 v[24:27], v[232:235], a[12:15], v[24:27]\n"
    "s_add_u32 m0, s49, 1088\n"
    "ds_read_b64_tr_b16 v[228:229], v245 offset:136\n"
    "v_mfma_f32_16x16x32_bf16 v[32:35], v[232:235], a[16:19], v[32:35]\n"
    "ds_read_b64_tr_b16 v[230:231], v245 offset:648\n"
    "v_mfma_f32_16x16x32_bf16 v[40:43], v[232:235], a[20:23], v[40:43]\n"
    "s_waitcnt lgkmcnt(4)\n"
    "v_mfma_f32_16x16x32_bf16 v[28:31], v[236:239], a[12:15], v[28:31]\n"
    "ds_read_b64_tr_b16 v[232:233], v245 offset:192\n"
    "v_mfma_f32_16x16x32_bf16 v[36:39], v[236:239], a[16:19], v[36:39]\n"
    "ds_read_b64_tr_b16 v[234:235], v245 offset:704\n"
    "v_mfma_f32_16x16x32_bf16 v[44:47], v[236:239], a[20:23], v[44:47]\n"
    "s_waitcnt lgkmcnt(4)\n"
    "buffer_load_dwordx4 v241, s[32:35], s56 offen offset:1024 nt lds\n"
    "v_mfma_f32_16x16x32_bf16 v[48:51], v[224:227], a[12:15], v[48:51]\n"
    "s_add_u32 m0, s49, 1632\n"
    "ds_read_b64_tr_b16 v[236:237], v245 offset:200\n"
    "v_mfma_f32_16x16x32_bf16 v[56:59], v[224:227], a[16:19], v[56:59]\n"
    "ds_read_b64_tr_b16 v[238:239], v245 offset:712\n"
    "v_mfma_f32_16x16x32_bf16 v[64:67], v[224:227], a[20:23], v[64:67]\n"
    "s_waitcnt lgkmcnt(4)\n"
    "buffer_load_dwordx4 v241, s[32:35], s56 offen offset:1536 nt lds\n"
    "v_mfma_f32_16x16x32_bf16 v[52:55], v[228:231], a[12:15], v[52:55]\n"
    "s_add_u32 s56, s56, 16384\n"
    "ds_read_b64_tr_b16 v[224:225], v245 offset:256\n"
    "v_mfma_f32_16x16x32_bf16 v[60:63], v[228:231], a[16:19], v[60:63]\n"
    "s_and_b32 s56, s56, 262143\n"
    "ds_read_b64_tr_b16 v[226:227], v245 offset:768\n"
    "v_mfma_f32_16x16x32_bf16 v[68:71], v[228:231], a[20:23], v[68:71]\n"
    "s_waitcnt lgkmcnt(4)\n"
    "v_mfma_f32_16x16x32_bf16 v[72:75], v[232:235], a[12:15], v[72:75]\n"
    "s_add_u32 s49, s49, 16896\n"
    "s_cmp_eq_u32 s49, s50\n"
    "s_cselect_b32 s49, s51, s49\n"
    "ds_read_b64_tr_b16 v[228:229], v245 offset:264\n"
    "v_mfma_f32_16x16x32_bf16 v[80:83], v[232:235], a[16:19], v[80:83]\n"
    "s_mov_b32 m0, s49\n"
    "ds_read_b64_tr_b16 v[230:231], v245 offset:776\n"
    "v_mfma_f32_16x16x32_bf16 v[88:91], v[232:235], a[20:23], v[88:91]\n"
    "s_waitcnt lgkmcnt(4)\n"
    "buffer_load_dwordx4 v241, s[28:31], s55 offen offset:0 nt lds\n"
    "v_mfma_f32_16x16x32_bf16 v[76:79], v[236:239], a[12:15], v[76:79]\n"
    "s_add_u32 m0, s49, 544\n"
    "ds_read_b64_tr_b16 v[232:233], v245 offset:320\n"
    "v_mfma_f32_16x16x32_bf16 v[84:87], v[236:239], a[16:19], v[84:87]\n"
    "ds_read_b64_tr_b16 v[234:235], v245 offset:832\n"
    "v_mfma_f32_16x16x32_bf16 v[92:95], v[236:239], a[20:23], v[92:95]\n"
    "s_waitcnt lgkmcnt(4)\n"
    "buffer_load_dwordx4 v241, s[28:31], s55 offen offset:512 nt lds\n"
    "v_mfma_f32_16x16x32_bf16 v[96:99], v[224:227], a[12:15], v[96:99]\n"
    "s_add_u32 m0, s49, 1088\n"
    "ds_read_b64_tr_b16 v[236:237], v245 offset:328\n"
    "v_mfma_f32_16x16x32_bf16 v[104:107], v[224:227], a[16:19], v[104:107]\n"
    "ds_read_b64_tr_b16 v[238:239], v245 offset:840\n"
    "v_mfma_f32_16x16x32_bf16 v[112:115], v[224:227], a[20:23], v[112:115]\n"
    "s_waitcnt lgkmcnt(4)\n"
    "v_mfma_f32_16x16x32_bf16 v[100:103], v[228:231], a[12:15], v[100:103]\n"
    "ds_read_b64_tr_b16 v[224:225], v245 offset:384\n"
    "v_mfma_f32_16x16x32_bf16 v[108:111], v[228:231], a[16:19], v[108:111]\n"
    "ds_read_b64_tr_b16 v[226:227], v245 offset:896\n"
    "v_mfma_f32_16x16x32_bf16 v[116:119], v[228:231], a[20:23], v[116:119]\n"
    "s_waitcnt lgkmcnt(4)\n"
    "buffer_load_dwordx4 v241, s[28:31], s55 offen offset:1024 nt lds\n"
    "v_mfma_f32_16x16x32_bf16 v[120:123], v[232:235], a[12:15], v[120:123]\n"
    "s_add_u32 m0, s49, 1632\n"
    "ds_read_b64_tr_b16 v[228:229], v245 offset:392\n"
    "v_mfma_f32_16x16x32_bf16 v[128:131], v[232:235], a[16:19], v[128:131]\n"
    "ds_read_b64_tr_b16 v[230:231], v245 offset:904\n"
    "v_mfma_f32_16x16x32_bf16 v[136:139], v[232:235], a[20:23], v[136:139]\n"
    "s_waitcnt lgkmcnt(4)\n"
    "buffer_load_dwordx4 v241, s[28:31], s55 offen offset:1536 nt lds\n"
    "v_mfma_f32_16x16x32_bf16 v[124:127], v[236:239], a[12:15], v[124:127]\n"
    "s_add_u32 s55, s55, 16384\n"
    "ds_read_b64_tr_b16 v[232:233], v245 offset:448\n"
    "v_mfma_f32_16x16x32_bf16 v[132:135], v[236:239], a[16:19], v[132:135]\n"
    "s_and_b32 s55, s55, 262143\n"
    "ds_read_b64_tr_b16 v[234:235], v245 offset:960\n"
    "v_mfma_f32_16x16x32_bf16 v[140:143], v[236:239], a[20:23], v[140:143]\n"
    "s_waitcnt lgkmcnt(4)\n"
    "v_mfma_f32_16x16x32_bf16 v[144:147], v[224:227], a[12:15], v[144:147]\n"
    "s_add_u32 s49, s49, 16896\n"
    "ds_read_b64_tr_b16 v[236:237], v245 offset:456\n"
    "v_mfma_f32_16x16x32_bf16 v[152:155], v[224:227], a[16:19], v[152:155]\n"
    "s_cmp_eq_u32 s49, s50\n"
    "ds_read_b64_tr_b16 v[238:239], v245 offset:968\n"
    "v_mfma_f32_16x16x32_bf16 v[160:163], v[224:227], a[20:23], v[160:163]\n"
    "s_waitcnt lgkmcnt(4)\n"
    "v_mfma_f32_16x16x32_bf16 v[148:151], v[228:231], a[12:15], v[148:151]\n"
    "s_cselect_b32 s49, s51, s49\n"
    "v_mfma_f32_16x16x32_bf16 v[156:159], v[228:231], a[16:19], v[156:159]\n"
    "v_mfma_f32_16x16x32_bf16 v[164:167], v[228:231], a[20:23], v[164:167]\n"
    "s_waitcnt lgkmcnt(2)\n"
    "v_mfma_f32_16x16x32_bf16 v[168:171], v[232:235], a[12:15], v[168:171]\n"
    "v_mfma_f32_16x16x32_bf16 v[176:179], v[232:235], a[16:19], v[176:179]\n"
    "v_mfma_f32_16x16x32_bf16 v[184:187], v[232:235], a[20:23], v[184:187]\n"
    "s_waitcnt lgkmcnt(0)\n"
    "v_mfma_f32_16x16x32_bf16 v[172:175], v[236:239], a[12:15], v[172:175]\n"
    "v_mfma_f32_16x16x32_bf16 v[180:183], v[236:239], a[16:19], v[180:183]\n"
    "v_mfma_f32_16x16x32_bf16 v[188:191], v[236:239], a[20:23], v[188:191]\n"
    "s_waitcnt vmcnt(16)\n"
    "s_barrier\n"
    "v_add_u32_e64 v245, s52, v254\n"
    "s_add_u32 s52, s52, 16896\n"
    "s_cmp_eq_u32 s52, s53\n"
    "s_cselect_b32 s52, s79, s52\n"
    "ds_read_b128 v[224:227], v245 offset:0\n"
    "ds_read_b128 v[228:231], v245 offset:512\n"
    "ds_read_b128 v[232:235], v245 offset:64\n"
    ".p2align 3\n"
    "v_pk_mul_f32 v[0:1], v[0:1], s[76:77]\n"
    "v_pk_mul_f32 v[2:3], v[2:3], s[76:77]\n"
    "v_pk_mul_f32 v[4:5], v[4:5], s[76:77]\n"
    "v_pk_mul_f32 v[6:7], v[6:7], s[76:77]\n"
    "v_cvt_pk_bf16_f32 v0, v0, v1\n"
    "v_cvt_pk_bf16_f32 v1, v2, v3\n"
    "v_cvt_pk_bf16_f32 v2, v4, v5\n"
    "v_cvt_pk_bf16_f32 v3, v6, v7\n"
    "v_pk_mul_f32 v[8:9], v[8:9], s[76:77]\n"
    "v_pk_mul_f32 v[10:11], v[10:11], s[76:77]\n"
    "v_pk_mul_f32 v[12:13], v[12:13], s[76:77]\n"
    "v_pk_mul_f32 v[14:15], v[14:15], s[76:77]\n"
    "v_cvt_pk_bf16_f32 v4, v8, v9\n"
    "v_cvt_pk_bf16_f32 v5, v10, v11\n"
    "v_cvt_pk_bf16_f32 v6, v12, v13\n"
    "v_cvt_pk_bf16_f32 v7, v14, v15\n"
    "v_pk_mul_f32 v[16:17], v[16:17], s[76:77]\n"
    "v_pk_mul_f32 v[18:19], v[18:19], s[76:77]\n"
    "v_pk_mul_f32 v[20:21], v[20:21], s[76:77]\n"
    "v_pk_mul_f32 v[22:23], v[22:23], s[76:77]\n"
    "v_cvt_pk_bf16_f32 v8, v16, v17\n"
    "v_cvt_pk_bf16_f32 v9, v18, v19\n"
    "v_cvt_pk_bf16_f32 v10, v20, v21\n"
    "v_cvt_pk_bf16_f32 v11, v22, v23\n"
    "v_pk_mul_f32 v[24:25], v[24:25], s[76:77]\n"
    "v_pk_mul_f32 v[26:27], v[26:27], s[76:77]\n"
    "v_pk_mul_f32 v[28:29], v[28:29], s[76:77]\n"
    "v_pk_mul_f32 v[30:31], v[30:31], s[76:77]\n"
    "v_cvt_pk_bf16_f32 v12, v24, v25\n"
    "v_cvt_pk_bf16_f32 v13, v26, v27\n"
    "v_cvt_pk_bf16_f32 v14, v28, v29\n"
    "v_cvt_pk_bf16_f32 v15, v30, v31\n"
    "v_pk_mul_f32 v[32:33], v[32:33], s[76:77]\n"
    "v_pk_mul_f32 v[34:35], v[34:35], s[76:77]\n"
    "v_pk_mul_f32 v[36:37], v[36:37], s[76:77]\n"
    "v_pk_mul_f32 v[38:39], v[38:39], s[76:77]\n"
    "v_cvt_pk_bf16_f32 v16, v32, v33\n"
    "v_cvt_pk_bf16_f32 v17, v34, v35\n"
    "v_cvt_pk_bf16_f32 v18, v36, v37\n"
    "v_cvt_pk_bf16_f32 v19, v38, v39\n"
    "v_pk_mul_f32 v[40:41], v[40:41], s[76:77]\n"
    "v_pk_mul_f32 v[42:43], v[42:43], s[76:77]\n"
    "v_pk_mul_f32 v[44:45], v[44:45], s[76:77]\n"
    "v_pk_mul_f32 v[46:47], v[46:47], s[76:77]\n"
    "v_cvt_pk_bf16_f32 v20, v40, v41\n"
    "v_cvt_pk_bf16_f32 v21, v42, v43\n"
    "v_cvt_pk_bf16_f32 v22, v44, v45\n"
    "v_cvt_pk_bf16_f32 v23, v46, v47\n"
    "v_pk_mul_f32 v[48:49], v[48:49], s[76:77]\n"
    "v_pk_mul_f32 v[50:51], v[50:51], s[76:77]\n"
    "v_pk_mul_f32 v[52:53], v[52:53], s[76:77]\n"
    "v_pk_mul_f32 v[54:55], v[54:55], s[76:77]\n"
    "v_cvt_pk_bf16_f32 v24, v48, v49\n"
    "v_cvt_pk_bf16_f32 v25, v50, v51\n"
    "v_cvt_pk_bf16_f32 v26, v52, v53\n"
    "v_cvt_pk_bf16_f32 v27, v54, v55\n"
    "v_pk_mul_f32 v[56:57], v[56:57], s[76:77]\n"
    "v_pk_mul_f32 v[58:59], v[58:59], s[76:77]\n"
    "v_pk_mul_f32 v[60:61], v[60:61], s[76:77]\n"
    "v_pk_mul_f32 v[62:63], v[62:63], s[76:77]\n"
    "v_cvt_pk_bf16_f32 v28, v56, v57\n"
    "v_cvt_pk_bf16_f32 v29, v58, v59\n"
    "v_cvt_pk_bf16_f32 v30, v60, v61\n"
    "v_cvt_pk_bf16_f32 v31, v62, v63\n"
    "v_pk_mul_f32 v[64:65], v[64:65], s[76:77]\n"
    "v_pk_mul_f32 v[66:67], v[66:67], s[76:77]\n"
    "v_pk_mul_f32 v[68:69], v[68:69], s[76:77]\n"
    "v_pk_mul_f32 v[70:71], v[70:71], s[76:77]\n"
    "v_cvt_pk_bf16_f32 v32, v64, v65\n"
    "v_cvt_pk_bf16_f32 v33, v66, v67\n"
    "v_cvt_pk_bf16_f32 v34, v68, v69\n"
    "v_cvt_pk_bf16_f32 v35, v70, v71\n"
    "v_pk_mul_f32 v[72:73], v[72:73], s[76:77]\n"
    "v_pk_mul_f32 v[74:75], v[74:75], s[76:77]\n"
    "v_pk_mul_f32 v[76:77], v[76:77], s[76:77]\n"
    "v_pk_mul_f32 v[78:79], v[78:79], s[76:77]\n"
    "v_cvt_pk_bf16_f32 v36, v72, v73\n"
    "v_cvt_pk_bf16_f32 v37, v74, v75\n"
    "v_cvt_pk_bf16_f32 v38, v76, v77\n"
    "v_cvt_pk_bf16_f32 v39, v78, v79\n"
    "v_pk_mul_f32 v[80:81], v[80:81], s[76:77]\n"
    "v_pk_mul_f32 v[82:83], v[82:83], s[76:77]\n"
    "v_pk_mul_f32 v[84:85], v[84:85], s[76:77]\n"
    "v_pk_mul_f32 v[86:87], v[86:87], s[76:77]\n"
    "v_cvt_pk_bf16_f32 v40, v80, v81\n"
    "v_cvt_pk_bf16_f32 v41, v82, v83\n"
    "v_cvt_pk_bf16_f32 v42, v84, v85\n"
    "v_cvt_pk_bf16_f32 v43, v86, v87\n"
    "v_pk_mul_f32 v[88:89], v[88:89], s[76:77]\n"
    "v_pk_mul_f32 v[90:91], v[90:91], s[76:77]\n"
    "v_pk_mul_f32 v[92:93], v[92:93], s[76:77]\n"
    "v_pk_mul_f32 v[94:95], v[94:95], s[76:77]\n"
    "v_cvt_pk_bf16_f32 v44, v88, v89\n"
    "v_cvt_pk_bf16_f32 v45, v90, v91\n"
    "v_cvt_pk_bf16_f32 v46, v92, v93\n"
    "v_cvt_pk_bf16_f32 v47, v94, v95\n"
    "v_pk_mul_f32 v[96:97], v[96:97], s[76:77]\n"
    "v_pk_mul_f32 v[98:99], v[98:99], s[76:77]\n"
    "v_pk_mul_f32 v[100:101], v[100:101], s[76:77]\n"
    "v_pk_mul_f32 v[102:103], v[102:103], s[76:77]\n"
    "v_cvt_pk_bf16_f32 v48, v96, v97\n"
    "v_cvt_pk_bf16_f32 v49, v98, v99\n"
    "v_cvt_pk_bf16_f32 v50, v100, v101\n"
    "v_cvt_pk_bf16_f32 v51, v102, v103\n"
    "v_pk_mul_f32 v[104:105], v[104:105], s[76:77]\n"
    "v_pk_mul_f32 v[106:107], v[106:107], s[76:77]\n"
    "v_pk_mul_f32 v[108:109], v[108:109], s[76:77]\n"
    "v_pk_mul_f32 v[110:111], v[110:111], s[76:77]\n"
    "v_cvt_pk_bf16_f32 v52, v104, v105\n"
    "v_cvt_pk_bf16_f32 v53, v106, v107\n"
    "v_cvt_pk_bf16_f32 v54, v108, v109\n"
    "v_cvt_pk_bf16_f32 v55, v110, v111\n"
    "v_pk_mul_f32 v[112:113], v[112:113], s[76:77]\n"
    "v_pk_mul_f32 v[114:115], v[114:115], s[76:77]\n"
    "v_pk_mul_f32 v[116:117], v[116:117], s[76:77]\n"
    "v_pk_mul_f32 v[118:119], v[118:119], s[76:77]\n"
    "v_cvt_pk_bf16_f32 v56, v112, v113\n"
    "v_cvt_pk_bf16_f32 v57, v114, v115\n"
    "v_cvt_pk_bf16_f32 v58, v116, v117\n"
    "v_cvt_pk_bf16_f32 v59, v118, v119\n"
    "v_pk_mul_f32 v[120:121], v[120:121], s[76:77]\n"
    "v_pk_mul_f32 v[122:123], v[122:123], s[76:77]\n"
    "v_pk_mul_f32 v[124:125], v[124:125], s[76:77]\n"
    "v_pk_mul_f32 v[126:127], v[126:127], s[76:77]\n"
    "v_cvt_pk_bf16_f32 v60, v120, v121\n"
    "v_cvt_pk_bf16_f32 v61, v122, v123\n"
    "v_cvt_pk_bf16_f32 v62, v124, v125\n"
    "v_cvt_pk_bf16_f32 v63, v126, v127\n"
    "v_pk_mul_f32 v[128:129], v[128:129], s[76:77]\n"
    "v_pk_mul_f32 v[130:131], v[130:131], s[76:77]\n"
    "v_pk_mul_f32 v[132:133], v[132:133], s[76:77]\n"
    "v_pk_mul_f32 v[134:135], v[134:135], s[76:77]\n"
    "v_cvt_pk_bf16_f32 v64, v128, v129\n"
    "v_cvt_pk_bf16_f32 v65, v130, v131\n"
    "v_cvt_pk_bf16_f32 v66, v132, v133\n"
    "v_cvt_pk_bf16_f32 v67, v134, v135\n"
    "v_pk_mul_f32 v[136:137], v[136:137], s[76:77]\n"
    "v_pk_mul_f32 v[138:139], v[138:139], s[76:77]\n"
    "v_pk_mul_f32 v[140:141], v[140:141], s[76:77]\n"
    "v_pk_mul_f32 v[142:143], v[142:143], s[76:77]\n"
    "v_cvt_pk_bf16_f32 v68, v136, v137\n"
    "v_cvt_pk_bf16_f32 v69, v138, v139\n"
    "v_cvt_pk_bf16_f32 v70, v140, v141\n"
    "v_cvt_pk_bf16_f32 v71, v142, v143\n"
    "v_pk_mul_f32 v[144:145], v[144:145], s[76:77]\n"
    "v_pk_mul_f32 v[146:147], v[146:147], s[76:77]\n"
    "v_pk_mul_f32 v[148:149], v[148:149], s[76:77]\n"
    "v_pk_mul_f32 v[150:151], v[150:151], s[76:77]\n"
    "v_cvt_pk_bf16_f32 v72, v144, v145\n"
    "v_cvt_pk_bf16_f32 v73, v146, v147\n"
    "v_cvt_pk_bf16_f32 v74, v148, v149\n"
    "v_cvt_pk_bf16_f32 v75, v150, v151\n"
    "v_pk_mul_f32 v[152:153], v[152:153], s[76:77]\n"
    "v_pk_mul_f32 v[154:155], v[154:155], s[76:77]\n"
    "v_pk_mul_f32 v[156:157], v[156:157], s[76:77]\n"
    "v_pk_mul_f32 v[158:159], v[158:159], s[76:77]\n"
    "v_cvt_pk_bf16_f32 v76, v152, v153\n"
    "v_cvt_pk_bf16_f32 v77, v154, v155\n"
    "v_cvt_pk_bf16_f32 v78, v156, v157\n"
    "v_cvt_pk_bf16_f32 v79, v158, v159\n"
    "v_pk_mul_f32 v[160:161], v[160:161], s[76:77]\n"
    "v_pk_mul_f32 v[162:163], v[162:163], s[76:77]\n"
    "v_pk_mul_f32 v[164:165], v[164:165], s[76:77]\n"
    "v_pk_mul_f32 v[166:167], v[166:167], s[76:77]\n"
    "v_cvt_pk_bf16_f32 v80, v160, v161\n"
    "v_cvt_pk_bf16_f32 v81, v162, v163\n"
    "v_cvt_pk_bf16_f32 v82, v164, v165\n"
    "v_cvt_pk_bf16_f32 v83, v166, v167\n"
    "v_pk_mul_f32 v[168:169], v[168:169], s[76:77]\n"
    "v_pk_mul_f32 v[170:171], v[170:171], s[76:77]\n"
    "v_pk_mul_f32 v[172:173], v[172:173], s[76:77]\n"
    "v_pk_mul_f32 v[174:175], v[174:175], s[76:77]\n"
    "v_cvt_pk_bf16_f32 v84, v168, v169\n"
    "v_cvt_pk_bf16_f32 v85, v170, v171\n"
    "v_cvt_pk_bf16_f32 v86, v172, v173\n"
    "v_cvt_pk_bf16_f32 v87, v174, v175\n"
    "v_pk_mul_f32 v[176:177], v[176:177], s[76:77]\n"
    "v_pk_mul_f32 v[178:179], v[178:179], s[76:77]\n"
    "v_pk_mul_f32 v[180:181], v[180:181], s[76:77]\n"
    "v_pk_mul_f32 v[182:183], v[182:183], s[76:77]\n"
    "v_cvt_pk_bf16_f32 v88, v176, v177\n"
    "v_cvt_pk_bf16_f32 v89, v178, v179\n"
    "v_cvt_pk_bf16_f32 v90, v180, v181\n"
    "v_cvt_pk_bf16_f32 v91, v182, v183\n"
    "v_pk_mul_f32 v[184:185], v[184:185], s[76:77]\n"
    "v_pk_mul_f32 v[186:187], v[186:187], s[76:77]\n"
    "v_pk_mul_f32 v[188:189], v[188:189], s[76:77]\n"
    "v_pk_mul_f32 v[190:191], v[190:191], s[76:77]\n"
    "v_cvt_pk_bf16_f32 v92, v184, v185\n"
    "v_cvt_pk_bf16_f32 v93, v186, v187\n"
    "v_cvt_pk_bf16_f32 v94, v188, v189\n"
    "v_cvt_pk_bf16_f32 v95, v190, v191\n"
    "v_mov_b32_e64 v190, v242\n"
    "v_mov_b32_e64 v191, v240\n"
    "v_mbcnt_lo_u32_b32 v172, -1, 0\n"
    "v_mbcnt_hi_u32_b32 v172, -1, v172\n"
    "v_lshrrev_b32 v181, 2, v172\n"
    "v_mul_u32_u24 v181, 8192, v181\n"
    "v_and_b32 v172, 3, v172\n"
    "v_lshlrev_b32 v172, 7, v172\n"
    "v_add_u32 v181, v181, v172\n"
    "v_mbcnt_lo_u32_b32 v182, -1, 0\n"
    "v_mbcnt_hi_u32_b32 v182, -1, v182\n"
    "v_lshlrev_b32 v182, 7, v182\n"
    "s_cmp_eq_u32 s71, 0\n"
    "s_cbranch_scc1 L_qc0_%=\n"
    "v_mbcnt_lo_u32_b32 v172, -1, 0\n"
    "v_mbcnt_hi_u32_b32 v172, -1, v172\n"
    "v_mul_u32_u24 v173, 147, v172\n"
    "v_lshrrev_b32 v173, 11, v173\n"
    "v_mul_u32_u24 v174, 14, v173\n"
    "v_sub_u32 v174, v172, v174\n"
    "v_lshlrev_b32 v174, 4, v174\n"
    "v_lshrrev_b32 v175, 1, v173\n"
    "v_and_b32 v173, 1, v173\n"
    "v_mul_u32_u24 v175, 28672, v175\n"
    "v_mul_u32_u24 v173, 14336, v173\n"
    "v_add_u32 v174, v174, v175\n"
    "v_add_u32 v174, v174, v173\n"
    "v_cmp_gt_u32 vcc, 56, v172\n"
    "s_nop 1\n"
    "v_cndmask_b32 v190, 0, v174, vcc\n"
    "v_mov_b32_e64 v191, v190\n"
    "v_mov_b32_e64 v181, v182\n"
    "s_mov_b32 s44, s36\n"
    "s_mov_b32 s45, s37\n"
    "s_mov_b32 s46, s38\n"
    "s_mov_b32 s47, s39\n"
    "s_mov_b32 s84, s36\n"
    "s_mov_b32 s85, s37\n"
    "s_mov_b32 s86, s38\n"
    "s_mov_b32 s87, s39\n"
    "s_mul_i32 s57, s48, 3584\n"
    "s_add_u32 s88, s57, 57344\n"
    "s_mov_b32 s58, 114688\n"
    "s_mov_b32 s89, 114688\n"
    "s_mov_b32 s59, 0\n"
    "s_mov_b32 s63, 0\n"
    "s_mov_b32 s90, 0\n"
    "s_mov_b32 s94, 0\n"
    "s_mov_b32 s60, 1056\n"
    "s_mov_b32 s64, 896\n"
    "s_mov_b32 s91, 1056\n"
    "s_mov_b32 s95, 896\n"
    "s_mov_b32 s61, 2112\n"
    "s_mov_b32 s65, 1792\n"
    "s_mov_b32 s92, 2112\n"
    "s_mov_b32 s96, 1792\n"
    "s_mov_b32 s62, 3168\n"
    "s_mov_b32 s66, 2688\n"
    "s_mov_b32 s93, 3168\n"
    "s_mov_b32 s97, 2688\n"
    "s_mov_b32 s12, s36\n"
    "s_mov_b32 s13, s37\n"
    "s_mov_b32 s14, s38\n"
    "s_mov_b32 s15, s39\n"
    "s_mov_b32 s16, s36\n"
    "s_mov_b32 s17, s37\n"
    "s_mov_b32 s18, s38\n"
    "s_mov_b32 s19, s39\n"
    "s_mul_i32 s0, s98, 57344\n"
    "s_mul_i32 s83, s48, 14336\n"
    "s_add_u32 s0, s0, s83\n"
    "s_add_u32 s1, s0, 6144\n"
    "s_mov_b32 s2, s1\n"
    "L_qc0_%=:\n"
    "s_waitcnt lgkmcnt(2)\n"
    "v_mfma_f32_16x16x32_bf16 v[96:99], v[224:227], v[0:3], 0\n"
    "s_mov_b32 m0, s49\n"
    "ds_read_b128 v[236:239], v245 offset:576\n"
    "v_mfma_f32_16x16x32_bf16 v[100:103], v[224:227], v[4:7], 0\n"
    "v_mfma_f32_16x16x32_bf16 v[104:107], v[224:227], v[8:11], 0\n"
    "s_waitcnt lgkmcnt(2)\n"
    "v_mfma_f32_16x16x32_bf16 v[108:111], v[228:231], v[0:3], 0\n"
    "ds_read_b128 v[224:227], v245 offset:128\n"
    "v_mfma_f32_16x16x32_bf16 v[112:115], v[228:231], v[4:7], 0\n"
    "buffer_load_dwordx4 v241, s[32:35], s56 offen offset:0 nt lds\n"
    "v_mfma_f32_16x16x32_bf16 v[116:119], v[228:231], v[8:11], 0\n"
    "s_nop 7\n"
    "s_waitcnt lgkmcnt(2)\n"
    "v_mfma_f32_16x16x32_bf16 v[96:99], v[232:235], v[12:15], v[96:99]\n"
    "s_add_u32 m0, s49, 544\n"
    "ds_read_b128 v[228:231], v245 offset:640\n"
    "v_mfma_f32_16x16x32_bf16 v[100:103], v[232:235], v[16:19], v[100:103]\n"
    "v_mfma_f32_16x16x32_bf16 v[104:107], v[232:235], v[20:23], v[104:107]\n"
    "s_waitcnt lgkmcnt(2)\n"
    "buffer_load_dwordx4 v241, s[32:35], s56 offen offset:512 nt lds\n"
    "v_mfma_f32_16x16x32_bf16 v[108:111], v[236:239], v[12:15], v[108:111]\n"
    "s_add_u32 m0, s49, 1088\n"
    "ds_read_b128 v[232:235], v245 offset:192\n"
    "v_mfma_f32_16x16x32_bf16 v[112:115], v[236:239], v[16:19], v[112:115]\n"
    "v_mfma_f32_16x16x32_bf16 v[116:119], v[236:239], v[20:23], v[116:119]\n"
    "s_nop 7\n"
    "s_waitcnt lgkmcnt(2)\n"
    "v_mfma_f32_16x16x32_bf16 v[96:99], v[224:227], v[24:27], v[96:99]\n"
    "ds_read_b128 v[236:239], v245 offset:704\n"
    "v_mfma_f32_16x16x32_bf16 v[100:103], v[224:227], v[28:31], v[100:103]\n"
    "buffer_load_dwordx4 v241, s[32:35], s56 offen offset:1024 nt lds\n"
    "v_mfma_f32_16x16x32_bf16 v[104:107], v[224:227], v[32:35], v[104:107]\n"
    "s_waitcnt lgkmcnt(2)\n"
    "v_mfma_f32_16x16x32_bf16 v[108:111], v[228:231], v[24:27], v[108:111]\n"
    "s_add_u32 m0, s49, 1632\n"
    "ds_read_b128 v[224:227], v245 offset:256\n"
    "v_mfma_f32_16x16x32_bf16 v[112:115], v[228:231], v[28:31], v[112:115]\n"
    "v_mfma_f32_16x16x32_bf16 v[116:119], v[228:231], v[32:35], v[116:119]\n"
    "s_nop 7\n"
    "s_waitcnt lgkmcnt(2)\n"
    "buffer_load_dwordx4 v241, s[32:35], s56 offen offset:1536 nt lds\n"
    "v_mfma_f32_16x16x32_bf16 v[96:99], v[232:235], v[36:39], v[96:99]\n"
    "s_add_u32 s56, s56, 16384\n"
    "ds_read_b128 v[228:231], v245 offset:768\n"
    "v_mfma_f32_16x16x32_bf16 v[100:103], v[232:235], v[40:43], v[100:103]\n"
    "s_and_b32 s56, s56, 262143\n"
    "v_mfma_f32_16x16x32_bf16 v[104:107], v[232:235], v[44:47], v[104:107]\n"
    "s_waitcnt lgkmcnt(2)\n"
    "v_mfma_f32_16x16x32_bf16 v[108:111], v[236:239], v[36:39], v[108:111]\n"
    "s_add_u32 s49, s49, 16896\n"
    "s_cmp_eq_u32 s49, s50\n"
    "s_cselect_b32 s49, s51, s49\n"
    "s_mov_b32 m0, s49\n"
    "ds_read_b128 v[232:235], v245 offset:320\n"
    "v_mfma_f32_16x16x32_bf16 v[112:115], v[236:239], v[40:43], v[112:115]\n"
    "buffer_load_dwordx4 v241, s[28:31], s55 offen offset:0 nt lds\n"
    "v_mfma_f32_16x16x32_bf16 v[116:119], v[236:239], v[44:47], v[116:119]\n"
    "s_nop 7\n"
    "s_waitcnt lgkmcnt(2)\n"
    "v_mfma_f32_16x16x32_bf16 v[96:99], v[224:227], v[48:51], v[96:99]\n"
    "s_add_u32 m0, s49, 544\n"
    "ds_read_b128 v[236:239], v245 offset:832\n"
    "v_mfma_f32_16x16x32_bf16 v[100:103], v[224:227], v[52:55], v[100:103]\n"
    "v_mfma_f32_16x16x32_bf16 v[104:107], v[224:227], v[56:59], v[104:107]\n"
    "s_waitcnt lgkmcnt(2)\n"
    "buffer_load_dwordx4 v241, s[28:31], s55 offen offset:512 nt lds\n"
    "v_mfma_f32_16x16x32_bf16 v[108:111], v[228:231], v[48:51], v[108:111]\n"
    "s_add_u32 m0, s49, 1088\n"
    "ds_read_b128 v[224:227], v245 offset:384\n"
    "v_mfma_f32_16x16x32_bf16 v[112:115], v[228:231], v[52:55], v[112:115]\n"
    "v_mfma_f32_16x16x32_bf16 v[116:119], v[228:231], v[56:59], v[116:119]\n"
    "s_nop 7\n"
    "s_waitcnt lgkmcnt(2)\n"
    "v_mfma_f32_16x16x32_bf16 v[96:99], v[232:235], v[60:63], v[96:99]\n"
    "ds_read_b128 v[228:231], v245 offset:896\n"
    "v_mfma_f32_16x16x32_bf16 v[100:103], v[232:235], v[64:67], v[100:103]\n"
    "buffer_load_dwordx4 v241, s[28:31], s55 offen offset:1024 nt lds\n"
    "v_mfma_f32_16x16x32_bf16 v[104:107], v[232:235], v[68:71], v[104:107]\n"
    "s_waitcnt lgkmcnt(2)\n"
    "v_mfma_f32_16x16x32_bf16 v[108:111], v[236:239], v[60:63], v[108:111]\n"
    "s_add_u32 m0, s49, 1632\n"
    "ds_read_b128 v[232:235], v245 offset:448\n"
    "v_mfma_f32_16x16x32_bf16 v[112:115], v[236:239], v[64:67], v[112:115]\n"
    "v_mfma_f32_16x16x32_bf16 v[116:119], v[236:239], v[68:71], v[116:119]\n"
    "s_nop 7\n"
    "s_waitcnt lgkmcnt(2)\n"
    "buffer_load_dwordx4 v241, s[28:31], s55 offen offset:1536 nt lds\n"
    "v_mfma_f32_16x16x32_bf16 v[96:99], v[224:227], v[72:75], v[96:99]\n"
    "s_add_u32 s55, s55, 16384\n"
    "ds_read_b128 v[236:239], v245 offset:960\n"
    "v_add_u32_e64 v245, s52, v254\n"
    "v_mfma_f32_16x16x32_bf16 v[100:103], v[224:227], v[76:79], v[100:103]\n"
    "s_add_u32 s52, s52, 16896\n"
    "v_mfma_f32_16x16x32_bf16 v[104:107], v[224:227], v[80:83], v[104:107]\n"
    "s_waitcnt lgkmcnt(2)\n"
    "v_mfma_f32_16x16x32_bf16 v[108:111], v[228:231], v[72:75], v[108:111]\n"
    "s_cmp_eq_u32 s52, s53\n"
    "ds_read_b128 v[224:227], v245 offset:0\n"
    "v_mfma_f32_16x16x32_bf16 v[112:115], v[228:231], v[76:79], v[112:115]\n"
    "s_cselect_b32 s52, s79, s52\n"
    "v_mfma_f32_16x16x32_bf16 v[116:119], v[228:231], v[80:83], v[116:119]\n"
    "s_nop 7\n"
    "s_waitcnt lgkmcnt(2)\n"
    "v_mfma_f32_16x16x32_bf16 v[96:99], v[232:235], v[84:87], v[96:99]\n"
    "s_and_b32 s55, s55, 262143\n"
    "ds_read_b128 v[228:231], v245 offset:512\n"
    "v_mfma_f32_16x16x32_bf16 v[100:103], v[232:235], v[88:91], v[100:103]\n"
    "s_add_u32 s49, s49, 16896\n"
    "v_mfma_f32_16x16x32_bf16 v[104:107], v[232:235], v[92:95], v[104:107]\n"
    "s_waitcnt lgkmcnt(2)\n"
    "v_mfma_f32_16x16x32_bf16 v[108:111], v[236:239], v[84:87], v[108:111]\n"
    "s_cmp_eq_u32 s49, s50\n"
    "ds_read_b128 v[232:235], v245 offset:64\n"
    "v_mfma_f32_16x16x32_bf16 v[112:115], v[236:239], v[88:91], v[112:115]\n"
    "s_cselect_b32 s49, s51, s49\n"
    "v_mfma_f32_16x16x32_bf16 v[116:119], v[236:239], v[92:95], v[116:119]\n"
    "s_nop 7\n"
    "s_waitcnt lgkmcnt(2)\n"
    ".p2align 3\n"
    "v_max3_f32 v172, v96, v97, v98\n"
    "v_max3_f32 v172, v172, v99, v108\n"
    "v_max3_f32 v172, v172, v109, v110\n"
    "v_max_f32_e64 v172, v172, v111\n"
    "v_max3_f32 v173, v100, v101, v102\n"
    "v_max3_f32 v173, v173, v103, v112\n"
    "v_max3_f32 v173, v173, v113, v114\n"
    "v_max_f32_e64 v173, v173, v115\n"
    "v_max3_f32 v174, v104, v105, v106\n"
    "v_max3_f32 v174, v174, v107, v116\n"
    "v_max3_f32 v174, v174, v117, v118\n"
    "v_max_f32_e64 v174, v174, v119\n"
    "v_mov_b32_e64 v175, v172\n"
    "v_mov_b32_e64 v176, v173\n"
    "v_mov_b32_e64 v177, v174\n"
    "v_permlane16_swap_b32 v172, v175\n"
    "v_permlane16_swap_b32 v173, v176\n"
    "v_permlane16_swap_b32 v174, v177\n"
    "v_max_f32_e64 v172, v172, v175\n"
    "v_max_f32_e64 v173, v173, v176\n"
    "v_max_f32_e64 v174, v174, v177\n"
    "v_mov_b32_e64 v175, v172\n"
    "v_mov_b32_e64 v176, v173\n"
    "v_mov_b32_e64 v177, v174\n"
    "v_permlane32_swap_b32 v172, v175\n"
    "v_permlane32_swap_b32 v173, v176\n"
    "v_permlane32_swap_b32 v174, v177\n"
    "v_max_f32_e64 v172, v172, v175\n"
    "v_max_f32_e64 v173, v173, v176\n"
    "v_max_f32_e64 v174, v174, v177\n"
    "v_sub_f32_e64 v156, 0, v172\n"
    "v_sub_f32_e64 v157, 0, v172\n"
    "v_sub_f32_e64 v158, 0, v172\n"
    "v_sub_f32_e64 v159, 0, v172\n"
    "v_sub_f32_e64 v160, 0, v173\n"
    "v_sub_f32_e64 v161, 0, v173\n"
    "v_sub_f32_e64 v162, 0, v173\n"
    "v_sub_f32_e64 v163, 0, v173\n"
    "v_sub_f32_e64 v164, 0, v174\n"
    "v_sub_f32_e64 v165, 0, v174\n"
    "v_sub_f32_e64 v166, 0, v174\n"
    "v_sub_f32_e64 v167, 0, v174\n"
    "v_sub_f32_e64 v96, v96, v172\n"
    "v_sub_f32_e64 v97, v97, v172\n"
    "v_sub_f32_e64 v98, v98, v172\n"
    "v_sub_f32_e64 v99, v99, v172\n"
    "v_sub_f32_e64 v108, v108, v172\n"
    "v_sub_f32_e64 v109, v109, v172\n"
    "v_sub_f32_e64 v110, v110, v172\n"
    "v_sub_f32_e64 v111, v111, v172\n"
    "v_mov_b32_e64 v168, 0\n"
    "v_sub_f32_e64 v100, v100, v173\n"
    "v_sub_f32_e64 v101, v101, v173\n"
    "v_sub_f32_e64 v102, v102, v173\n"
    "v_sub_f32_e64 v103, v103, v173\n"
    "v_sub_f32_e64 v112, v112, v173\n"
    "v_sub_f32_e64 v113, v113, v173\n"
    "v_sub_f32_e64 v114, v114, v173\n"
    "v_sub_f32_e64 v115, v115, v173\n"
    "v_mov_b32_e64 v169, 0\n"
    "v_sub_f32_e64 v104, v104, v174\n"
    "v_sub_f32_e64 v105, v105, v174\n"
    "v_sub_f32_e64 v106, v106, v174\n"
    "v_sub_f32_e64 v107, v107, v174\n"
    "v_sub_f32_e64 v116, v116, v174\n"
    "v_sub_f32_e64 v117, v117, v174\n"
    "v_sub_f32_e64 v118, v118, v174\n"
    "v_sub_f32_e64 v119, v119, v174\n"
    "v_mov_b32_e64 v170, 0\n"
    "s_cmp_lg_u32 s48, 0\n"
    "s_cbranch_scc1 L_hs1_%=\n"
    "s_cmp_lg_u32 s71, 0\n"
    "s_cbranch_scc1 L_hs1_%=\n"
    "v_mbcnt_lo_u32_b32 v184, -1, 0\n"
    "v_mbcnt_hi_u32_b32 v184, -1, v184\n"
    "v_lshlrev_b32 v184, 2, v184\n"
    "v_add_u32 v184, s79, v184\n"
    "v_add_u32 v184, 152064, v184\n"
    "ds_read_b32 v185, v184\n"
    "ds_read_b32 v188, v184 offset:768\n"
    "v_mbcnt_lo_u32_b32 v187, -1, 0\n"
    "v_mbcnt_hi_u32_b32 v187, -1, v187\n"
    "v_lshlrev_b32 v186, 2, v187\n"
    "v_cmp_gt_u32 vcc, 8, v187\n"
    "v_mov_b32 v187, 1048576\n"
    "s_nop 0\n"
    "v_cndmask_b32 v186, v187, v186, vcc\n"
    "s_lshr_b32 s82, s100, 10\n"
    "s_mul_i32 s82, s82, 172032\n"
    "s_sub_u32 s80, s40, s82\n"
    "s_subb_u32 s81, s41, 0\n"
    "s_add_u32 s80, s80, 1376256\n"
    "s_addc_u32 s81, s81, 0\n"
    "s_mov_b32 s82, 3072\n"
    "s_mov_b32 s83, 131072\n"
    "s_lshr_b32 s74, s100, 10\n"
    "s_mul_i32 s74, s74, 128\n"
    "s_add_u32 s74, s74, 0\n"
    "s_waitcnt lgkmcnt(0)\n"
    "v_add_u32 v187, 2, v185\n"
    "v_cmp_eq_u32 vcc, v188, v187\n"
    "v_add_u32 v189, 3, v185\n"
    "v_add_u32 v185, 1, v185\n"
    "v_cndmask_b32 v185, v185, v189, vcc\n"
    "buffer_store_dword v185, v186, s[80:83], s74 offen sc1\n"
    "ds_write_b32 v184, v185\n"
    "s_waitcnt lgkmcnt(0)\n"
    "L_hs1_%=:\n"
    "s_mov_b32 s70, 5\n"
    "v_mfma_f32_16x16x32_bf16 v[120:123], v[224:227], v[0:3], v[156:159]\n"
    "ds_read_b128 v[236:239], v245 offset:576\n"
    "v_mfma_f32_16x16x32_bf16 v[124:127], v[224:227], v[4:7], v[160:163]\n"
    "buffer_load_dword v183, v181, s[12:15], s0 offen offset:0\n"
    "v_mfma_f32_16x16x32_bf16 v[128:131], v[224:227], v[8:11], v[164:167]\n"
    "s_waitcnt lgkmcnt(2)\n"
    "v_exp_f32_e64 v96, v96\n"
    "v_mfma_f32_16x16x32_bf16 v[132:135], v[228:231], v[0:3], v[156:159]\n"
    "ds_read_b128 v[224:227], v245 offset:128\n"
    "v_mfma_f32_16x16x32_bf16 v[136:139], v[228:231], v[4:7], v[160:163]\n"
    "v_exp_f32_e64 v97, v97\n"
    "v_mfma_f32_16x16x32_bf16 v[140:143], v[228:231], v[8:11], v[164:167]\n"
    "s_nop 7\n"
    "s_waitcnt lgkmcnt(2)\n"
    "v_exp_f32_e64 v98, v98\n"
    "v_mfma_f32_16x16x32_bf16 v[120:123], v[232:235], v[12:15], v[120:123]\n"
    "ds_read_b128 v[228:231], v245 offset:640\n"
    "v_cvt_pk_bf16_f32 v144, v96, v97\n"
    "v_mfma_f32_16x16x32_bf16 v[124:127], v[232:235], v[16:19], v[124:127]\n"
    "v_exp_f32_e64 v99, v99\n"
    "v_mfma_f32_16x16x32_bf16 v[128:131], v[232:235], v[20:23], v[128:131]\n"
    "s_waitcnt lgkmcnt(2)\n"
    "v_exp_f32_e64 v108, v108\n"
    "v_mfma_f32_16x16x32_bf16 v[132:135], v[236:239], v[12:15], v[132:135]\n"
    "ds_read_b128 v[232:235], v245 offset:192\n"
    "v_cvt_pk_bf16_f32 v145, v98, v99\n"
    "v_mfma_f32_16x16x32_bf16 v[136:139], v[236:239], v[16:19], v[136:139]\n"
    "v_exp_f32_e64 v109, v109\n"
    "v_mfma_f32_16x16x32_bf16 v[140:143], v[236:239], v[20:23], v[140:143]\n"
    "s_nop 7\n"
    "s_waitcnt lgkmcnt(2)\n"
    "v_exp_f32_e64 v110, v110\n"
    "v_mfma_f32_16x16x32_bf16 v[120:123], v[224:227], v[24:27], v[120:123]\n"
    "ds_read_b128 v[236:239], v245 offset:704\n"
    "v_cvt_pk_bf16_f32 v146, v108, v109\n"
    "v_mfma_f32_16x16x32_bf16 v[124:127], v[224:227], v[28:31], v[124:127]\n"
    "v_exp_f32_e64 v111, v111\n"
    "v_mfma_f32_16x16x32_bf16 v[128:131], v[224:227], v[32:35], v[128:131]\n"
    "s_waitcnt lgkmcnt(2)\n"
    "v_exp_f32_e64 v100, v100\n"
    "v_mfma_f32_16x16x32_bf16 v[132:135], v[228:231], v[24:27], v[132:135]\n"
    "ds_read_b128 v[224:227], v245 offset:256\n"
    "v_cvt_pk_bf16_f32 v147, v110, v111\n"
    "v_mfma_f32_16x16x32_bf16 v[136:139], v[228:231], v[28:31], v[136:139]\n"
    "v_exp_f32_e64 v101, v101\n"
    "v_mfma_f32_16x16x32_bf16 v[140:143], v[228:231], v[32:35], v[140:143]\n"
    "s_nop 7\n"
    "s_waitcnt lgkmcnt(2)\n"
    "v_exp_f32_e64 v102, v102\n"
    "v_mfma_f32_16x16x32_bf16 v[120:123], v[232:235], v[36:39], v[120:123]\n"
    "ds_read_b128 v[228:231], v245 offset:768\n"
    "v_cvt_pk_bf16_f32 v148, v100, v101\n"
    "v_mfma_f32_16x16x32_bf16 v[124:127], v[232:235], v[40:43], v[124:127]\n"
    "v_exp_f32_e64 v103, v103\n"
    "v_mfma_f32_16x16x32_bf16 v[128:131], v[232:235], v[44:47], v[128:131]\n"
    "s_waitcnt lgkmcnt(2)\n"
    "v_exp_f32_e64 v112, v112\n"
    "v_mfma_f32_16x16x32_bf16 v[132:135], v[236:239], v[36:39], v[132:135]\n"
    "ds_read_b128 v[232:235], v245 offset:320\n"
    "v_cvt_pk_bf16_f32 v149, v102, v103\n"
    "v_mfma_f32_16x16x32_bf16 v[136:139], v[236:239], v[40:43], v[136:139]\n"
    "v_exp_f32_e64 v113, v113\n"
    "v_mfma_f32_16x16x32_bf16 v[140:143], v[236:239], v[44:47], v[140:143]\n"
    "s_nop 7\n"
    "s_waitcnt lgkmcnt(2)\n"
    "v_exp_f32_e64 v114, v114\n"
    "v_mfma_f32_16x16x32_bf16 v[120:123], v[224:227], v[48:51], v[120:123]\n"
    "ds_read_b128 v[236:239], v245 offset:832\n"
    "v_cvt_pk_bf16_f32 v150, v112, v113\n"
    "v_mfma_f32_16x16x32_bf16 v[124:127], v[224:227], v[52:55], v[124:127]\n"
    "v_exp_f32_e64 v115, v115\n"
    "v_mfma_f32_16x16x32_bf16 v[128:131], v[224:227], v[56:59], v[128:131]\n"
    "s_waitcnt lgkmcnt(2)\n"
    "v_exp_f32_e64 v104, v104\n"
    "v_mfma_f32_16x16x32_bf16 v[132:135], v[228:231], v[48:51], v[132:135]\n"
    "ds_read_b128 v[224:227], v245 offset:384\n"
    "v_cvt_pk_bf16_f32 v151, v114, v115\n"
    "v_mfma_f32_16x16x32_bf16 v[136:139], v[228:231], v[52:55], v[136:139]\n"
    "v_exp_f32_e64 v105, v105\n"
    "v_mfma_f32_16x16x32_bf16 v[140:143], v[228:231], v[56:59], v[140:143]\n"
    "s_nop 7\n"
    "s_waitcnt lgkmcnt(2)\n"
    "v_exp_f32_e64 v106, v106\n"
    "v_mfma_f32_16x16x32_bf16 v[120:123], v[232:235], v[60:63], v[120:123]\n"
    "ds_read_b128 v[228:231], v245 offset:896\n"
    "v_cvt_pk_bf16_f32 v152, v104, v105\n"
    "v_mfma_f32_16x16x32_bf16 v[124:127], v[232:235], v[64:67], v[124:127]\n"
    "v_exp_f32_e64 v107, v107\n"
    "v_mfma_f32_16x16x32_bf16 v[128:131], v[232:235], v[68:71], v[128:131]\n"
    "s_waitcnt lgkmcnt(2)\n"
    "v_exp_f32_e64 v116, v116\n"
    "v_mfma_f32_16x16x32_bf16 v[132:135], v[236:239], v[60:63], v[132:135]\n"
    "ds_read_b128 v[232:235], v245 offset:448\n"
    "v_cvt_pk_bf16_f32 v153, v106, v107\n"
    "v_mfma_f32_16x16x32_bf16 v[136:139], v[236:239], v[64:67], v[136:139]\n"
    "v_exp_f32_e64 v117, v117\n"
    "v_mfma_f32_16x16x32_bf16 v[140:143], v[236:239], v[68:71], v[140:143]\n"
    "s_nop 7\n"
    "s_waitcnt lgkmcnt(2)\n"
    "v_exp_f32_e64 v118, v118\n"
    "v_mfma_f32_16x16x32_bf16 v[120:123], v[224:227], v[72:75], v[120:123]\n"
    "ds_read_b128 v[236:239], v245 offset:960\n"
    "v_cvt_pk_bf16_f32 v154, v116, v117\n"
    "v_mfma_f32_16x16x32_bf16 v[124:127], v[224:227], v[76:79], v[124:127]\n"
    "s_waitcnt vmcnt(17)\n"
    "v_exp_f32_e64 v119, v119\n"
    "v_mfma_f32_16x16x32_bf16 v[128:131], v[224:227], v[80:83], v[128:131]\n"
    "s_waitcnt lgkmcnt(2)\n"
    "v_cvt_pk_bf16_f32 v155, v118, v119\n"
    "v_add_u32_e64 v246, s52, v255\n"
    "v_mfma_f32_16x16x32_bf16 v[132:135], v[228:231], v[72:75], v[132:135]\n"
    "s_barrier\n"
    "ds_read_b64_tr_b16 v[224:225], v246 offset:0\n"
    "ds_read_b64_tr_b16 v[226:227], v246 offset:512\n"
    "v_mfma_f32_16x16x32_bf16 v[136:139], v[228:231], v[76:79], v[136:139]\n"
    "s_add_u32 s52, s52, 16896\n"
    "v_mfma_f32_16x16x32_bf16 v[140:143], v[228:231], v[80:83], v[140:143]\n"
    "s_nop 7\n"
    "s_waitcnt lgkmcnt(3)\n"
    "v_mfma_f32_16x16x32_bf16 v[120:123], v[232:235], v[84:87], v[120:123]\n"
    "s_cmp_eq_u32 s52, s53\n"
    "ds_read_b64_tr_b16 v[228:229], v246 offset:32\n"
    "ds_read_b64_tr_b16 v[230:231], v246 offset:544\n"
    "v_mfma_f32_16x16x32_bf16 v[124:127], v[232:235], v[88:91], v[124:127]\n"
    "s_cselect_b32 s52, s79, s52\n"
    "v_mfma_f32_16x16x32_bf16 v[128:131], v[232:235], v[92:95], v[128:131]\n"
    "s_waitcnt lgkmcnt(4)\n"
    "v_mfma_f32_16x16x32_bf16 v[132:135], v[236:239], v[84:87], v[132:135]\n"
    "ds_read_b64_tr_b16 v[232:233], v246 offset:64\n"
    "ds_read_b64_tr_b16 v[234:235], v246 offset:576\n"
    "v_mfma_f32_16x16x32_bf16 v[136:139], v[236:239], v[88:91], v[136:139]\n"
    "v_mfma_f32_16x16x32_bf16 v[140:143], v[236:239], v[92:95], v[140:143]\n"
    "s_nop 7\n"
    "s_waitcnt lgkmcnt(4)\n"
    "v_mfma_f32_16x16x32_bf16 a[0:3], v[224:227], v[144:147], 0\n"
    "s_mov_b32 m0, s49\n"
    "ds_read_b64_tr_b16 v[236:237], v246 offset:96\n"
    "ds_read_b64_tr_b16 v[238:239], v246 offset:608\n"
    "v_mfma_f32_16x16x32_bf16 a[4:7], v[224:227], v[148:151], 0\n"
    "v_add_f32_e64 v168, v168, v96\n"
    "v_add_f32_e64 v168, v168, v97\n"
    "v_mfma_f32_16x16x32_bf16 a[8:11], v[224:227], v[152:155], 0\n"
    "s_waitcnt lgkmcnt(4)\n"
    "v_add_f32_e64 v168, v168, v98\n"
    "v_add_f32_e64 v168, v168, v99\n"
    "v_mfma_f32_16x16x32_bf16 a[12:15], v[228:231], v[144:147], 0\n"
    "ds_read_b64_tr_b16 v[224:225], v246 offset:128\n"
    "ds_read_b64_tr_b16 v[226:227], v246 offset:640\n"
    "v_mfma_f32_16x16x32_bf16 a[16:19], v[228:231], v[148:151], 0\n"
    "buffer_load_dwordx4 v241, s[32:35], s56 offen offset:0 nt lds\n"
    "v_mfma_f32_16x16x32_bf16 a[20:23], v[228:231], v[152:155], 0\n"
    "s_nop 7\n"
    "s_waitcnt lgkmcnt(4)\n"
    "v_max3_f32 v171, v120, v121, v122\n"
    "v_max3_f32 v171, v171, v123, v124\n"
    "v_mfma_f32_16x16x32_bf16 a[24:27], v[232:235], v[144:147], 0\n"
    "s_add_u32 m0, s49, 544\n"
    "ds_read_b64_tr_b16 v[228:229], v246 offset:160\n"
    "ds_read_b64_tr_b16 v[230:231], v246 offset:672\n"
    "v_mfma_f32_16x16x32_bf16 a[28:31], v[232:235], v[148:151], 0\n"
    "v_max3_f32 v171, v171, v125, v126\n"
    "v_max3_f32 v171, v171, v127, v128\n"
    "v_mfma_f32_16x16x32_bf16 a[32:35], v[232:235], v[152:155], 0\n"
    "s_waitcnt lgkmcnt(4)\n"
    "buffer_load_dwordx4 v241, s[32:35], s56 offen offset:512 nt lds\n"
    "v_mfma_f32_16x16x32_bf16 a[36:39], v[236:239], v[144:147], 0\n"
    "s_add_u32 m0, s49, 1088\n"
    "ds_read_b64_tr_b16 v[232:233], v246 offset:192\n"
    "ds_read_b64_tr_b16 v[234:235], v246 offset:704\n"
    "v_mfma_f32_16x16x32_bf16 a[40:43], v[236:239], v[148:151], 0\n"
    "v_max3_f32 v171, v171, v129, v130\n"
    "v_max3_f32 v171, v171, v131, v132\n"
    "v_mfma_f32_16x16x32_bf16 a[44:47], v[236:239], v[152:155], 0\n"
    "s_nop 7\n"
    "s_waitcnt lgkmcnt(4)\n"
    "v_max3_f32 v171, v171, v133, v134\n"
    "v_max3_f32 v171, v171, v135, v136\n"
    "v_mfma_f32_16x16x32_bf16 a[48:51], v[224:227], v[144:147], 0\n"
    "ds_read_b64_tr_b16 v[236:237], v246 offset:224\n"
    "ds_read_b64_tr_b16 v[238:239], v246 offset:736\n"
    "v_mfma_f32_16x16x32_bf16 a[52:55], v[224:227], v[148:151], 0\n"
    "buffer_load_dwordx4 v241, s[32:35], s56 offen offset:1024 nt lds\n"
    "v_mfma_f32_16x16x32_bf16 a[56:59], v[224:227], v[152:155], 0\n"
    "s_waitcnt lgkmcnt(4)\n"
    "v_max3_f32 v171, v171, v137, v138\n"
    "v_max3_f32 v171, v171, v139, v140\n"
    "v_mfma_f32_16x16x32_bf16 a[60:63], v[228:231], v[144:147], 0\n"
    "s_add_u32 m0, s49, 1632\n"
    "ds_read_b64_tr_b16 v[224:225], v246 offset:256\n"
    "ds_read_b64_tr_b16 v[226:227], v246 offset:768\n"
    "v_mfma_f32_16x16x32_bf16 a[64:67], v[228:231], v[148:151], 0\n"
    "v_max3_f32 v171, v171, v141, v142\n"
    "v_max_f32_e64 v171, v171, v143\n"
    "v_mfma_f32_16x16x32_bf16 a[68:71], v[228:231], v[152:155], 0\n"
    "s_nop 7\n"
    "s_waitcnt lgkmcnt(4)\n"
    "buffer_load_dwordx4 v241, s[32:35], s56 offen offset:1536 nt lds\n"
    "v_mfma_f32_16x16x32_bf16 a[72:75], v[232:235], v[144:147], 0\n"
    "s_add_u32 s56, s56, 16384\n"
    "ds_read_b64_tr_b16 v[228:229], v246 offset:288\n"
    "ds_read_b64_tr_b16 v[230:231], v246 offset:800\n"
    "v_mfma_f32_16x16x32_bf16 a[76:79], v[232:235], v[148:151], 0\n"
    "s_and_b32 s56, s56, 262143\n"
    "v_cmp_lt_f32 vcc, 0x41000000, v171\n"
    "v_add_f32_e64 v168, v168, v108\n"
    "v_mfma_f32_16x16x32_bf16 a[80:83], v[232:235], v[152:155], 0\n"
    "s_waitcnt lgkmcnt(4)\n"
    "v_add_f32_e64 v168, v168, v109\n"
    "v_add_f32_e64 v168, v168, v110\n"
    "v_mfma_f32_16x16x32_bf16 a[84:87], v[236:239], v[144:147], 0\n"
    "s_add_u32 s49, s49, 16896\n"
    "s_cmp_eq_u32 s49, s50\n"
    "s_cselect_b32 s49, s51, s49\n"
    "s_mov_b32 m0, s49\n"
    "ds_read_b64_tr_b16 v[232:233], v246 offset:320\n"
    "ds_read_b64_tr_b16 v[234:235], v246 offset:832\n"
    "v_mfma_f32_16x16x32_bf16 a[88:91], v[236:239], v[148:151], 0\n"
    "buffer_load_dwordx4 v241, s[28:31], s55 offen offset:0 nt lds\n"
    "v_mfma_f32_16x16x32_bf16 a[92:95], v[236:239], v[152:155], 0\n"
    "s_nop 7\n"
    "s_waitcnt lgkmcnt(4)\n"
    "v_add_f32_e64 v168, v168, v111\n"
    "v_add_f32_e64 v169, v169, v100\n"
    "v_mfma_f32_16x16x32_bf16 a[96:99], v[224:227], v[144:147], 0\n"
    "s_add_u32 m0, s49, 544\n"
    "ds_read_b64_tr_b16 v[236:237], v246 offset:352\n"
    "ds_read_b64_tr_b16 v[238:239], v246 offset:864\n"
    "v_mfma_f32_16x16x32_bf16 a[100:103], v[224:227], v[148:151], 0\n"
    "v_add_f32_e64 v169, v169, v101\n"
    "v_add_f32_e64 v169, v169, v102\n"
    "v_mfma_f32_16x16x32_bf16 a[104:107], v[224:227], v[152:155], 0\n"
    "s_waitcnt lgkmcnt(4)\n"
    "buffer_load_dwordx4 v241, s[28:31], s55 offen offset:512 nt lds\n"
    "v_mfma_f32_16x16x32_bf16 a[108:111], v[228:231], v[144:147], 0\n"
    "s_add_u32 m0, s49, 1088\n"
    "ds_read_b64_tr_b16 v[224:225], v246 offset:384\n"
    "ds_read_b64_tr_b16 v[226:227], v246 offset:896\n"
    "v_mfma_f32_16x16x32_bf16 a[112:115], v[228:231], v[148:151], 0\n"
    "v_add_f32_e64 v169, v169, v103\n"
    "v_add_f32_e64 v169, v169, v112\n"
    "v_mfma_f32_16x16x32_bf16 a[116:119], v[228:231], v[152:155], 0\n"
    "s_nop 7\n"
    "s_waitcnt lgkmcnt(4)\n"
    "v_add_f32_e64 v169, v169, v113\n"
    "v_add_f32_e64 v169, v169, v114\n"
    "v_mfma_f32_16x16x32_bf16 a[120:123], v[232:235], v[144:147], 0\n"
    "ds_read_b64_tr_b16 v[228:229], v246 offset:416\n"
    "ds_read_b64_tr_b16 v[230:231], v246 offset:928\n"
    "v_mfma_f32_16x16x32_bf16 a[124:127], v[232:235], v[148:151], 0\n"
    "buffer_load_dwordx4 v241, s[28:31], s55 offen offset:1024 nt lds\n"
    "v_mfma_f32_16x16x32_bf16 a[128:131], v[232:235], v[152:155], 0\n"
    "s_waitcnt lgkmcnt(4)\n"
    "v_add_f32_e64 v169, v169, v115\n"
    "v_add_f32_e64 v170, v170, v104\n"
    "v_mfma_f32_16x16x32_bf16 a[132:135], v[236:239], v[144:147], 0\n"
    "s_add_u32 m0, s49, 1632\n"
    "ds_read_b64_tr_b16 v[232:233], v246 offset:448\n"
    "ds_read_b64_tr_b16 v[234:235], v246 offset:960\n"
    "v_mfma_f32_16x16x32_bf16 a[136:139], v[236:239], v[148:151], 0\n"
    "v_add_f32_e64 v170, v170, v105\n"
    "v_add_f32_e64 v170, v170, v106\n"
    "v_mfma_f32_16x16x32_bf16 a[140:143], v[236:239], v[152:155], 0\n"
    "s_nop 7\n"
    "s_waitcnt lgkmcnt(4)\n"
    "buffer_load_dwordx4 v241, s[28:31], s55 offen offset:1536 nt lds\n"
    "v_mfma_f32_16x16x32_bf16 a[144:147], v[224:227], v[144:147], 0\n"
    "s_add_u32 s55, s55, 16384\n"
    "ds_read_b64_tr_b16 v[236:237], v246 offset:480\n"
    "ds_read_b64_tr_b16 v[238:239], v246 offset:992\n"
    "v_mfma_f32_16x16x32_bf16 a[148:151], v[224:227], v[148:151], 0\n"
    "s_and_b32 s55, s55, 262143\n"
    "v_add_u32_e64 v245, s52, v254\n"
    "v_add_f32_e64 v170, v170, v107\n"
    "v_mfma_f32_16x16x32_bf16 a[152:155], v[224:227], v[152:155], 0\n"
    "s_waitcnt lgkmcnt(4)\n"
    "v_add_f32_e64 v170, v170, v116\n"
    "v_add_f32_e64 v170, v170, v117\n"
    "v_mfma_f32_16x16x32_bf16 a[156:159], v[228:231], v[144:147], 0\n"
    "s_add_u32 s52, s52, 16896\n"
    "ds_read_b128 v[224:227], v245 offset:0\n"
    "v_add_f32_e64 v170, v170, v118\n"
    "v_mfma_f32_16x16x32_bf16 a[160:163], v[228:231], v[148:151], 0\n"
    "s_cmp_eq_u32 s52, s53\n"
    "v_add_f32_e64 v170, v170, v119\n"
    "v_mfma_f32_16x16x32_bf16 a[164:167], v[228:231], v[152:155], 0\n"
    "s_nop 7\n"
    "s_waitcnt lgkmcnt(3)\n"
    "v_mfma_f32_16x16x32_bf16 a[168:171], v[232:235], v[144:147], 0\n"
    "s_cselect_b32 s52, s79, s52\n"
    "ds_read_b128 v[228:231], v245 offset:512\n"
    "v_mfma_f32_16x16x32_bf16 a[172:175], v[232:235], v[148:151], 0\n"
    "s_add_u32 s49, s49, 16896\n"
    "v_mfma_f32_16x16x32_bf16 a[176:179], v[232:235], v[152:155], 0\n"
    "s_waitcnt lgkmcnt(2)\n"
    "v_mfma_f32_16x16x32_bf16 a[180:183], v[236:239], v[144:147], 0\n"
    "s_cmp_eq_u32 s49, s50\n"
    "ds_read_b128 v[232:235], v245 offset:64\n"
    "v_mfma_f32_16x16x32_bf16 a[184:187], v[236:239], v[148:151], 0\n"
    "s_cselect_b32 s49, s51, s49\n"
    "v_mfma_f32_16x16x32_bf16 a[188:191], v[236:239], v[152:155], 0\n"
    "s_nop 7\n"
    "s_waitcnt lgkmcnt(2)\n"
    "s_cbranch_vccnz L_stub0_%=\n"
    "L_ret0_%=:\n"
    "L_att_%=:\n"
    "v_mfma_f32_16x16x32_bf16 v[96:99], v[224:227], v[0:3], v[156:159]\n"
    "ds_read_b128 v[236:239], v245 offset:576\n"
    "v_mfma_f32_16x16x32_bf16 v[100:103], v[224:227], v[4:7], v[160:163]\n"
    "v_exp_f32_e64 v120, v120\n"
    "v_mfma_f32_16x16x32_bf16 v[104:107], v[224:227], v[8:11], v[164:167]\n"
    "s_waitcnt lgkmcnt(2)\n"
    "v_exp_f32_e64 v121, v121\n"
    "v_mfma_f32_16x16x32_bf16 v[108:111], v[228:231], v[0:3], v[156:159]\n"
    "ds_read_b128 v[224:227], v245 offset:128\n"
    "v_cvt_pk_bf16_f32 v144, v120, v121\n"
    "v_mfma_f32_16x16x32_bf16 v[112:115], v[228:231], v[4:7], v[160:163]\n"
    "v_exp_f32_e64 v122, v122\n"
    "v_mfma_f32_16x16x32_bf16 v[116:119], v[228:231], v[8:11], v[164:167]\n"
    "s_nop 7\n"
    "s_waitcnt lgkmcnt(2)\n"
    "v_exp_f32_e64 v123, v123\n"
    "v_mfma_f32_16x16x32_bf16 v[96:99], v[232:235], v[12:15], v[96:99]\n"
    "ds_read_b128 v[228:231], v245 offset:640\n"
    "v_cvt_pk_bf16_f32 v145, v122, v123\n"
    "v_mfma_f32_16x16x32_bf16 v[100:103], v[232:235], v[16:19], v[100:103]\n"
    "v_exp_f32_e64 v132, v132\n"
    "v_mfma_f32_16x16x32_bf16 v[104:107], v[232:235], v[20:23], v[104:107]\n"
    "s_waitcnt lgkmcnt(2)\n"
    "v_exp_f32_e64 v133, v133\n"
    "v_mfma_f32_16x16x32_bf16 v[108:111], v[236:239], v[12:15], v[108:111]\n"
    "ds_read_b128 v[232:235], v245 offset:192\n"
    "v_cvt_pk_bf16_f32 v146, v132, v133\n"
    "v_mfma_f32_16x16x32_bf16 v[112:115], v[236:239], v[16:19], v[112:115]\n"
    "v_exp_f32_e64 v134, v134\n"
    "v_mfma_f32_16x16x32_bf16 v[116:119], v[236:239], v[20:23], v[116:119]\n"
    "s_nop 7\n"
    "s_waitcnt lgkmcnt(2)\n"
    "v_exp_f32_e64 v135, v135\n"
    "v_mfma_f32_16x16x32_bf16 v[96:99], v[224:227], v[24:27], v[96:99]\n"
    "ds_read_b128 v[236:239], v245 offset:704\n"
    "v_cvt_pk_bf16_f32 v147, v134, v135\n"
    "v_mfma_f32_16x16x32_bf16 v[100:103], v[224:227], v[28:31], v[100:103]\n"
    "v_exp_f32_e64 v124, v124\n"
    "v_mfma_f32_16x16x32_bf16 v[104:107], v[224:227], v[32:35], v[104:107]\n"
    "s_waitcnt lgkmcnt(2)\n"
    "v_exp_f32_e64 v125, v125\n"
    "v_mfma_f32_16x16x32_bf16 v[108:111], v[228:231], v[24:27], v[108:111]\n"
    "ds_read_b128 v[224:227], v245 offset:256\n"
    "v_cvt_pk_bf16_f32 v148, v124, v125\n"
    "v_mfma_f32_16x16x32_bf16 v[112:115], v[228:231], v[28:31], v[112:115]\n"
    "v_exp_f32_e64 v126, v126\n"
    "v_mfma_f32_16x16x32_bf16 v[116:119], v[228:231], v[32:35], v[116:119]\n"
    "s_nop 7\n"
    "s_waitcnt lgkmcnt(2)\n"
    "v_exp_f32_e64 v127, v127\n"
    "v_mfma_f32_16x16x32_bf16 v[96:99], v[232:235], v[36:39], v[96:99]\n"
    "ds_read_b128 v[228:231], v245 offset:768\n"
    "v_cvt_pk_bf16_f32 v149, v126, v127\n"
    "v_mfma_f32_16x16x32_bf16 v[100:103], v[232:235], v[40:43], v[100:103]\n"
    "v_exp_f32_e64 v136, v136\n"
    "v_mfma_f32_16x16x32_bf16 v[104:107], v[232:235], v[44:47], v[104:107]\n"
    "s_waitcnt lgkmcnt(2)\n"
    "v_exp_f32_e64 v137, v137\n"
    "v_mfma_f32_16x16x32_bf16 v[108:111], v[236:239], v[36:39], v[108:111]\n"
    "ds_read_b128 v[232:235], v245 offset:320\n"
    "v_cvt_pk_bf16_f32 v150, v136, v137\n"
    "v_mfma_f32_16x16x32_bf16 v[112:115], v[236:239], v[40:43], v[112:115]\n"
    "v_exp_f32_e64 v138, v138\n"
    "v_mfma_f32_16x16x32_bf16 v[116:119], v[236:239], v[44:47], v[116:119]\n"
    "s_nop 7\n"
    "s_waitcnt lgkmcnt(2)\n"
    "v_exp_f32_e64 v139, v139\n"
    "v_mfma_f32_16x16x32_bf16 v[96:99], v[224:227], v[48:51], v[96:99]\n"
    "ds_read_b128 v[236:239], v245 offset:832\n"
    "v_cvt_pk_bf16_f32 v151, v138, v139\n"
    "v_mfma_f32_16x16x32_bf16 v[100:103], v[224:227], v[52:55], v[100:103]\n"
    "v_exp_f32_e64 v128, v128\n"
    "v_mfma_f32_16x16x32_bf16 v[104:107], v[224:227], v[56:59], v[104:107]\n"
    "s_waitcnt lgkmcnt(2)\n"
    "v_exp_f32_e64 v129, v129\n"
    "v_mfma_f32_16x16x32_bf16 v[108:111], v[228:231], v[48:51], v[108:111]\n"
    "ds_read_b128 v[224:227], v245 offset:384\n"
    "v_cvt_pk_bf16_f32 v152, v128, v129\n"
    "v_mfma_f32_16x16x32_bf16 v[112:115], v[228:231], v[52:55], v[112:115]\n"
    "v_exp_f32_e64 v130, v130\n"
    "v_mfma_f32_16x16x32_bf16 v[116:119], v[228:231], v[56:59], v[116:119]\n"
    "s_nop 7\n"
    "s_waitcnt lgkmcnt(2)\n"
    "v_exp_f32_e64 v131, v131\n"
    "v_mfma_f32_16x16x32_bf16 v[96:99], v[232:235], v[60:63], v[96:99]\n"
    "ds_read_b128 v[228:231], v245 offset:896\n"
    "v_cvt_pk_bf16_f32 v153, v130, v131\n"
    "v_mfma_f32_16x16x32_bf16 v[100:103], v[232:235], v[64:67], v[100:103]\n"
    "v_exp_f32_e64 v140, v140\n"
    "v_mfma_f32_16x16x32_bf16 v[104:107], v[232:235], v[68:71], v[104:107]\n"
    "s_waitcnt lgkmcnt(2)\n"
    "v_exp_f32_e64 v141, v141\n"
    "v_mfma_f32_16x16x32_bf16 v[108:111], v[236:239], v[60:63], v[108:111]\n"
    "ds_read_b128 v[232:235], v245 offset:448\n"
    "v_cvt_pk_bf16_f32 v154, v140, v141\n"
    "v_mfma_f32_16x16x32_bf16 v[112:115], v[236:239], v[64:67], v[112:115]\n"
    "v_exp_f32_e64 v142, v142\n"
    "v_mfma_f32_16x16x32_bf16 v[116:119], v[236:239], v[68:71], v[116:119]\n"
    "s_nop 7\n"
    "s_waitcnt lgkmcnt(2)\n"
    "v_exp_f32_e64 v143, v143\n"
    "v_mfma_f32_16x16x32_bf16 v[96:99], v[224:227], v[72:75], v[96:99]\n"
    "ds_read_b128 v[236:239], v245 offset:960\n"
    "v_cvt_pk_bf16_f32 v155, v142, v143\n"
    "v_mfma_f32_16x16x32_bf16 v[100:103], v[224:227], v[76:79], v[100:103]\n"
    "s_waitcnt vmcnt(16)\n"
    "v_add_u32_e64 v246, s52, v255\n"
    "v_mfma_f32_16x16x32_bf16 v[104:107], v[224:227], v[80:83], v[104:107]\n"
    "s_waitcnt lgkmcnt(2)\n"
    "v_mfma_f32_16x16x32_bf16 v[108:111], v[228:231], v[72:75], v[108:111]\n"
    "s_barrier\n"
    "ds_read_b64_tr_b16 v[224:225], v246 offset:0\n"
    "ds_read_b64_tr_b16 v[226:227], v246 offset:512\n"
    "v_mfma_f32_16x16x32_bf16 v[112:115], v[228:231], v[76:79], v[112:115]\n"
    "s_add_u32 s52, s52, 16896\n"
    "v_mfma_f32_16x16x32_bf16 v[116:119], v[228:231], v[80:83], v[116:119]\n"
    "s_nop 7\n"
    "s_waitcnt lgkmcnt(3)\n"
    "v_mfma_f32_16x16x32_bf16 v[96:99], v[232:235], v[84:87], v[96:99]\n"
    "s_cmp_eq_u32 s52, s53\n"
    "ds_read_b64_tr_b16 v[228:229], v246 offset:32\n"
    "ds_read_b64_tr_b16 v[230:231], v246 offset:544\n"
    "v_mfma_f32_16x16x32_bf16 v[100:103], v[232:235], v[88:91], v[100:103]\n"
    "s_cselect_b32 s52, s79, s52\n"
    "v_mfma_f32_16x16x32_bf16 v[104:107], v[232:235], v[92:95], v[104:107]\n"
    "s_waitcnt lgkmcnt(4)\n"
    "v_mfma_f32_16x16x32_bf16 v[108:111], v[236:239], v[84:87], v[108:111]\n"
    "ds_read_b64_tr_b16 v[232:233], v246 offset:64\n"
    "ds_read_b64_tr_b16 v[234:235], v246 offset:576\n"
    "v_mfma_f32_16x16x32_bf16 v[112:115], v[236:239], v[88:91], v[112:115]\n"
    "v_mfma_f32_16x16x32_bf16 v[116:119], v[236:239], v[92:95], v[116:119]\n"
    "s_nop 7\n"
    "s_waitcnt lgkmcnt(4)\n"
    "v_mfma_f32_16x16x32_bf16 a[0:3], v[224:227], v[144:147], a[0:3]\n"
    "s_mov_b32 m0, s49\n"
    "ds_read_b64_tr_b16 v[236:237], v246 offset:96\n"
    "ds_read_b64_tr_b16 v[238:239], v246 offset:608\n"
    "v_mfma_f32_16x16x32_bf16 a[4:7], v[224:227], v[148:151], a[4:7]\n"
    "v_add_f32_e64 v168, v168, v120\n"
    "v_add_f32_e64 v168, v168, v121\n"
    "v_mfma_f32_16x16x32_bf16 a[8:11], v[224:227], v[152:155], a[8:11]\n"
    "s_waitcnt lgkmcnt(4)\n"
    "v_add_f32_e64 v168, v168, v122\n"
    "v_add_f32_e64 v168, v168, v123\n"
    "v_mfma_f32_16x16x32_bf16 a[12:15], v[228:231], v[144:147], a[12:15]\n"
    "ds_read_b64_tr_b16 v[224:225], v246 offset:128\n"
    "ds_read_b64_tr_b16 v[226:227], v246 offset:640\n"
    "v_mfma_f32_16x16x32_bf16 a[16:19], v[228:231], v[148:151], a[16:19]\n"
    "buffer_load_dwordx4 v241, s[32:35], s56 offen offset:0 nt lds\n"
    "v_mfma_f32_16x16x32_bf16 a[20:23], v[228:231], v[152:155], a[20:23]\n"
    "s_nop 7\n"
    "s_waitcnt lgkmcnt(4)\n"
    "v_max3_f32 v171, v96, v97, v98\n"
    "v_max3_f32 v171, v171, v99, v100\n"
    "v_mfma_f32_16x16x32_bf16 a[24:27], v[232:235], v[144:147], a[24:27]\n"
    "s_add_u32 m0, s49, 544\n"
    "ds_read_b64_tr_b16 v[228:229], v246 offset:160\n"
    "ds_read_b64_tr_b16 v[230:231], v246 offset:672\n"
    "v_mfma_f32_16x16x32_bf16 a[28:31], v[232:235], v[148:151], a[28:31]\n"
    "v_max3_f32 v171, v171, v101, v102\n"
    "v_max3_f32 v171, v171, v103, v104\n"
    "v_mfma_f32_16x16x32_bf16 a[32:35], v[232:235], v[152:155], a[32:35]\n"
    "s_waitcnt lgkmcnt(4)\n"
    "buffer_load_dwordx4 v241, s[32:35], s56 offen offset:512 nt lds\n"
    "v_mfma_f32_16x16x32_bf16 a[36:39], v[236:239], v[144:147], a[36:39]\n"
    "s_add_u32 m0, s49, 1088\n"
    "ds_read_b64_tr_b16 v[232:233], v246 offset:192\n"
    "ds_read_b64_tr_b16 v[234:235], v246 offset:704\n"
    "v_mfma_f32_16x16x32_bf16 a[40:43], v[236:239], v[148:151], a[40:43]\n"
    "v_max3_f32 v171, v171, v105, v106\n"
    "v_max3_f32 v171, v171, v107, v108\n"
    "v_mfma_f32_16x16x32_bf16 a[44:47], v[236:239], v[152:155], a[44:47]\n"
    "s_nop 7\n"
    "s_waitcnt lgkmcnt(4)\n"
    "v_max3_f32 v171, v171, v109, v110\n"
    "v_max3_f32 v171, v171, v111, v112\n"
    "v_mfma_f32_16x16x32_bf16 a[48:51], v[224:227], v[144:147], a[48:51]\n"
    "ds_read_b64_tr_b16 v[236:237], v246 offset:224\n"
    "ds_read_b64_tr_b16 v[238:239], v246 offset:736\n"
    "v_mfma_f32_16x16x32_bf16 a[52:55], v[224:227], v[148:151], a[52:55]\n"
    "buffer_load_dwordx4 v241, s[32:35], s56 offen offset:1024 nt lds\n"
    "v_mfma_f32_16x16x32_bf16 a[56:59], v[224:227], v[152:155], a[56:59]\n"
    "s_waitcnt lgkmcnt(4)\n"
    "v_max3_f32 v171, v171, v113, v114\n"
    "v_max3_f32 v171, v171, v115, v116\n"
    "v_mfma_f32_16x16x32_bf16 a[60:63], v[228:231], v[144:147], a[60:63]\n"
    "s_add_u32 m0, s49, 1632\n"
    "ds_read_b64_tr_b16 v[224:225], v246 offset:256\n"
    "ds_read_b64_tr_b16 v[226:227], v246 offset:768\n"
    "v_mfma_f32_16x16x32_bf16 a[64:67], v[228:231], v[148:151], a[64:67]\n"
    "v_max3_f32 v171, v171, v117, v118\n"
    "v_max_f32_e64 v171, v171, v119\n"
    "v_mfma_f32_16x16x32_bf16 a[68:71], v[228:231], v[152:155], a[68:71]\n"
    "s_nop 7\n"
    "s_waitcnt lgkmcnt(4)\n"
    "buffer_load_dwordx4 v241, s[32:35], s56 offen offset:1536 nt lds\n"
    "v_mfma_f32_16x16x32_bf16 a[72:75], v[232:235], v[144:147], a[72:75]\n"
    "s_add_u32 s56, s56, 16384\n"
    "ds_read_b64_tr_b16 v[228:229], v246 offset:288\n"
    "ds_read_b64_tr_b16 v[230:231], v246 offset:800\n"
    "v_mfma_f32_16x16x32_bf16 a[76:79], v[232:235], v[148:151], a[76:79]\n"
    "s_and_b32 s56, s56, 262143\n"
    "v_cmp_lt_f32 vcc, 0x41000000, v171\n"
    "v_add_f32_e64 v168, v168, v132\n"
    "v_mfma_f32_16x16x32_bf16 a[80:83], v[232:235], v[152:155], a[80:83]\n"
    "s_waitcnt lgkmcnt(4)\n"
    "v_add_f32_e64 v168, v168, v133\n"
    "v_add_f32_e64 v168, v168, v134\n"
    "v_mfma_f32_16x16x32_bf16 a[84:87], v[236:239], v[144:147], a[84:87]\n"
    "s_add_u32 s49, s49, 16896\n"
    "s_cmp_eq_u32 s49, s50\n"
    "s_cselect_b32 s49, s51, s49\n"
    "s_mov_b32 m0, s49\n"
    "ds_read_b64_tr_b16 v[232:233], v246 offset:320\n"
    "ds_read_b64_tr_b16 v[234:235], v246 offset:832\n"
    "v_mfma_f32_16x16x32_bf16 a[88:91], v[236:239], v[148:151], a[88:91]\n"
    "buffer_load_dwordx4 v241, s[28:31], s55 offen offset:0 nt lds\n"
    "v_mfma_f32_16x16x32_bf16 a[92:95], v[236:239], v[152:155], a[92:95]\n"
    "s_nop 7\n"
    "s_waitcnt lgkmcnt(4)\n"
    "v_add_f32_e64 v168, v168, v135\n"
    "v_add_f32_e64 v169, v169, v124\n"
    "v_mfma_f32_16x16x32_bf16 a[96:99], v[224:227], v[144:147], a[96:99]\n"
    "s_add_u32 m0, s49, 544\n"
    "ds_read_b64_tr_b16 v[236:237], v246 offset:352\n"
    "ds_read_b64_tr_b16 v[238:239], v246 offset:864\n"
    "v_mfma_f32_16x16x32_bf16 a[100:103], v[224:227], v[148:151], a[100:103]\n"
    "v_add_f32_e64 v169, v169, v125\n"
    "v_add_f32_e64 v169, v169, v126\n"
    "v_mfma_f32_16x16x32_bf16 a[104:107], v[224:227], v[152:155], a[104:107]\n"
    "s_waitcnt lgkmcnt(4)\n"
    "buffer_load_dwordx4 v241, s[28:31], s55 offen offset:512 nt lds\n"
    "v_mfma_f32_16x16x32_bf16 a[108:111], v[228:231], v[144:147], a[108:111]\n"
    "s_add_u32 m0, s49, 1088\n"
    "ds_read_b64_tr_b16 v[224:225], v246 offset:384\n"
    "ds_read_b64_tr_b16 v[226:227], v246 offset:896\n"
    "v_mfma_f32_16x16x32_bf16 a[112:115], v[228:231], v[148:151], a[112:115]\n"
    "v_add_f32_e64 v169, v169, v127\n"
    "v_add_f32_e64 v169, v169, v136\n"
    "v_mfma_f32_16x16x32_bf16 a[116:119], v[228:231], v[152:155], a[116:119]\n"
    "s_nop 7\n"
    "s_waitcnt lgkmcnt(4)\n"
    "v_add_f32_e64 v169, v169, v137\n"
    "v_add_f32_e64 v169, v169, v138\n"
    "v_mfma_f32_16x16x32_bf16 a[120:123], v[232:235], v[144:147], a[120:123]\n"
    "ds_read_b64_tr_b16 v[228:229], v246 offset:416\n"
    "ds_read_b64_tr_b16 v[230:231], v246 offset:928\n"
    "v_mfma_f32_16x16x32_bf16 a[124:127], v[232:235], v[148:151], a[124:127]\n"
    "buffer_load_dwordx4 v241, s[28:31], s55 offen offset:1024 nt lds\n"
    "v_mfma_f32_16x16x32_bf16 a[128:131], v[232:235], v[152:155], a[128:131]\n"
    "s_waitcnt lgkmcnt(4)\n"
    "v_add_f32_e64 v169, v169, v139\n"
    "v_add_f32_e64 v170, v170, v128\n"
    "v_mfma_f32_16x16x32_bf16 a[132:135], v[236:239], v[144:147], a[132:135]\n"
    "s_add_u32 m0, s49, 1632\n"
    "ds_read_b64_tr_b16 v[232:233], v246 offset:448\n"
    "ds_read_b64_tr_b16 v[234:235], v246 offset:960\n"
    "v_mfma_f32_16x16x32_bf16 a[136:139], v[236:239], v[148:151], a[136:139]\n"
    "v_add_f32_e64 v170, v170, v129\n"
    "v_add_f32_e64 v170, v170, v130\n"
    "v_mfma_f32_16x16x32_bf16 a[140:143], v[236:239], v[152:155], a[140:143]\n"
    "s_nop 7\n"
    "s_waitcnt lgkmcnt(4)\n"
    "buffer_load_dwordx4 v241, s[28:31], s55 offen offset:1536 nt lds\n"
    "v_mfma_f32_16x16x32_bf16 a[144:147], v[224:227], v[144:147], a[144:147]\n"
    "s_add_u32 s55, s55, 16384\n"
    "ds_read_b64_tr_b16 v[236:237], v246 offset:480\n"
    "ds_read_b64_tr_b16 v[238:239], v246 offset:992\n"
    "v_mfma_f32_16x16x32_bf16 a[148:151], v[224:227], v[148:151], a[148:151]\n"
    "s_and_b32 s55, s55, 262143\n"
    "v_add_u32_e64 v245, s52, v254\n"
    "v_add_f32_e64 v170, v170, v131\n"
    "v_mfma_f32_16x16x32_bf16 a[152:155], v[224:227], v[152:155], a[152:155]\n"
    "s_waitcnt lgkmcnt(4)\n"
    "v_add_f32_e64 v170, v170, v140\n"
    "v_add_f32_e64 v170, v170, v141\n"
    "v_mfma_f32_16x16x32_bf16 a[156:159], v[228:231], v[144:147], a[156:159]\n"
    "s_add_u32 s52, s52, 16896\n"
    "ds_read_b128 v[224:227], v245 offset:0\n"
    "v_add_f32_e64 v170, v170, v142\n"
    "v_mfma_f32_16x16x32_bf16 a[160:163], v[228:231], v[148:151], a[160:163]\n"
    "s_cmp_eq_u32 s52, s53\n"
    "v_add_f32_e64 v170, v170, v143\n"
    "v_mfma_f32_16x16x32_bf16 a[164:167], v[228:231], v[152:155], a[164:167]\n"
    "s_nop 7\n"
    "s_waitcnt lgkmcnt(3)\n"
    "v_mfma_f32_16x16x32_bf16 a[168:171], v[232:235], v[144:147], a[168:171]\n"
    "s_cselect_b32 s52, s79, s52\n"
    "ds_read_b128 v[228:231], v245 offset:512\n"
    "v_mfma_f32_16x16x32_bf16 a[172:175], v[232:235], v[148:151], a[172:175]\n"
    "s_add_u32 s49, s49, 16896\n"
    "v_mfma_f32_16x16x32_bf16 a[176:179], v[232:235], v[152:155], a[176:179]\n"
    "s_waitcnt lgkmcnt(2)\n"
    "v_mfma_f32_16x16x32_bf16 a[180:183], v[236:239], v[144:147], a[180:183]\n"
    "s_cmp_eq_u32 s49, s50\n"
    "ds_read_b128 v[232:235], v245 offset:64\n"
    "v_mfma_f32_16x16x32_bf16 a[184:187], v[236:239], v[148:151], a[184:187]\n"
    "s_cselect_b32 s49, s51, s49\n"
    "v_mfma_f32_16x16x32_bf16 a[188:191], v[236:239], v[152:155], a[188:191]\n"
    "s_nop 7\n"
    "s_waitcnt lgkmcnt(2)\n"
    "s_cbranch_vccnz L_stub1_%=\n"
    "L_ret1_%=:\n"
    "v_mfma_f32_16x16x32_bf16 v[120:123], v[224:227], v[0:3], v[156:159]\n"
    "ds_read_b128 v[236:239], v245 offset:576\n"
    "v_mfma_f32_16x16x32_bf16 v[124:127], v[224:227], v[4:7], v[160:163]\n"
    "v_exp_f32_e64 v96, v96\n"
    "v_mfma_f32_16x16x32_bf16 v[128:131], v[224:227], v[8:11], v[164:167]\n"
    "s_waitcnt lgkmcnt(2)\n"
    "v_exp_f32_e64 v97, v97\n"
    "v_mfma_f32_16x16x32_bf16 v[132:135], v[228:231], v[0:3], v[156:159]\n"
    "ds_read_b128 v[224:227], v245 offset:128\n"
    "v_cvt_pk_bf16_f32 v144, v96, v97\n"
    "v_mfma_f32_16x16x32_bf16 v[136:139], v[228:231], v[4:7], v[160:163]\n"
    "v_exp_f32_e64 v98, v98\n"
    "v_mfma_f32_16x16x32_bf16 v[140:143], v[228:231], v[8:11], v[164:167]\n"
    "s_nop 7\n"
    "s_waitcnt lgkmcnt(2)\n"
    "v_exp_f32_e64 v99, v99\n"
    "v_mfma_f32_16x16x32_bf16 v[120:123], v[232:235], v[12:15], v[120:123]\n"
    "ds_read_b128 v[228:231], v245 offset:640\n"
    "v_cvt_pk_bf16_f32 v145, v98, v99\n"
    "v_mfma_f32_16x16x32_bf16 v[124:127], v[232:235], v[16:19], v[124:127]\n"
    "v_exp_f32_e64 v108, v108\n"
    "v_mfma_f32_16x16x32_bf16 v[128:131], v[232:235], v[20:23], v[128:131]\n"
    "s_waitcnt lgkmcnt(2)\n"
    "v_exp_f32_e64 v109, v109\n"
    "v_mfma_f32_16x16x32_bf16 v[132:135], v[236:239], v[12:15], v[132:135]\n"
    "ds_read_b128 v[232:235], v245 offset:192\n"
    "v_cvt_pk_bf16_f32 v146, v108, v109\n"
    "v_mfma_f32_16x16x32_bf16 v[136:139], v[236:239], v[16:19], v[136:139]\n"
    "v_exp_f32_e64 v110, v110\n"
    "v_mfma_f32_16x16x32_bf16 v[140:143], v[236:239], v[20:23], v[140:143]\n"
    "s_nop 7\n"
    "s_waitcnt lgkmcnt(2)\n"
    "v_exp_f32_e64 v111, v111\n"
    "v_mfma_f32_16x16x32_bf16 v[120:123], v[224:227], v[24:27], v[120:123]\n"
    "ds_read_b128 v[236:239], v245 offset:704\n"
    "v_cvt_pk_bf16_f32 v147, v110, v111\n"
    "v_mfma_f32_16x16x32_bf16 v[124:127], v[224:227], v[28:31], v[124:127]\n"
    "v_exp_f32_e64 v100, v100\n"
    "v_mfma_f32_16x16x32_bf16 v[128:131], v[224:227], v[32:35], v[128:131]\n"
    "s_waitcnt lgkmcnt(2)\n"
    "v_exp_f32_e64 v101, v101\n"
    "v_mfma_f32_16x16x32_bf16 v[132:135], v[228:231], v[24:27], v[132:135]\n"
    "ds_read_b128 v[224:227], v245 offset:256\n"
    "v_cvt_pk_bf16_f32 v148, v100, v101\n"
    "v_mfma_f32_16x16x32_bf16 v[136:139], v[228:231], v[28:31], v[136:139]\n"
    "v_exp_f32_e64 v102, v102\n"
    "v_mfma_f32_16x16x32_bf16 v[140:143], v[228:231], v[32:35], v[140:143]\n"
    "s_nop 7\n"
    "s_waitcnt lgkmcnt(2)\n"
    "v_exp_f32_e64 v103, v103\n"
    "v_mfma_f32_16x16x32_bf16 v[120:123], v[232:235], v[36:39], v[120:123]\n"
    "ds_read_b128 v[228:231], v245 offset:768\n"
    "v_cvt_pk_bf16_f32 v149, v102, v103\n"
    "v_mfma_f32_16x16x32_bf16 v[124:127], v[232:235], v[40:43], v[124:127]\n"
    "v_exp_f32_e64 v112, v112\n"
    "v_mfma_f32_16x16x32_bf16 v[128:131], v[232:235], v[44:47], v[128:131]\n"
    "s_waitcnt lgkmcnt(2)\n"
    "v_exp_f32_e64 v113, v113\n"
    "v_mfma_f32_16x16x32_bf16 v[132:135], v[236:239], v[36:39], v[132:135]\n"
    "ds_read_b128 v[232:235], v245 offset:320\n"
    "v_cvt_pk_bf16_f32 v150, v112, v113\n"
    "v_mfma_f32_16x16x32_bf16 v[136:139], v[236:239], v[40:43], v[136:139]\n"
    "v_exp_f32_e64 v114, v114\n"
    "v_mfma_f32_16x16x32_bf16 v[140:143], v[236:239], v[44:47], v[140:143]\n"
    "s_nop 7\n"
    "s_waitcnt lgkmcnt(2)\n"
    "v_exp_f32_e64 v115, v115\n"
    "v_mfma_f32_16x16x32_bf16 v[120:123], v[224:227], v[48:51], v[120:123]\n"
    "ds_read_b128 v[236:239], v245 offset:832\n"
    "v_cvt_pk_bf16_f32 v151, v114, v115\n"
    "v_mfma_f32_16x16x32_bf16 v[124:127], v[224:227], v[52:55], v[124:127]\n"
    "v_exp_f32_e64 v104, v104\n"
    "v_mfma_f32_16x16x32_bf16 v[128:131], v[224:227], v[56:59], v[128:131]\n"
    "s_waitcnt lgkmcnt(2)\n"
    "v_exp_f32_e64 v105, v105\n"
    "v_mfma_f32_16x16x32_bf16 v[132:135], v[228:231], v[48:51], v[132:135]\n"
    "ds_read_b128 v[224:227], v245 offset:384\n"
    "v_cvt_pk_bf16_f32 v152, v104, v105\n"
    "v_mfma_f32_16x16x32_bf16 v[136:139], v[228:231], v[52:55], v[136:139]\n"
    "v_exp_f32_e64 v106, v106\n"
    "v_mfma_f32_16x16x32_bf16 v[140:143], v[228:231], v[56:59], v[140:143]\n"
    "s_nop 7\n"
    "s_waitcnt lgkmcnt(2)\n"
    "v_exp_f32_e64 v107, v107\n"
    "v_mfma_f32_16x16x32_bf16 v[120:123], v[232:235], v[60:63], v[120:123]\n"
    "ds_read_b128 v[228:231], v245 offset:896\n"
    "v_cvt_pk_bf16_f32 v153, v106, v107\n"
    "v_mfma_f32_16x16x32_bf16 v[124:127], v[232:235], v[64:67], v[124:127]\n"
    "v_exp_f32_e64 v116, v116\n"
    "v_mfma_f32_16x16x32_bf16 v[128:131], v[232:235], v[68:71], v[128:131]\n"
    "s_waitcnt lgkmcnt(2)\n"
    "v_exp_f32_e64 v117, v117\n"
    "v_mfma_f32_16x16x32_bf16 v[132:135], v[236:239], v[60:63], v[132:135]\n"
    "ds_read_b128 v[232:235], v245 offset:448\n"
    "v_cvt_pk_bf16_f32 v154, v116, v117\n"
    "v_mfma_f32_16x16x32_bf16 v[136:139], v[236:239], v[64:67], v[136:139]\n"
    "v_exp_f32_e64 v118, v118\n"
    "v_mfma_f32_16x16x32_bf16 v[140:143], v[236:239], v[68:71], v[140:143]\n"
    "s_nop 7\n"
    "s_waitcnt lgkmcnt(2)\n"
    "v_exp_f32_e64 v119, v119\n"
    "v_mfma_f32_16x16x32_bf16 v[120:123], v[224:227], v[72:75], v[120:123]\n"
    "ds_read_b128 v[236:239], v245 offset:960\n"
    "v_cvt_pk_bf16_f32 v155, v118, v119\n"
    "v_mfma_f32_16x16x32_bf16 v[124:127], v[224:227], v[76:79], v[124:127]\n"
    "s_waitcnt vmcnt(16)\n"
    "v_add_u32_e64 v246, s52, v255\n"
    "v_mfma_f32_16x16x32_bf16 v[128:131], v[224:227], v[80:83], v[128:131]\n"
    "s_waitcnt lgkmcnt(2)\n"
    "v_mfma_f32_16x16x32_bf16 v[132:135], v[228:231], v[72:75], v[132:135]\n"
    "s_barrier\n"
    "ds_read_b64_tr_b16 v[224:225], v246 offset:0\n"
    "ds_read_b64_tr_b16 v[226:227], v246 offset:512\n"
    "v_mfma_f32_16x16x32_bf16 v[136:139], v[228:231], v[76:79], v[136:139]\n"
    "s_add_u32 s52, s52, 16896\n"
    "v_mfma_f32_16x16x32_bf16 v[140:143], v[228:231], v[80:83], v[140:143]\n"
    "s_nop 7\n"
    "s_waitcnt lgkmcnt(3)\n"
    "v_mfma_f32_16x16x32_bf16 v[120:123], v[232:235], v[84:87], v[120:123]\n"
    "s_cmp_eq_u32 s52, s53\n"
    "ds_read_b64_tr_b16 v[228:229], v246 offset:32\n"
    "ds_read_b64_tr_b16 v[230:231], v246 offset:544\n"
    "v_mfma_f32_16x16x32_bf16 v[124:127], v[232:235], v[88:91], v[124:127]\n"
    "s_cselect_b32 s52, s79, s52\n"
    "v_mfma_f32_16x16x32_bf16 v[128:131], v[232:235], v[92:95], v[128:131]\n"
    "s_waitcnt lgkmcnt(4)\n"
    "v_mfma_f32_16x16x32_bf16 v[132:135], v[236:239], v[84:87], v[132:135]\n"
    "ds_read_b64_tr_b16 v[232:233], v246 offset:64\n"
    "ds_read_b64_tr_b16 v[234:235], v246 offset:576\n"
    "v_mfma_f32_16x16x32_bf16 v[136:139], v[236:239], v[88:91], v[136:139]\n"
    "v_mfma_f32_16x16x32_bf16 v[140:143], v[236:239], v[92:95], v[140:143]\n"
    "s_nop 7\n"
    "s_waitcnt lgkmcnt(4)\n"
    "v_mfma_f32_16x16x32_bf16 a[0:3], v[224:227], v[144:147], a[0:3]\n"
    "s_mov_b32 m0, s49\n"
    "ds_read_b64_tr_b16 v[236:237], v246 offset:96\n"
    "ds_read_b64_tr_b16 v[238:239], v246 offset:608\n"
    "v_mfma_f32_16x16x32_bf16 a[4:7], v[224:227], v[148:151], a[4:7]\n"
    "v_add_f32_e64 v168, v168, v96\n"
    "v_add_f32_e64 v168, v168, v97\n"
    "v_mfma_f32_16x16x32_bf16 a[8:11], v[224:227], v[152:155], a[8:11]\n"
    "s_waitcnt lgkmcnt(4)\n"
    "v_add_f32_e64 v168, v168, v98\n"
    "v_add_f32_e64 v168, v168, v99\n"
    "v_mfma_f32_16x16x32_bf16 a[12:15], v[228:231], v[144:147], a[12:15]\n"
    "ds_read_b64_tr_b16 v[224:225], v246 offset:128\n"
    "ds_read_b64_tr_b16 v[226:227], v246 offset:640\n"
    "v_mfma_f32_16x16x32_bf16 a[16:19], v[228:231], v[148:151], a[16:19]\n"
    "buffer_load_dwordx4 v241, s[32:35], s56 offen offset:0 nt lds\n"
    "v_mfma_f32_16x16x32_bf16 a[20:23], v[228:231], v[152:155], a[20:23]\n"
    "s_nop 7\n"
    "s_waitcnt lgkmcnt(4)\n"
    "v_max3_f32 v171, v120, v121, v122\n"
    "v_max3_f32 v171, v171, v123, v124\n"
    "v_mfma_f32_16x16x32_bf16 a[24:27], v[232:235], v[144:147], a[24:27]\n"
    "s_add_u32 m0, s49, 544\n"
    "ds_read_b64_tr_b16 v[228:229], v246 offset:160\n"
    "ds_read_b64_tr_b16 v[230:231], v246 offset:672\n"
    "v_mfma_f32_16x16x32_bf16 a[28:31], v[232:235], v[148:151], a[28:31]\n"
    "v_max3_f32 v171, v171, v125, v126\n"
    "v_max3_f32 v171, v171, v127, v128\n"
    "v_mfma_f32_16x16x32_bf16 a[32:35], v[232:235], v[152:155], a[32:35]\n"
    "s_waitcnt lgkmcnt(4)\n"
    "buffer_load_dwordx4 v241, s[32:35], s56 offen offset:512 nt lds\n"
    "v_mfma_f32_16x16x32_bf16 a[36:39], v[236:239], v[144:147], a[36:39]\n"
    "s_add_u32 m0, s49, 1088\n"
    "ds_read_b64_tr_b16 v[232:233], v246 offset:192\n"
    "ds_read_b64_tr_b16 v[234:235], v246 offset:704\n"
    "v_mfma_f32_16x16x32_bf16 a[40:43], v[236:239], v[148:151], a[40:43]\n"
    "v_max3_f32 v171, v171, v129, v130\n"
    "v_max3_f32 v171, v171, v131, v132\n"
    "v_mfma_f32_16x16x32_bf16 a[44:47], v[236:239], v[152:155], a[44:47]\n"
    "s_nop 7\n"
    "s_waitcnt lgkmcnt(4)\n"
    "v_max3_f32 v171, v171, v133, v134\n"
    "v_max3_f32 v171, v171, v135, v136\n"
    "v_mfma_f32_16x16x32_bf16 a[48:51], v[224:227], v[144:147], a[48:51]\n"
    "ds_read_b64_tr_b16 v[236:237], v246 offset:224\n"
    "ds_read_b64_tr_b16 v[238:239], v246 offset:736\n"
    "v_mfma_f32_16x16x32_bf16 a[52:55], v[224:227], v[148:151], a[52:55]\n"
    "buffer_load_dwordx4 v241, s[32:35], s56 offen offset:1024 nt lds\n"
    "v_mfma_f32_16x16x32_bf16 a[56:59], v[224:227], v[152:155], a[56:59]\n"
    "s_waitcnt lgkmcnt(4)\n"
    "v_max3_f32 v171, v171, v137, v138\n"
    "v_max3_f32 v171, v171, v139, v140\n"
    "v_mfma_f32_16x16x32_bf16 a[60:63], v[228:231], v[144:147], a[60:63]\n"
    "s_add_u32 m0, s49, 1632\n"
    "ds_read_b64_tr_b16 v[224:225], v246 offset:256\n"
    "ds_read_b64_tr_b16 v[226:227], v246 offset:768\n"
    "v_mfma_f32_16x16x32_bf16 a[64:67], v[228:231], v[148:151], a[64:67]\n"
    "v_max3_f32 v171, v171, v141, v142\n"
    "v_max_f32_e64 v171, v171, v143\n"
    "v_mfma_f32_16x16x32_bf16 a[68:71], v[228:231], v[152:155], a[68:71]\n"
    "s_nop 7\n"
    "s_waitcnt lgkmcnt(4)\n"
    "buffer_load_dwordx4 v241, s[32:35], s56 offen offset:1536 nt lds\n"
    "v_mfma_f32_16x16x32_bf16 a[72:75], v[232:235], v[144:147], a[72:75]\n"
    "s_add_u32 s56, s56, 16384\n"
    "ds_read_b64_tr_b16 v[228:229], v246 offset:288\n"
    "ds_read_b64_tr_b16 v[230:231], v246 offset:800\n"
    "v_mfma_f32_16x16x32_bf16 a[76:79], v[232:235], v[148:151], a[76:79]\n"
    "s_and_b32 s56, s56, 262143\n"
    "v_cmp_lt_f32 vcc, 0x41000000, v171\n"
    "v_add_f32_e64 v168, v168, v108\n"
    "v_mfma_f32_16x16x32_bf16 a[80:83], v[232:235], v[152:155], a[80:83]\n"
    "s_waitcnt lgkmcnt(4)\n"
    "v_add_f32_e64 v168, v168, v109\n"
    "v_add_f32_e64 v168, v168, v110\n"
    "v_mfma_f32_16x16x32_bf16 a[84:87], v[236:239], v[144:147], a[84:87]\n"
    "s_add_u32 s49, s49, 16896\n"
    "s_cmp_eq_u32 s49, s50\n"
    "s_cselect_b32 s49, s51, s49\n"
    "s_mov_b32 m0, s49\n"
    "ds_read_b64_tr_b16 v[232:233], v246 offset:320\n"
    "ds_read_b64_tr_b16 v[234:235], v246 offset:832\n"
    "v_mfma_f32_16x16x32_bf16 a[88:91], v[236:239], v[148:151], a[88:91]\n"
    "buffer_load_dwordx4 v241, s[28:31], s55 offen offset:0 nt lds\n"
    "v_mfma_f32_16x16x32_bf16 a[92:95], v[236:239], v[152:155], a[92:95]\n"
    "s_nop 7\n"
    "s_waitcnt lgkmcnt(4)\n"
    "v_add_f32_e64 v168, v168, v111\n"
    "v_add_f32_e64 v169, v169, v100\n"
    "v_mfma_f32_16x16x32_bf16 a[96:99], v[224:227], v[144:147], a[96:99]\n"
    "s_add_u32 m0, s49, 544\n"
    "ds_read_b64_tr_b16 v[236:237], v246 offset:352\n"
    "ds_read_b64_tr_b16 v[238:239], v246 offset:864\n"
    "v_mfma_f32_16x16x32_bf16 a[100:103], v[224:227], v[148:151], a[100:103]\n"
    "v_add_f32_e64 v169, v169, v101\n"
    "v_add_f32_e64 v169, v169, v102\n"
    "v_mfma_f32_16x16x32_bf16 a[104:107], v[224:227], v[152:155], a[104:107]\n"
    "s_waitcnt lgkmcnt(4)\n"
    "buffer_load_dwordx4 v241, s[28:31], s55 offen offset:512 nt lds\n"
    "v_mfma_f32_16x16x32_bf16 a[108:111], v[228:231], v[144:147], a[108:111]\n"
    "s_add_u32 m0, s49, 1088\n"
    "ds_read_b64_tr_b16 v[224:225], v246 offset:384\n"
    "ds_read_b64_tr_b16 v[226:227], v246 offset:896\n"
    "v_mfma_f32_16x16x32_bf16 a[112:115], v[228:231], v[148:151], a[112:115]\n"
    "v_add_f32_e64 v169, v169, v103\n"
    "v_add_f32_e64 v169, v169, v112\n"
    "v_mfma_f32_16x16x32_bf16 a[116:119], v[228:231], v[152:155], a[116:119]\n"
    "s_nop 7\n"
    "s_waitcnt lgkmcnt(4)\n"
    "v_add_f32_e64 v169, v169, v113\n"
    "v_add_f32_e64 v169, v169, v114\n"
    "v_mfma_f32_16x16x32_bf16 a[120:123], v[232:235], v[144:147], a[120:123]\n"
    "ds_read_b64_tr_b16 v[228:229], v246 offset:416\n"
    "ds_read_b64_tr_b16 v[230:231], v246 offset:928\n"
    "v_mfma_f32_16x16x32_bf16 a[124:127], v[232:235], v[148:151], a[124:127]\n"
    "buffer_load_dwordx4 v241, s[28:31], s55 offen offset:1024 nt lds\n"
    "v_mfma_f32_16x16x32_bf16 a[128:131], v[232:235], v[152:155], a[128:131]\n"
    "s_waitcnt lgkmcnt(4)\n"
    "v_add_f32_e64 v169, v169, v115\n"
    "v_add_f32_e64 v170, v170, v104\n"
    "v_mfma_f32_16x16x32_bf16 a[132:135], v[236:239], v[144:147], a[132:135]\n"
    "s_add_u32 m0, s49, 1632\n"
    "ds_read_b64_tr_b16 v[232:233], v246 offset:448\n"
    "ds_read_b64_tr_b16 v[234:235], v246 offset:960\n"
    "v_mfma_f32_16x16x32_bf16 a[136:139], v[236:239], v[148:151], a[136:139]\n"
    "v_add_f32_e64 v170, v170, v105\n"
    "v_add_f32_e64 v170, v170, v106\n"
    "v_mfma_f32_16x16x32_bf16 a[140:143], v[236:239], v[152:155], a[140:143]\n"
    "s_nop 7\n"
    "s_waitcnt lgkmcnt(4)\n"
    "buffer_load_dwordx4 v241, s[28:31], s55 offen offset:1536 nt lds\n"
    "v_mfma_f32_16x16x32_bf16 a[144:147], v[224:227], v[144:147], a[144:147]\n"
    "s_add_u32 s55, s55, 16384\n"
    "ds_read_b64_tr_b16 v[236:237], v246 offset:480\n"
    "ds_read_b64_tr_b16 v[238:239], v246 offset:992\n"
    "v_mfma_f32_16x16x32_bf16 a[148:151], v[224:227], v[148:151], a[148:151]\n"
    "s_and_b32 s55, s55, 262143\n"
    "v_add_u32_e64 v245, s52, v254\n"
    "v_add_f32_e64 v170, v170, v107\n"
    "v_mfma_f32_16x16x32_bf16 a[152:155], v[224:227], v[152:155], a[152:155]\n"
    "s_waitcnt lgkmcnt(4)\n"
    "v_add_f32_e64 v170, v170, v116\n"
    "v_add_f32_e64 v170, v170, v117\n"
    "v_mfma_f32_16x16x32_bf16 a[156:159], v[228:231], v[144:147], a[156:159]\n"
    "s_add_u32 s52, s52, 16896\n"
    "ds_read_b128 v[224:227], v245 offset:0\n"
    "v_add_f32_e64 v170, v170, v118\n"
    "v_mfma_f32_16x16x32_bf16 a[160:163], v[228:231], v[148:151], a[160:163]\n"
    "s_cmp_eq_u32 s52, s53\n"
    "v_add_f32_e64 v170, v170, v119\n"
    "v_mfma_f32_16x16x32_bf16 a[164:167], v[228:231], v[152:155], a[164:167]\n"
    "s_nop 7\n"
    "s_waitcnt lgkmcnt(3)\n"
    "v_mfma_f32_16x16x32_bf16 a[168:171], v[232:235], v[144:147], a[168:171]\n"
    "s_cselect_b32 s52, s79, s52\n"
    "ds_read_b128 v[228:231], v245 offset:512\n"
    "v_mfma_f32_16x16x32_bf16 a[172:175], v[232:235], v[148:151], a[172:175]\n"
    "s_add_u32 s49, s49, 16896\n"
    "v_mfma_f32_16x16x32_bf16 a[176:179], v[232:235], v[152:155], a[176:179]\n"
    "s_waitcnt lgkmcnt(2)\n"
    "v_mfma_f32_16x16x32_bf16 a[180:183], v[236:239], v[144:147], a[180:183]\n"
    "s_cmp_eq_u32 s49, s50\n"
    "ds_read_b128 v[232:235], v245 offset:64\n"
    "v_mfma_f32_16x16x32_bf16 a[184:187], v[236:239], v[148:151], a[184:187]\n"
    "s_cselect_b32 s49, s51, s49\n"
    "v_mfma_f32_16x16x32_bf16 a[188:191], v[236:239], v[152:155], a[188:191]\n"
    "s_nop 7\n"
    "s_waitcnt lgkmcnt(2)\n"
    "s_cbranch_vccnz L_stub2_%=\n"
    "L_ret2_%=:\n"
    "s_sub_u32 s70, s70, 1\n"
    "s_cmp_lg_u32 s70, 0\n"
    "s_cbranch_scc1 L_att_%=\n"
    "v_mfma_f32_16x16x32_bf16 v[96:99], v[224:227], v[0:3], v[156:159]\n"
    "ds_read_b128 v[236:239], v245 offset:576\n"
    "v_mfma_f32_16x16x32_bf16 v[100:103], v[224:227], v[4:7], v[160:163]\n"
    "v_exp_f32_e64 v120, v120\n"
    "v_mfma_f32_16x16x32_bf16 v[104:107], v[224:227], v[8:11], v[164:167]\n"
    "s_waitcnt lgkmcnt(2)\n"
    "v_exp_f32_e64 v121, v121\n"
    "v_mfma_f32_16x16x32_bf16 v[108:111], v[228:231], v[0:3], v[156:159]\n"
    "ds_read_b128 v[224:227], v245 offset:128\n"
    "v_cvt_pk_bf16_f32 v144, v120, v121\n"
    "v_mfma_f32_16x16x32_bf16 v[112:115], v[228:231], v[4:7], v[160:163]\n"
    "v_exp_f32_e64 v122, v122\n"
    "v_mfma_f32_16x16x32_bf16 v[116:119], v[228:231], v[8:11], v[164:167]\n"
    "s_nop 7\n"
    "s_waitcnt lgkmcnt(2)\n"
    "v_exp_f32_e64 v123, v123\n"
    "v_mfma_f32_16x16x32_bf16 v[96:99], v[232:235], v[12:15], v[96:99]\n"
    "ds_read_b128 v[228:231], v245 offset:640\n"
    "v_cvt_pk_bf16_f32 v145, v122, v123\n"
    "v_mfma_f32_16x16x32_bf16 v[100:103], v[232:235], v[16:19], v[100:103]\n"
    "v_exp_f32_e64 v132, v132\n"
    "v_mfma_f32_16x16x32_bf16 v[104:107], v[232:235], v[20:23], v[104:107]\n"
    "s_waitcnt lgkmcnt(2)\n"
    "v_exp_f32_e64 v133, v133\n"
    "v_mfma_f32_16x16x32_bf16 v[108:111], v[236:239], v[12:15], v[108:111]\n"
    "ds_read_b128 v[232:235], v245 offset:192\n"
    "v_cvt_pk_bf16_f32 v146, v132, v133\n"
    "v_mfma_f32_16x16x32_bf16 v[112:115], v[236:239], v[16:19], v[112:115]\n"
    "v_exp_f32_e64 v134, v134\n"
    "v_mfma_f32_16x16x32_bf16 v[116:119], v[236:239], v[20:23], v[116:119]\n"
    "s_nop 7\n"
    "s_waitcnt lgkmcnt(2)\n"
    "v_exp_f32_e64 v135, v135\n"
    "v_mfma_f32_16x16x32_bf16 v[96:99], v[224:227], v[24:27], v[96:99]\n"
    "ds_read_b128 v[236:239], v245 offset:704\n"
    "v_cvt_pk_bf16_f32 v147, v134, v135\n"
    "v_mfma_f32_16x16x32_bf16 v[100:103], v[224:227], v[28:31], v[100:103]\n"
    "v_exp_f32_e64 v124, v124\n"
    "v_mfma_f32_16x16x32_bf16 v[104:107], v[224:227], v[32:35], v[104:107]\n"
    "s_waitcnt lgkmcnt(2)\n"
    "v_exp_f32_e64 v125, v125\n"
    "v_mfma_f32_16x16x32_bf16 v[108:111], v[228:231], v[24:27], v[108:111]\n"
    "ds_read_b128 v[224:227], v245 offset:256\n"
    "v_cvt_pk_bf16_f32 v148, v124, v125\n"
    "v_mfma_f32_16x16x32_bf16 v[112:115], v[228:231], v[28:31], v[112:115]\n"
    "v_exp_f32_e64 v126, v126\n"
    "v_mfma_f32_16x16x32_bf16 v[116:119], v[228:231], v[32:35], v[116:119]\n"
    "s_nop 7\n"
    "s_waitcnt lgkmcnt(2)\n"
    "v_exp_f32_e64 v127, v127\n"
    "v_mfma_f32_16x16x32_bf16 v[96:99], v[232:235], v[36:39], v[96:99]\n"
    "ds_read_b128 v[228:231], v245 offset:768\n"
    "v_cvt_pk_bf16_f32 v149, v126, v127\n"
    "v_mfma_f32_16x16x32_bf16 v[100:103], v[232:235], v[40:43], v[100:103]\n"
    "v_exp_f32_e64 v136, v136\n"
    "v_mfma_f32_16x16x32_bf16 v[104:107], v[232:235], v[44:47], v[104:107]\n"
    "s_waitcnt lgkmcnt(2)\n"
    "v_exp_f32_e64 v137, v137\n"
    "v_mfma_f32_16x16x32_bf16 v[108:111], v[236:239], v[36:39], v[108:111]\n"
    "ds_read_b128 v[232:235], v245 offset:320\n"
    "v_cvt_pk_bf16_f32 v150, v136, v137\n"
    "v_mfma_f32_16x16x32_bf16 v[112:115], v[236:239], v[40:43], v[112:115]\n"
    "v_exp_f32_e64 v138, v138\n"
    "v_mfma_f32_16x16x32_bf16 v[116:119], v[236:239], v[44:47], v[116:119]\n"
    "s_nop 7\n"
    "s_waitcnt lgkmcnt(2)\n"
    "v_exp_f32_e64 v139, v139\n"
    "v_mfma_f32_16x16x32_bf16 v[96:99], v[224:227], v[48:51], v[96:99]\n"
    "ds_read_b128 v[236:239], v245 offset:832\n"
    "v_cvt_pk_bf16_f32 v151, v138, v139\n"
    "v_mfma_f32_16x16x32_bf16 v[100:103], v[224:227], v[52:55], v[100:103]\n"
    "v_exp_f32_e64 v128, v128\n"
    "v_mfma_f32_16x16x32_bf16 v[104:107], v[224:227], v[56:59], v[104:107]\n"
    "s_waitcnt lgkmcnt(2)\n"
    "v_exp_f32_e64 v129, v129\n"
    "v_mfma_f32_16x16x32_bf16 v[108:111], v[228:231], v[48:51], v[108:111]\n"
    "ds_read_b128 v[224:227], v245 offset:384\n"
    "v_cvt_pk_bf16_f32 v152, v128, v129\n"
    "v_mfma_f32_16x16x32_bf16 v[112:115], v[228:231], v[52:55], v[112:115]\n"
    "v_exp_f32_e64 v130, v130\n"
    "v_mfma_f32_16x16x32_bf16 v[116:119], v[228:231], v[56:59], v[116:119]\n"
    "s_nop 7\n"
    "s_waitcnt lgkmcnt(2)\n"
    "v_exp_f32_e64 v131, v131\n"
    "v_mfma_f32_16x16x32_bf16 v[96:99], v[232:235], v[60:63], v[96:99]\n"
    "ds_read_b128 v[228:231], v245 offset:896\n"
    "v_cvt_pk_bf16_f32 v153, v130, v131\n"
    "v_mfma_f32_16x16x32_bf16 v[100:103], v[232:235], v[64:67], v[100:103]\n"
    "v_exp_f32_e64 v140, v140\n"
    "v_mfma_f32_16x16x32_bf16 v[104:107], v[232:235], v[68:71], v[104:107]\n"
    "s_waitcnt lgkmcnt(2)\n"
    "v_exp_f32_e64 v141, v141\n"
    "v_mfma_f32_16x16x32_bf16 v[108:111], v[236:239], v[60:63], v[108:111]\n"
    "ds_read_b128 v[232:235], v245 offset:448\n"
    "v_cvt_pk_bf16_f32 v154, v140, v141\n"
    "v_mfma_f32_16x16x32_bf16 v[112:115], v[236:239], v[64:67], v[112:115]\n"
    "v_exp_f32_e64 v142, v142\n"
    "v_mfma_f32_16x16x32_bf16 v[116:119], v[236:239], v[68:71], v[116:119]\n"
    "s_nop 7\n"
    "s_waitcnt lgkmcnt(2)\n"
    "v_exp_f32_e64 v143, v143\n"
    "v_mfma_f32_16x16x32_bf16 v[96:99], v[224:227], v[72:75], v[96:99]\n"
    "ds_read_b128 v[236:239], v245 offset:960\n"
    "v_cvt_pk_bf16_f32 v155, v142, v143\n"
    "v_mfma_f32_16x16x32_bf16 v[100:103], v[224:227], v[76:79], v[100:103]\n"
    "s_waitcnt vmcnt(16)\n"
    "v_add_u32_e64 v246, s52, v255\n"
    "v_mfma_f32_16x16x32_bf16 v[104:107], v[224:227], v[80:83], v[104:107]\n"
    "s_waitcnt lgkmcnt(2)\n"
    "v_mfma_f32_16x16x32_bf16 v[108:111], v[228:231], v[72:75], v[108:111]\n"
    "s_barrier\n"
    "ds_read_b64_tr_b16 v[224:225], v246 offset:0\n"
    "ds_read_b64_tr_b16 v[226:227], v246 offset:512\n"
    "v_mfma_f32_16x16x32_bf16 v[112:115], v[228:231], v[76:79], v[112:115]\n"
    "s_add_u32 s52, s52, 16896\n"
    "v_mfma_f32_16x16x32_bf16 v[116:119], v[228:231], v[80:83], v[116:119]\n"
    "s_nop 7\n"
    "s_waitcnt lgkmcnt(3)\n"
    "v_mfma_f32_16x16x32_bf16 v[96:99], v[232:235], v[84:87], v[96:99]\n"
    "s_cmp_eq_u32 s52, s53\n"
    "ds_read_b64_tr_b16 v[228:229], v246 offset:32\n"
    "ds_read_b64_tr_b16 v[230:231], v246 offset:544\n"
    "v_mfma_f32_16x16x32_bf16 v[100:103], v[232:235], v[88:91], v[100:103]\n"
    "s_cselect_b32 s52, s79, s52\n"
    "v_mfma_f32_16x16x32_bf16 v[104:107], v[232:235], v[92:95], v[104:107]\n"
    "s_waitcnt lgkmcnt(4)\n"
    "v_mfma_f32_16x16x32_bf16 v[108:111], v[236:239], v[84:87], v[108:111]\n"
    "ds_read_b64_tr_b16 v[232:233], v246 offset:64\n"
    "ds_read_b64_tr_b16 v[234:235], v246 offset:576\n"
    "v_mfma_f32_16x16x32_bf16 v[112:115], v[236:239], v[88:91], v[112:115]\n"
    "v_mfma_f32_16x16x32_bf16 v[116:119], v[236:239], v[92:95], v[116:119]\n"
    "s_nop 7\n"
    "s_waitcnt lgkmcnt(4)\n"
    "v_mfma_f32_16x16x32_bf16 a[0:3], v[224:227], v[144:147], a[0:3]\n"
    "s_mov_b32 m0, s49\n"
    "ds_read_b64_tr_b16 v[236:237], v246 offset:96\n"
    "ds_read_b64_tr_b16 v[238:239], v246 offset:608\n"
    "v_mfma_f32_16x16x32_bf16 a[4:7], v[224:227], v[148:151], a[4:7]\n"
    "v_add_f32_e64 v168, v168, v120\n"
    "v_add_f32_e64 v168, v168, v121\n"
    "v_mfma_f32_16x16x32_bf16 a[8:11], v[224:227], v[152:155], a[8:11]\n"
    "s_waitcnt lgkmcnt(4)\n"
    "v_add_f32_e64 v168, v168, v122\n"
    "v_add_f32_e64 v168, v168, v123\n"
    "v_mfma_f32_16x16x32_bf16 a[12:15], v[228:231], v[144:147], a[12:15]\n"
    "ds_read_b64_tr_b16 v[224:225], v246 offset:128\n"
    "ds_read_b64_tr_b16 v[226:227], v246 offset:640\n"
    "v_mfma_f32_16x16x32_bf16 a[16:19], v[228:231], v[148:151], a[16:19]\n"
    "buffer_load_dwordx4 v241, s[32:35], s56 offen offset:0 nt lds\n"
    "v_mfma_f32_16x16x32_bf16 a[20:23], v[228:231], v[152:155], a[20:23]\n"
    "s_nop 7\n"
    "s_waitcnt lgkmcnt(4)\n"
    "v_max3_f32 v171, v96, v97, v98\n"
    "v_max3_f32 v171, v171, v99, v100\n"
    "v_mfma_f32_16x16x32_bf16 a[24:27], v[232:235], v[144:147], a[24:27]\n"
    "s_add_u32 m0, s49, 544\n"
    "ds_read_b64_tr_b16 v[228:229], v246 offset:160\n"
    "ds_read_b64_tr_b16 v[230:231], v246 offset:672\n"
    "v_mfma_f32_16x16x32_bf16 a[28:31], v[232:235], v[148:151], a[28:31]\n"
    "v_max3_f32 v171, v171, v101, v102\n"
    "v_max3_f32 v171, v171, v103, v104\n"
    "v_mfma_f32_16x16x32_bf16 a[32:35], v[232:235], v[152:155], a[32:35]\n"
    "s_waitcnt lgkmcnt(4)\n"
    "buffer_load_dwordx4 v241, s[32:35], s56 offen offset:512 nt lds\n"
    "v_mfma_f32_16x16x32_bf16 a[36:39], v[236:239], v[144:147], a[36:39]\n"
    "s_add_u32 m0, s49, 1088\n"
    "ds_read_b64_tr_b16 v[232:233], v246 offset:192\n"
    "ds_read_b64_tr_b16 v[234:235], v246 offset:704\n"
    "v_mfma_f32_16x16x32_bf16 a[40:43], v[236:239], v[148:151], a[40:43]\n"
    "v_max3_f32 v171, v171, v105, v106\n"
    "v_max3_f32 v171, v171, v107, v108\n"
    "v_mfma_f32_16x16x32_bf16 a[44:47], v[236:239], v[152:155], a[44:47]\n"
    "s_nop 7\n"
    "s_waitcnt lgkmcnt(4)\n"
    "v_max3_f32 v171, v171, v109, v110\n"
    "v_max3_f32 v171, v171, v111, v112\n"
    "v_mfma_f32_16x16x32_bf16 a[48:51], v[224:227], v[144:147], a[48:51]\n"
    "ds_read_b64_tr_b16 v[236:237], v246 offset:224\n"
    "ds_read_b64_tr_b16 v[238:239], v246 offset:736\n"
    "v_mfma_f32_16x16x32_bf16 a[52:55], v[224:227], v[148:151], a[52:55]\n"
    "buffer_load_dwordx4 v241, s[32:35], s56 offen offset:1024 nt lds\n"
    "v_mfma_f32_16x16x32_bf16 a[56:59], v[224:227], v[152:155], a[56:59]\n"
    "s_waitcnt lgkmcnt(4)\n"
    "v_max3_f32 v171, v171, v113, v114\n"
    "v_max3_f32 v171, v171, v115, v116\n"
    "v_mfma_f32_16x16x32_bf16 a[60:63], v[228:231], v[144:147], a[60:63]\n"
    "s_add_u32 m0, s49, 1632\n"
    "ds_read_b64_tr_b16 v[224:225], v246 offset:256\n"
    "ds_read_b64_tr_b16 v[226:227], v246 offset:768\n"
    "v_mfma_f32_16x16x32_bf16 a[64:67], v[228:231], v[148:151], a[64:67]\n"
    "v_max3_f32 v171, v171, v117, v118\n"
    "v_max_f32_e64 v171, v171, v119\n"
    "v_mfma_f32_16x16x32_bf16 a[68:71], v[228:231], v[152:155], a[68:71]\n"
    "s_nop 7\n"
    "s_waitcnt lgkmcnt(4)\n"
    "buffer_load_dwordx4 v241, s[32:35], s56 offen offset:1536 nt lds\n"
    "v_mfma_f32_16x16x32_bf16 a[72:75], v[232:235], v[144:147], a[72:75]\n"
    "s_add_u32 s56, s56, 16384\n"
    "ds_read_b64_tr_b16 v[228:229], v246 offset:288\n"
    "ds_read_b64_tr_b16 v[230:231], v246 offset:800\n"
    "v_mfma_f32_16x16x32_bf16 a[76:79], v[232:235], v[148:151], a[76:79]\n"
    "s_and_b32 s56, s56, 262143\n"
    "v_cmp_lt_f32 vcc, 0x41000000, v171\n"
    "v_add_f32_e64 v168, v168, v132\n"
    "v_mfma_f32_16x16x32_bf16 a[80:83], v[232:235], v[152:155], a[80:83]\n"
    "s_waitcnt lgkmcnt(4)\n"
    "v_add_f32_e64 v168, v168, v133\n"
    "v_add_f32_e64 v168, v168, v134\n"
    "v_mfma_f32_16x16x32_bf16 a[84:87], v[236:239], v[144:147], a[84:87]\n"
    "s_add_u32 s49, s49, 16896\n"
    "s_cmp_eq_u32 s49, s50\n"
    "s_cselect_b32 s49, s51, s49\n"
    "s_mov_b32 m0, s49\n"
    "ds_read_b64_tr_b16 v[232:233], v246 offset:320\n"
    "ds_read_b64_tr_b16 v[234:235], v246 offset:832\n"
    "v_mfma_f32_16x16x32_bf16 a[88:91], v[236:239], v[148:151], a[88:91]\n"
    "buffer_load_dwordx4 v241, s[32:35], s56 offen offset:0 nt lds\n"
    "v_mfma_f32_16x16x32_bf16 a[92:95], v[236:239], v[152:155], a[92:95]\n"
    "s_nop 7\n"
    "s_waitcnt lgkmcnt(4)\n"
    "v_add_f32_e64 v168, v168, v135\n"
    "v_add_f32_e64 v169, v169, v124\n"
    "v_mfma_f32_16x16x32_bf16 a[96:99], v[224:227], v[144:147], a[96:99]\n"
    "s_add_u32 m0, s49, 544\n"
    "ds_read_b64_tr_b16 v[236:237], v246 offset:352\n"
    "ds_read_b64_tr_b16 v[238:239], v246 offset:864\n"
    "v_mfma_f32_16x16x32_bf16 a[100:103], v[224:227], v[148:151], a[100:103]\n"
    "v_add_f32_e64 v169, v169, v125\n"
    "v_add_f32_e64 v169, v169, v126\n"
    "v_mfma_f32_16x16x32_bf16 a[104:107], v[224:227], v[152:155], a[104:107]\n"
    "s_waitcnt lgkmcnt(4)\n"
    "buffer_load_dwordx4 v241, s[32:35], s56 offen offset:512 nt lds\n"
    "v_mfma_f32_16x16x32_bf16 a[108:111], v[228:231], v[144:147], a[108:111]\n"
    "s_add_u32 m0, s49, 1088\n"
    "ds_read_b64_tr_b16 v[224:225], v246 offset:384\n"
    "ds_read_b64_tr_b16 v[226:227], v246 offset:896\n"
    "v_mfma_f32_16x16x32_bf16 a[112:115], v[228:231], v[148:151], a[112:115]\n"
    "v_add_f32_e64 v169, v169, v127\n"
    "v_add_f32_e64 v169, v169, v136\n"
    "v_mfma_f32_16x16x32_bf16 a[116:119], v[228:231], v[152:155], a[116:119]\n"
    "s_nop 7\n"
    "s_waitcnt lgkmcnt(4)\n"
    "v_add_f32_e64 v169, v169, v137\n"
    "v_add_f32_e64 v169, v169, v138\n"
    "v_mfma_f32_16x16x32_bf16 a[120:123], v[232:235], v[144:147], a[120:123]\n"
    "ds_read_b64_tr_b16 v[228:229], v246 offset:416\n"
    "ds_read_b64_tr_b16 v[230:231], v246 offset:928\n"
    "v_mfma_f32_16x16x32_bf16 a[124:127], v[232:235], v[148:151], a[124:127]\n"
    "buffer_load_dwordx4 v241, s[32:35], s56 offen offset:1024 nt lds\n"
    "v_mfma_f32_16x16x32_bf16 a[128:131], v[232:235], v[152:155], a[128:131]\n"
    "s_waitcnt lgkmcnt(4)\n"
    "v_add_f32_e64 v169, v169, v139\n"
    "v_add_f32_e64 v170, v170, v128\n"
    "v_mfma_f32_16x16x32_bf16 a[132:135], v[236:239], v[144:147], a[132:135]\n"
    "s_add_u32 m0, s49, 1632\n"
    "ds_read_b64_tr_b16 v[232:233], v246 offset:448\n"
    "ds_read_b64_tr_b16 v[234:235], v246 offset:960\n"
    "v_mfma_f32_16x16x32_bf16 a[136:139], v[236:239], v[148:151], a[136:139]\n"
    "v_add_f32_e64 v170, v170, v129\n"
    "v_add_f32_e64 v170, v170, v130\n"
    "v_mfma_f32_16x16x32_bf16 a[140:143], v[236:239], v[152:155], a[140:143]\n"
    "s_nop 7\n"
    "s_waitcnt lgkmcnt(4)\n"
    "buffer_load_dwordx4 v241, s[32:35], s56 offen offset:1536 nt lds\n"
    "v_mfma_f32_16x16x32_bf16 a[144:147], v[224:227], v[144:147], a[144:147]\n"
    "s_add_u32 s56, s56, 16384\n"
    "ds_read_b64_tr_b16 v[236:237], v246 offset:480\n"
    "ds_read_b64_tr_b16 v[238:239], v246 offset:992\n"
    "v_mfma_f32_16x16x32_bf16 a[148:151], v[224:227], v[148:151], a[148:151]\n"
    "s_and_b32 s56, s56, 262143\n"
    "v_add_u32_e64 v245, s52, v254\n"
    "v_add_f32_e64 v170, v170, v131\n"
    "v_mfma_f32_16x16x32_bf16 a[152:155], v[224:227], v[152:155], a[152:155]\n"
    "s_waitcnt lgkmcnt(4)\n"
    "v_add_f32_e64 v170, v170, v140\n"
    "v_add_f32_e64 v170, v170, v141\n"
    "v_mfma_f32_16x16x32_bf16 a[156:159], v[228:231], v[144:147], a[156:159]\n"
    "s_add_u32 s52, s52, 16896\n"
    "ds_read_b128 v[224:227], v245 offset:0\n"
    "v_add_f32_e64 v170, v170, v142\n"
    "v_mfma_f32_16x16x32_bf16 a[160:163], v[228:231], v[148:151], a[160:163]\n"
    "s_cmp_eq_u32 s52, s53\n"
    "v_add_f32_e64 v170, v170, v143\n"
    "v_mfma_f32_16x16x32_bf16 a[164:167], v[228:231], v[152:155], a[164:167]\n"
    "s_nop 7\n"
    "s_waitcnt lgkmcnt(3)\n"
    "v_mfma_f32_16x16x32_bf16 a[168:171], v[232:235], v[144:147], a[168:171]\n"
    "s_cselect_b32 s52, s79, s52\n"
    "ds_read_b128 v[228:231], v245 offset:512\n"
    "v_mfma_f32_16x16x32_bf16 a[172:175], v[232:235], v[148:151], a[172:175]\n"
    "s_add_u32 s49, s49, 16896\n"
    "v_mfma_f32_16x16x32_bf16 a[176:179], v[232:235], v[152:155], a[176:179]\n"
    "s_waitcnt lgkmcnt(2)\n"
    "v_mfma_f32_16x16x32_bf16 a[180:183], v[236:239], v[144:147], a[180:183]\n"
    "s_cmp_eq_u32 s49, s50\n"
    "ds_read_b128 v[232:235], v245 offset:64\n"
    "v_mfma_f32_16x16x32_bf16 a[184:187], v[236:239], v[148:151], a[184:187]\n"
    "s_cselect_b32 s49, s51, s49\n"
    "v_mfma_f32_16x16x32_bf16 a[188:191], v[236:239], v[152:155], a[188:191]\n"
    "s_nop 7\n"
    "s_waitcnt lgkmcnt(2)\n"
    "s_cbranch_vccnz L_stub3_%=\n"
    "L_ret3_%=:\n"
    "v_mfma_f32_16x16x32_bf16 v[120:123], v[224:227], v[0:3], v[156:159]\n"
    "ds_read_b128 v[236:239], v245 offset:576\n"
    "v_mfma_f32_16x16x32_bf16 v[124:127], v[224:227], v[4:7], v[160:163]\n"
    "buffer_load_dword v183, v182, s[16:19], s1 offen offset:0\n"
    "v_mfma_f32_16x16x32_bf16 v[128:131], v[224:227], v[8:11], v[164:167]\n"
    "s_waitcnt lgkmcnt(2)\n"
    "v_exp_f32_e64 v96, v96\n"
    "v_mfma_f32_16x16x32_bf16 v[132:135], v[228:231], v[0:3], v[156:159]\n"
    "ds_read_b128 v[224:227], v245 offset:128\n"
    "v_mfma_f32_16x16x32_bf16 v[136:139], v[228:231], v[4:7], v[160:163]\n"
    "v_exp_f32_e64 v97, v97\n"
    "v_mfma_f32_16x16x32_bf16 v[140:143], v[228:231], v[8:11], v[164:167]\n"
    "s_waitcnt lgkmcnt(2)\n"
    "v_exp_f32_e64 v98, v98\n"
    "v_mfma_f32_16x16x32_bf16 v[120:123], v[232:235], v[12:15], v[120:123]\n"
    "ds_read_b128 v[228:231], v245 offset:640\n"
    "v_cvt_pk_bf16_f32 v144, v96, v97\n"
    "v_mfma_f32_16x16x32_bf16 v[124:127], v[232:235], v[16:19], v[124:127]\n"
    "v_exp_f32_e64 v99, v99\n"
    "v_mfma_f32_16x16x32_bf16 v[128:131], v[232:235], v[20:23], v[128:131]\n"
    "s_waitcnt lgkmcnt(2)\n"
    "v_exp_f32_e64 v108, v108\n"
    "v_mfma_f32_16x16x32_bf16 v[132:135], v[236:239], v[12:15], v[132:135]\n"
    "ds_read_b128 v[232:235], v245 offset:192\n"
    "v_cvt_pk_bf16_f32 v145, v98, v99\n"
    "v_mfma_f32_16x16x32_bf16 v[136:139], v[236:239], v[16:19], v[136:139]\n"
    "v_exp_f32_e64 v109, v109\n"
    "v_mfma_f32_16x16x32_bf16 v[140:143], v[236:239], v[20:23], v[140:143]\n"
    "s_waitcnt lgkmcnt(2)\n"
    "v_exp_f32_e64 v110, v110\n"
    "v_mfma_f32_16x16x32_bf16 v[120:123], v[224:227], v[24:27], v[120:123]\n"
    "ds_read_b128 v[236:239], v245 offset:704\n"
    "v_cvt_pk_bf16_f32 v146, v108, v109\n"
    "v_mfma_f32_16x16x32_bf16 v[124:127], v[224:227], v[28:31], v[124:127]\n"
    "v_exp_f32_e64 v111, v111\n"
    "v_mfma_f32_16x16x32_bf16 v[128:131], v[224:227], v[32:35], v[128:131]\n"
    "s_waitcnt lgkmcnt(2)\n"
    "v_exp_f32_e64 v100, v100\n"
    "v_mfma_f32_16x16x32_bf16 v[132:135], v[228:231], v[24:27], v[132:135]\n"
    "ds_read_b128 v[224:227], v245 offset:256\n"
    "v_cvt_pk_bf16_f32 v147, v110, v111\n"
    "v_mfma_f32_16x16x32_bf16 v[136:139], v[228:231], v[28:31], v[136:139]\n"
    "v_exp_f32_e64 v101, v101\n"
    "v_mfma_f32_16x16x32_bf16 v[140:143], v[228:231], v[32:35], v[140:143]\n"
    "s_waitcnt lgkmcnt(2)\n"
    "v_exp_f32_e64 v102, v102\n"
    "v_mfma_f32_16x16x32_bf16 v[120:123], v[232:235], v[36:39], v[120:123]\n"
    "ds_read_b128 v[228:231], v245 offset:768\n"
    "v_cvt_pk_bf16_f32 v148, v100, v101\n"
    "v_mfma_f32_16x16x32_bf16 v[124:127], v[232:235], v[40:43], v[124:127]\n"
    "v_exp_f32_e64 v103, v103\n"
    "v_mfma_f32_16x16x32_bf16 v[128:131], v[232:235], v[44:47], v[128:131]\n"
    "s_waitcnt lgkmcnt(2)\n"
    "v_exp_f32_e64 v112, v112\n"
    "v_mfma_f32_16x16x32_bf16 v[132:135], v[236:239], v[36:39], v[132:135]\n"
    "ds_read_b128 v[232:235], v245 offset:320\n"
    "v_cvt_pk_bf16_f32 v149, v102, v103\n"
    "v_mfma_f32_16x16x32_bf16 v[136:139], v[236:239], v[40:43], v[136:139]\n"
    "v_exp_f32_e64 v113, v113\n"
    "v_mfma_f32_16x16x32_bf16 v[140:143], v[236:239], v[44:47], v[140:143]\n"
    "s_waitcnt lgkmcnt(2)\n"
    "v_exp_f32_e64 v114, v114\n"
    "v_mfma_f32_16x16x32_bf16 v[120:123], v[224:227], v[48:51], v[120:123]\n"
    "ds_read_b128 v[236:239], v245 offset:832\n"
    "v_cvt_pk_bf16_f32 v150, v112, v113\n"
    "v_mfma_f32_16x16x32_bf16 v[124:127], v[224:227], v[52:55], v[124:127]\n"
    "v_exp_f32_e64 v115, v115\n"
    "v_mfma_f32_16x16x32_bf16 v[128:131], v[224:227], v[56:59], v[128:131]\n"
    "s_waitcnt lgkmcnt(2)\n"
    "v_exp_f32_e64 v104, v104\n"
    "v_mfma_f32_16x16x32_bf16 v[132:135], v[228:231], v[48:51], v[132:135]\n"
    "ds_read_b128 v[224:227], v245 offset:384\n"
    "v_cvt_pk_bf16_f32 v151, v114, v115\n"
    "v_mfma_f32_16x16x32_bf16 v[136:139], v[228:231], v[52:55], v[136:139]\n"
    "v_exp_f32_e64 v105, v105\n"
    "v_mfma_f32_16x16x32_bf16 v[140:143], v[228:231], v[56:59], v[140:143]\n"
    "s_waitcnt lgkmcnt(2)\n"
    "v_exp_f32_e64 v106, v106\n"
    "v_mfma_f32_16x16x32_bf16 v[120:123], v[232:235], v[60:63], v[120:123]\n"
    "ds_read_b128 v[228:231], v245 offset:896\n"
    "v_cvt_pk_bf16_f32 v152, v104, v105\n"
    "v_mfma_f32_16x16x32_bf16 v[124:127], v[232:235], v[64:67], v[124:127]\n"
    "v_exp_f32_e64 v107, v107\n"
    "v_mfma_f32_16x16x32_bf16 v[128:131], v[232:235], v[68:71], v[128:131]\n"
    "s_waitcnt lgkmcnt(2)\n"
    "v_exp_f32_e64 v116, v116\n"
    "v_mfma_f32_16x16x32_bf16 v[132:135], v[236:239], v[60:63], v[132:135]\n"
    "ds_read_b128 v[232:235], v245 offset:448\n"
    "v_cvt_pk_bf16_f32 v153, v106, v107\n"
    "v_mfma_f32_16x16x32_bf16 v[136:139], v[236:239], v[64:67], v[136:139]\n"
    "v_exp_f32_e64 v117, v117\n"
    "v_mfma_f32_16x16x32_bf16 v[140:143], v[236:239], v[68:71], v[140:143]\n"
    "s_waitcnt lgkmcnt(2)\n"
    "v_exp_f32_e64 v118, v118\n"
    "v_mfma_f32_16x16x32_bf16 v[120:123], v[224:227], v[72:75], v[120:123]\n"
    "ds_read_b128 v[236:239], v245 offset:960\n"
    "v_cvt_pk_bf16_f32 v154, v116, v117\n"
    "v_mfma_f32_16x16x32_bf16 v[124:127], v[224:227], v[76:79], v[124:127]\n"
    "s_waitcnt vmcnt(17)\n"
    "v_exp_f32_e64 v119, v119\n"
    "v_mfma_f32_16x16x32_bf16 v[128:131], v[224:227], v[80:83], v[128:131]\n"
    "s_waitcnt lgkmcnt(2)\n"
    "v_cvt_pk_bf16_f32 v155, v118, v119\n"
    "v_add_u32_e64 v246, s52, v255\n"
    "v_mfma_f32_16x16x32_bf16 v[132:135], v[228:231], v[72:75], v[132:135]\n"
    "s_barrier\n"
    "ds_read_b64_tr_b16 v[224:225], v246 offset:0\n"
    "ds_read_b64_tr_b16 v[226:227], v246 offset:512\n"
    "v_mfma_f32_16x16x32_bf16 v[136:139], v[228:231], v[76:79], v[136:139]\n"
    "s_add_u32 s52, s52, 16896\n"
    "v_mfma_f32_16x16x32_bf16 v[140:143], v[228:231], v[80:83], v[140:143]\n"
    "s_waitcnt lgkmcnt(3)\n"
    "v_mfma_f32_16x16x32_bf16 v[120:123], v[232:235], v[84:87], v[120:123]\n"
    "s_cmp_eq_u32 s52, s53\n"
    "ds_read_b64_tr_b16 v[228:229], v246 offset:32\n"
    "ds_read_b64_tr_b16 v[230:231], v246 offset:544\n"
    "v_mfma_f32_16x16x32_bf16 v[124:127], v[232:235], v[88:91], v[124:127]\n"
    "s_cselect_b32 s52, s79, s52\n"
    "v_mfma_f32_16x16x32_bf16 v[128:131], v[232:235], v[92:95], v[128:131]\n"
    "s_waitcnt lgkmcnt(4)\n"
    "v_mfma_f32_16x16x32_bf16 v[132:135], v[236:239], v[84:87], v[132:135]\n"
    "ds_read_b64_tr_b16 v[232:233], v246 offset:64\n"
    "ds_read_b64_tr_b16 v[234:235], v246 offset:576\n"
    "v_mfma_f32_16x16x32_bf16 v[136:139], v[236:239], v[88:91], v[136:139]\n"
    "v_mfma_f32_16x16x32_bf16 v[140:143], v[236:239], v[92:95], v[140:143]\n"
    "s_waitcnt lgkmcnt(4)\n"
    "v_mfma_f32_16x16x32_bf16 a[0:3], v[224:227], v[144:147], a[0:3]\n"
    "s_add_u32 m0, s49, s59\n"
    "ds_read_b64_tr_b16 v[236:237], v246 offset:96\n"
    "ds_read_b64_tr_b16 v[238:239], v246 offset:608\n"
    "v_mfma_f32_16x16x32_bf16 a[4:7], v[224:227], v[148:151], a[4:7]\n"
    "s_add_u32 s74, s57, s63\n"
    "v_add_f32_e64 v168, v168, v96\n"
    "v_add_f32_e64 v168, v168, v97\n"
    "v_mfma_f32_16x16x32_bf16 a[8:11], v[224:227], v[152:155], a[8:11]\n"
    "s_waitcnt lgkmcnt(4)\n"
    "v_add_f32_e64 v168, v168, v98\n"
    "v_add_f32_e64 v168, v168, v99\n"
    "v_mfma_f32_16x16x32_bf16 a[12:15], v[228:231], v[144:147], a[12:15]\n"
    "ds_read_b64_tr_b16 v[224:225], v246 offset:128\n"
    "ds_read_b64_tr_b16 v[226:227], v246 offset:640\n"
    "v_mfma_f32_16x16x32_bf16 a[16:19], v[228:231], v[148:151], a[16:19]\n"
    "buffer_load_dwordx4 v190, s[44:47], s74 offen offset:0 lds\n"
    "v_mfma_f32_16x16x32_bf16 a[20:23], v[228:231], v[152:155], a[20:23]\n"
    "s_waitcnt lgkmcnt(4)\n"
    "v_max3_f32 v171, v120, v121, v122\n"
    "v_max3_f32 v171, v171, v123, v124\n"
    "v_mfma_f32_16x16x32_bf16 a[24:27], v[232:235], v[144:147], a[24:27]\n"
    "s_add_u32 m0, s49, s60\n"
    "ds_read_b64_tr_b16 v[228:229], v246 offset:160\n"
    "ds_read_b64_tr_b16 v[230:231], v246 offset:672\n"
    "v_mfma_f32_16x16x32_bf16 a[28:31], v[232:235], v[148:151], a[28:31]\n"
    "s_add_u32 s75, s57, s64\n"
    "v_max3_f32 v171, v171, v125, v126\n"
    "v_max3_f32 v171, v171, v127, v128\n"
    "v_mfma_f32_16x16x32_bf16 a[32:35], v[232:235], v[152:155], a[32:35]\n"
    "s_waitcnt lgkmcnt(4)\n"
    "buffer_load_dwordx4 v190, s[44:47], s75 offen offset:0 lds\n"
    "v_mfma_f32_16x16x32_bf16 a[36:39], v[236:239], v[144:147], a[36:39]\n"
    "s_add_u32 m0, s49, s61\n"
    "ds_read_b64_tr_b16 v[232:233], v246 offset:192\n"
    "ds_read_b64_tr_b16 v[234:235], v246 offset:704\n"
    "v_mfma_f32_16x16x32_bf16 a[40:43], v[236:239], v[148:151], a[40:43]\n"
    "s_add_u32 s74, s57, s65\n"
    "v_max3_f32 v171, v171, v129, v130\n"
    "v_max3_f32 v171, v171, v131, v132\n"
    "v_mfma_f32_16x16x32_bf16 a[44:47], v[236:239], v[152:155], a[44:47]\n"
    "s_waitcnt lgkmcnt(4)\n"
    "v_max3_f32 v171, v171, v133, v134\n"
    "v_max3_f32 v171, v171, v135, v136\n"
    "v_mfma_f32_16x16x32_bf16 a[48:51], v[224:227], v[144:147], a[48:51]\n"
    "ds_read_b64_tr_b16 v[236:237], v246 offset:224\n"
    "ds_read_b64_tr_b16 v[238:239], v246 offset:736\n"
    "v_mfma_f32_16x16x32_bf16 a[52:55], v[224:227], v[148:151], a[52:55]\n"
    "buffer_load_dwordx4 v190, s[44:47], s74 offen offset:0 lds\n"
    "v_mfma_f32_16x16x32_bf16 a[56:59], v[224:227], v[152:155], a[56:59]\n"
    "s_waitcnt lgkmcnt(4)\n"
    "v_max3_f32 v171, v171, v137, v138\n"
    "v_max3_f32 v171, v171, v139, v140\n"
    "v_mfma_f32_16x16x32_bf16 a[60:63], v[228:231], v[144:147], a[60:63]\n"
    "s_add_u32 m0, s49, s62\n"
    "ds_read_b64_tr_b16 v[224:225], v246 offset:256\n"
    "ds_read_b64_tr_b16 v[226:227], v246 offset:768\n"
    "v_mfma_f32_16x16x32_bf16 a[64:67], v[228:231], v[148:151], a[64:67]\n"
    "s_add_u32 s75, s57, s66\n"
    "v_max3_f32 v171, v171, v141, v142\n"
    "v_max_f32_e64 v171, v171, v143\n"
    "v_mfma_f32_16x16x32_bf16 a[68:71], v[228:231], v[152:155], a[68:71]\n"
    "s_waitcnt lgkmcnt(4)\n"
    "buffer_load_dwordx4 v190, s[44:47], s75 offen offset:0 lds\n"
    "v_mfma_f32_16x16x32_bf16 a[72:75], v[232:235], v[144:147], a[72:75]\n"
    "s_add_u32 s57, s57, s58\n"
    "ds_read_b64_tr_b16 v[228:229], v246 offset:288\n"
    "ds_read_b64_tr_b16 v[230:231], v246 offset:800\n"
    "v_mfma_f32_16x16x32_bf16 a[76:79], v[232:235], v[148:151], a[76:79]\n"
    "s_add_u32 s49, s49, 16896\n"
    "s_cmp_eq_u32 s49, s50\n"
    "s_cselect_b32 s49, s51, s49\n"
    "v_cmp_lt_f32 vcc, 0x41000000, v171\n"
    "v_add_f32_e64 v168, v168, v108\n"
    "v_mfma_f32_16x16x32_bf16 a[80:83], v[232:235], v[152:155], a[80:83]\n"
    "s_waitcnt lgkmcnt(4)\n"
    "v_add_f32_e64 v168, v168, v109\n"
    "v_add_f32_e64 v168, v168, v110\n"
    "v_mfma_f32_16x16x32_bf16 a[84:87], v[236:239], v[144:147], a[84:87]\n"
    "s_add_u32 m0, s49, s90\n"
    "s_add_u32 s74, s88, s94\n"
    "ds_read_b64_tr_b16 v[232:233], v246 offset:320\n"
    "ds_read_b64_tr_b16 v[234:235], v246 offset:832\n"
    "v_mfma_f32_16x16x32_bf16 a[88:91], v[236:239], v[148:151], a[88:91]\n"
    "buffer_load_dwordx4 v191, s[84:87], s74 offen offset:0 lds\n"
    "v_mfma_f32_16x16x32_bf16 a[92:95], v[236:239], v[152:155], a[92:95]\n"
    "s_waitcnt lgkmcnt(4)\n"
    "v_add_f32_e64 v168, v168, v111\n"
    "v_add_f32_e64 v169, v169, v100\n"
    "v_mfma_f32_16x16x32_bf16 a[96:99], v[224:227], v[144:147], a[96:99]\n"
    "s_add_u32 m0, s49, s91\n"
    "ds_read_b64_tr_b16 v[236:237], v246 offset:352\n"
    "ds_read_b64_tr_b16 v[238:239], v246 offset:864\n"
    "v_mfma_f32_16x16x32_bf16 a[100:103], v[224:227], v[148:151], a[100:103]\n"
    "s_add_u32 s75, s88, s95\n"
    "v_add_f32_e64 v169, v169, v101\n"
    "v_add_f32_e64 v169, v169, v102\n"
    "v_mfma_f32_16x16x32_bf16 a[104:107], v[224:227], v[152:155], a[104:107]\n"
    "s_waitcnt lgkmcnt(4)\n"
    "buffer_load_dwordx4 v191, s[84:87], s75 offen offset:0 lds\n"
    "v_mfma_f32_16x16x32_bf16 a[108:111], v[228:231], v[144:147], a[108:111]\n"
    "s_add_u32 m0, s49, s92\n"
    "ds_read_b64_tr_b16 v[224:225], v246 offset:384\n"
    "ds_read_b64_tr_b16 v[226:227], v246 offset:896\n"
    "v_mfma_f32_16x16x32_bf16 a[112:115], v[228:231], v[148:151], a[112:115]\n"
    "s_add_u32 s74, s88, s96\n"
    "v_add_f32_e64 v169, v169, v103\n"
    "v_add_f32_e64 v169, v169, v112\n"
    "v_mfma_f32_16x16x32_bf16 a[116:119], v[228:231], v[152:155], a[116:119]\n"
    "s_waitcnt lgkmcnt(4)\n"
    "v_add_f32_e64 v169, v169, v113\n"
    "v_add_f32_e64 v169, v169, v114\n"
    "v_mfma_f32_16x16x32_bf16 a[120:123], v[232:235], v[144:147], a[120:123]\n"
    "ds_read_b64_tr_b16 v[228:229], v246 offset:416\n"
    "ds_read_b64_tr_b16 v[230:231], v246 offset:928\n"
    "v_mfma_f32_16x16x32_bf16 a[124:127], v[232:235], v[148:151], a[124:127]\n"
    "buffer_load_dwordx4 v191, s[84:87], s74 offen offset:0 lds\n"
    "v_mfma_f32_16x16x32_bf16 a[128:131], v[232:235], v[152:155], a[128:131]\n"
    "s_waitcnt lgkmcnt(4)\n"
    "v_add_f32_e64 v169, v169, v115\n"
    "v_add_f32_e64 v170, v170, v104\n"
    "v_mfma_f32_16x16x32_bf16 a[132:135], v[236:239], v[144:147], a[132:135]\n"
    "s_add_u32 m0, s49, s93\n"
    "ds_read_b64_tr_b16 v[232:233], v246 offset:448\n"
    "ds_read_b64_tr_b16 v[234:235], v246 offset:960\n"
    "v_mfma_f32_16x16x32_bf16 a[136:139], v[236:239], v[148:151], a[136:139]\n"
    "s_add_u32 s75, s88, s97\n"
    "v_add_f32_e64 v170, v170, v105\n"
    "v_add_f32_e64 v170, v170, v106\n"
    "v_mfma_f32_16x16x32_bf16 a[140:143], v[236:239], v[152:155], a[140:143]\n"
    "s_waitcnt lgkmcnt(4)\n"
    "buffer_load_dwordx4 v191, s[84:87], s75 offen offset:0 lds\n"
    "v_mfma_f32_16x16x32_bf16 a[144:147], v[224:227], v[144:147], a[144:147]\n"
    "s_add_u32 s88, s88, s89\n"
    "ds_read_b64_tr_b16 v[236:237], v246 offset:480\n"
    "ds_read_b64_tr_b16 v[238:239], v246 offset:992\n"
    "v_mfma_f32_16x16x32_bf16 a[148:151], v[224:227], v[148:151], a[148:151]\n"
    "s_add_u32 s49, s49, 16896\n"
    "v_add_u32_e64 v245, s52, v254\n"
    "v_add_f32_e64 v170, v170, v107\n"
    "v_mfma_f32_16x16x32_bf16 a[152:155], v[224:227], v[152:155], a[152:155]\n"
    "s_waitcnt lgkmcnt(4)\n"
    "v_add_f32_e64 v170, v170, v116\n"
    "v_add_f32_e64 v170, v170, v117\n"
    "v_mfma_f32_16x16x32_bf16 a[156:159], v[228:231], v[144:147], a[156:159]\n"
    "s_add_u32 s52, s52, 16896\n"
    "ds_read_b128 v[224:227], v245 offset:0\n"
    "v_add_f32_e64 v170, v170, v118\n"
    "v_mfma_f32_16x16x32_bf16 a[160:163], v[228:231], v[148:151], a[160:163]\n"
    "s_cmp_eq_u32 s52, s53\n"
    "v_add_f32_e64 v170, v170, v119\n"
    "v_mfma_f32_16x16x32_bf16 a[164:167], v[228:231], v[152:155], a[164:167]\n"
    "s_waitcnt lgkmcnt(3)\n"
    "v_mfma_f32_16x16x32_bf16 a[168:171], v[232:235], v[144:147], a[168:171]\n"
    "s_cselect_b32 s52, s79, s52\n"
    "ds_read_b128 v[228:231], v245 offset:512\n"
    "v_mfma_f32_16x16x32_bf16 a[172:175], v[232:235], v[148:151], a[172:175]\n"
    "s_cmp_eq_u32 s49, s50\n"
    "v_mfma_f32_16x16x32_bf16 a[176:179], v[232:235], v[152:155], a[176:179]\n"
    "s_waitcnt lgkmcnt(2)\n"
    "v_mfma_f32_16x16x32_bf16 a[180:183], v[236:239], v[144:147], a[180:183]\n"
    "s_cselect_b32 s49, s51, s49\n"
    "ds_read_b128 v[232:235], v245 offset:64\n"
    "v_mfma_f32_16x16x32_bf16 a[184:187], v[236:239], v[148:151], a[184:187]\n"
    "v_mfma_f32_16x16x32_bf16 a[188:191], v[236:239], v[152:155], a[188:191]\n"
    "s_waitcnt lgkmcnt(2)\n"
    "s_cbranch_vccnz L_stub4_%=\n"
    "L_ret4_%=:\n"
    "v_mfma_f32_16x16x32_bf16 v[96:99], v[224:227], v[0:3], v[156:159]\n"
    "ds_read_b128 v[236:239], v245 offset:576\n"
    "v_mfma_f32_16x16x32_bf16 v[100:103], v[224:227], v[4:7], v[160:163]\n"
    "buffer_load_dword v183, v182, s[16:19], s2 offen offset:0\n"
    "v_mfma_f32_16x16x32_bf16 v[104:107], v[224:227], v[8:11], v[164:167]\n"
    "s_waitcnt lgkmcnt(2)\n"
    "v_exp_f32_e64 v120, v120\n"
    "v_mfma_f32_16x16x32_bf16 v[108:111], v[228:231], v[0:3], v[156:159]\n"
    "ds_read_b128 v[224:227], v245 offset:128\n"
    "v_mfma_f32_16x16x32_bf16 v[112:115], v[228:231], v[4:7], v[160:163]\n"
    "v_exp_f32_e64 v121, v121\n"
    "v_mfma_f32_16x16x32_bf16 v[116:119], v[228:231], v[8:11], v[164:167]\n"
    "s_waitcnt lgkmcnt(2)\n"
    "v_exp_f32_e64 v122, v122\n"
    "v_mfma_f32_16x16x32_bf16 v[96:99], v[232:235], v[12:15], v[96:99]\n"
    "ds_read_b128 v[228:231], v245 offset:640\n"
    "v_cvt_pk_bf16_f32 v144, v120, v121\n"
    "v_mfma_f32_16x16x32_bf16 v[100:103], v[232:235], v[16:19], v[100:103]\n"
    "v_exp_f32_e64 v123, v123\n"
    "v_mfma_f32_16x16x32_bf16 v[104:107], v[232:235], v[20:23], v[104:107]\n"
    "s_waitcnt lgkmcnt(2)\n"
    "v_exp_f32_e64 v132, v132\n"
    "v_mfma_f32_16x16x32_bf16 v[108:111], v[236:239], v[12:15], v[108:111]\n"
    "ds_read_b128 v[232:235], v245 offset:192\n"
    "v_cvt_pk_bf16_f32 v145, v122, v123\n"
    "v_mfma_f32_16x16x32_bf16 v[112:115], v[236:239], v[16:19], v[112:115]\n"
    "v_exp_f32_e64 v133, v133\n"
    "v_mfma_f32_16x16x32_bf16 v[116:119], v[236:239], v[20:23], v[116:119]\n"
    "s_waitcnt lgkmcnt(2)\n"
    "v_exp_f32_e64 v134, v134\n"
    "v_mfma_f32_16x16x32_bf16 v[96:99], v[224:227], v[24:27], v[96:99]\n"
    "ds_read_b128 v[236:239], v245 offset:704\n"
    "v_cvt_pk_bf16_f32 v146, v132, v133\n"
    "v_mfma_f32_16x16x32_bf16 v[100:103], v[224:227], v[28:31], v[100:103]\n"
    "v_exp_f32_e64 v135, v135\n"
    "v_mfma_f32_16x16x32_bf16 v[104:107], v[224:227], v[32:35], v[104:107]\n"
    "s_waitcnt lgkmcnt(2)\n"
    "v_exp_f32_e64 v124, v124\n"
    "v_mfma_f32_16x16x32_bf16 v[108:111], v[228:231], v[24:27], v[108:111]\n"
    "ds_read_b128 v[224:227], v245 offset:256\n"
    "v_cvt_pk_bf16_f32 v147, v134, v135\n"
    "v_mfma_f32_16x16x32_bf16 v[112:115], v[228:231], v[28:31], v[112:115]\n"
    "v_exp_f32_e64 v125, v125\n"
    "v_mfma_f32_16x16x32_bf16 v[116:119], v[228:231], v[32:35], v[116:119]\n"
    "s_waitcnt lgkmcnt(2)\n"
    "v_exp_f32_e64 v126, v126\n"
    "v_mfma_f32_16x16x32_bf16 v[96:99], v[232:235], v[36:39], v[96:99]\n"
    "ds_read_b128 v[228:231], v245 offset:768\n"
    "v_cvt_pk_bf16_f32 v148, v124, v125\n"
    "v_mfma_f32_16x16x32_bf16 v[100:103], v[232:235], v[40:43], v[100:103]\n"
    "v_exp_f32_e64 v127, v127\n"
    "v_mfma_f32_16x16x32_bf16 v[104:107], v[232:235], v[44:47], v[104:107]\n"
    "s_waitcnt lgkmcnt(2)\n"
    "v_exp_f32_e64 v136, v136\n"
    "v_mfma_f32_16x16x32_bf16 v[108:111], v[236:239], v[36:39], v[108:111]\n"
    "ds_read_b128 v[232:235], v245 offset:320\n"
    "v_cvt_pk_bf16_f32 v149, v126, v127\n"
    "v_mfma_f32_16x16x32_bf16 v[112:115], v[236:239], v[40:43], v[112:115]\n"
    "v_exp_f32_e64 v137, v137\n"
    "v_mfma_f32_16x16x32_bf16 v[116:119], v[236:239], v[44:47], v[116:119]\n"
    "s_waitcnt lgkmcnt(2)\n"
    "v_exp_f32_e64 v138, v138\n"
    "v_mfma_f32_16x16x32_bf16 v[96:99], v[224:227], v[48:51], v[96:99]\n"
    "ds_read_b128 v[236:239], v245 offset:832\n"
    "v_cvt_pk_bf16_f32 v150, v136, v137\n"
    "v_mfma_f32_16x16x32_bf16 v[100:103], v[224:227], v[52:55], v[100:103]\n"
    "v_exp_f32_e64 v139, v139\n"
    "v_mfma_f32_16x16x32_bf16 v[104:107], v[224:227], v[56:59], v[104:107]\n"
    "s_waitcnt lgkmcnt(2)\n"
    "v_exp_f32_e64 v128, v128\n"
    "v_mfma_f32_16x16x32_bf16 v[108:111], v[228:231], v[48:51], v[108:111]\n"
    "ds_read_b128 v[224:227], v245 offset:384\n"
    "v_cvt_pk_bf16_f32 v151, v138, v139\n"
    "v_mfma_f32_16x16x32_bf16 v[112:115], v[228:231], v[52:55], v[112:115]\n"
    "v_exp_f32_e64 v129, v129\n"
    "v_mfma_f32_16x16x32_bf16 v[116:119], v[228:231], v[56:59], v[116:119]\n"
    "s_waitcnt lgkmcnt(2)\n"
    "v_exp_f32_e64 v130, v130\n"
    "v_mfma_f32_16x16x32_bf16 v[96:99], v[232:235], v[60:63], v[96:99]\n"
    "ds_read_b128 v[228:231], v245 offset:896\n"
    "v_cvt_pk_bf16_f32 v152, v128, v129\n"
    "v_mfma_f32_16x16x32_bf16 v[100:103], v[232:235], v[64:67], v[100:103]\n"
    "v_exp_f32_e64 v131, v131\n"
    "v_mfma_f32_16x16x32_bf16 v[104:107], v[232:235], v[68:71], v[104:107]\n"
    "s_waitcnt lgkmcnt(2)\n"
    "v_exp_f32_e64 v140, v140\n"
    "v_mfma_f32_16x16x32_bf16 v[108:111], v[236:239], v[60:63], v[108:111]\n"
    "ds_read_b128 v[232:235], v245 offset:448\n"
    "v_cvt_pk_bf16_f32 v153, v130, v131\n"
    "v_mfma_f32_16x16x32_bf16 v[112:115], v[236:239], v[64:67], v[112:115]\n"
    "v_exp_f32_e64 v141, v141\n"
    "v_mfma_f32_16x16x32_bf16 v[116:119], v[236:239], v[68:71], v[116:119]\n"
    "s_waitcnt lgkmcnt(2)\n"
    "v_exp_f32_e64 v142, v142\n"
    "v_mfma_f32_16x16x32_bf16 v[96:99], v[224:227], v[72:75], v[96:99]\n"
    "ds_read_b128 v[236:239], v245 offset:960\n"
    "v_cvt_pk_bf16_f32 v154, v140, v141\n"
    "v_mfma_f32_16x16x32_bf16 v[100:103], v[224:227], v[76:79], v[100:103]\n"
    "s_waitcnt vmcnt(18)\n"
    "v_exp_f32_e64 v143, v143\n"
    "v_mfma_f32_16x16x32_bf16 v[104:107], v[224:227], v[80:83], v[104:107]\n"
    "s_waitcnt lgkmcnt(2)\n"
    "v_cvt_pk_bf16_f32 v155, v142, v143\n"
    "v_add_u32_e64 v246, s52, v255\n"
    "v_mfma_f32_16x16x32_bf16 v[108:111], v[228:231], v[72:75], v[108:111]\n"
    "s_barrier\n"
    "ds_read_b64_tr_b16 v[224:225], v246 offset:0\n"
    "ds_read_b64_tr_b16 v[226:227], v246 offset:512\n"
    "v_mfma_f32_16x16x32_bf16 v[112:115], v[228:231], v[76:79], v[112:115]\n"
    "s_add_u32 s52, s52, 16896\n"
    "v_mfma_f32_16x16x32_bf16 v[116:119], v[228:231], v[80:83], v[116:119]\n"
    "s_waitcnt lgkmcnt(3)\n"
    "v_mfma_f32_16x16x32_bf16 v[96:99], v[232:235], v[84:87], v[96:99]\n"
    "s_cmp_eq_u32 s52, s53\n"
    "ds_read_b64_tr_b16 v[228:229], v246 offset:32\n"
    "ds_read_b64_tr_b16 v[230:231], v246 offset:544\n"
    "v_mfma_f32_16x16x32_bf16 v[100:103], v[232:235], v[88:91], v[100:103]\n"
    "s_cselect_b32 s52, s79, s52\n"
    "v_mfma_f32_16x16x32_bf16 v[104:107], v[232:235], v[92:95], v[104:107]\n"
    "s_waitcnt lgkmcnt(4)\n"
    "v_mfma_f32_16x16x32_bf16 v[108:111], v[236:239], v[84:87], v[108:111]\n"
    "ds_read_b64_tr_b16 v[232:233], v246 offset:64\n"
    "ds_read_b64_tr_b16 v[234:235], v246 offset:576\n"
    "v_mfma_f32_16x16x32_bf16 v[112:115], v[236:239], v[88:91], v[112:115]\n"
    "v_mfma_f32_16x16x32_bf16 v[116:119], v[236:239], v[92:95], v[116:119]\n"
    "s_waitcnt lgkmcnt(4)\n"
    "v_mfma_f32_16x16x32_bf16 a[0:3], v[224:227], v[144:147], a[0:3]\n"
    "s_add_u32 m0, s49, s59\n"
    "ds_read_b64_tr_b16 v[236:237], v246 offset:96\n"
    "ds_read_b64_tr_b16 v[238:239], v246 offset:608\n"
    "v_mfma_f32_16x16x32_bf16 a[4:7], v[224:227], v[148:151], a[4:7]\n"
    "s_add_u32 s74, s57, s63\n"
    "v_add_f32_e64 v168, v168, v120\n"
    "v_add_f32_e64 v168, v168, v121\n"
    "v_mfma_f32_16x16x32_bf16 a[8:11], v[224:227], v[152:155], a[8:11]\n"
    "s_waitcnt lgkmcnt(4)\n"
    "v_add_f32_e64 v168, v168, v122\n"
    "v_add_f32_e64 v168, v168, v123\n"
    "v_mfma_f32_16x16x32_bf16 a[12:15], v[228:231], v[144:147], a[12:15]\n"
    "ds_read_b64_tr_b16 v[224:225], v246 offset:128\n"
    "ds_read_b64_tr_b16 v[226:227], v246 offset:640\n"
    "v_mfma_f32_16x16x32_bf16 a[16:19], v[228:231], v[148:151], a[16:19]\n"
    "buffer_load_dwordx4 v190, s[44:47], s74 offen offset:0 lds\n"
    "v_mfma_f32_16x16x32_bf16 a[20:23], v[228:231], v[152:155], a[20:23]\n"
    "s_waitcnt lgkmcnt(4)\n"
    "v_max3_f32 v171, v96, v97, v98\n"
    "v_max3_f32 v171, v171, v99, v100\n"
    "v_mfma_f32_16x16x32_bf16 a[24:27], v[232:235], v[144:147], a[24:27]\n"
    "s_add_u32 m0, s49, s60\n"
    "ds_read_b64_tr_b16 v[228:229], v246 offset:160\n"
    "ds_read_b64_tr_b16 v[230:231], v246 offset:672\n"
    "v_mfma_f32_16x16x32_bf16 a[28:31], v[232:235], v[148:151], a[28:31]\n"
    "s_add_u32 s75, s57, s64\n"
    "v_max3_f32 v171, v171, v101, v102\n"
    "v_max3_f32 v171, v171, v103, v104\n"
    "v_mfma_f32_16x16x32_bf16 a[32:35], v[232:235], v[152:155], a[32:35]\n"
    "s_waitcnt lgkmcnt(4)\n"
    "buffer_load_dwordx4 v190, s[44:47], s75 offen offset:0 lds\n"
    "v_mfma_f32_16x16x32_bf16 a[36:39], v[236:239], v[144:147], a[36:39]\n"
    "s_add_u32 m0, s49, s61\n"
    "ds_read_b64_tr_b16 v[232:233], v246 offset:192\n"
    "ds_read_b64_tr_b16 v[234:235], v246 offset:704\n"
    "v_mfma_f32_16x16x32_bf16 a[40:43], v[236:239], v[148:151], a[40:43]\n"
    "s_add_u32 s74, s57, s65\n"
    "v_max3_f32 v171, v171, v105, v106\n"
    "v_max3_f32 v171, v171, v107, v108\n"
    "v_mfma_f32_16x16x32_bf16 a[44:47], v[236:239], v[152:155], a[44:47]\n"
    "s_waitcnt lgkmcnt(4)\n"
    "v_max3_f32 v171, v171, v109, v110\n"
    "v_max3_f32 v171, v171, v111, v112\n"
    "v_mfma_f32_16x16x32_bf16 a[48:51], v[224:227], v[144:147], a[48:51]\n"
    "ds_read_b64_tr_b16 v[236:237], v246 offset:224\n"
    "ds_read_b64_tr_b16 v[238:239], v246 offset:736\n"
    "v_mfma_f32_16x16x32_bf16 a[52:55], v[224:227], v[148:151], a[52:55]\n"
    "buffer_load_dwordx4 v190, s[44:47], s74 offen offset:0 lds\n"
    "v_mfma_f32_16x16x32_bf16 a[56:59], v[224:227], v[152:155], a[56:59]\n"
    "s_waitcnt lgkmcnt(4)\n"
    "v_max3_f32 v171, v171, v113, v114\n"
    "v_max3_f32 v171, v171, v115, v116\n"
    "v_mfma_f32_16x16x32_bf16 a[60:63], v[228:231], v[144:147], a[60:63]\n"
    "s_add_u32 m0, s49, s62\n"
    "ds_read_b64_tr_b16 v[224:225], v246 offset:256\n"
    "ds_read_b64_tr_b16 v[226:227], v246 offset:768\n"
    "v_mfma_f32_16x16x32_bf16 a[64:67], v[228:231], v[148:151], a[64:67]\n"
    "s_add_u32 s75, s57, s66\n"
    "v_max3_f32 v171, v171, v117, v118\n"
    "v_max_f32_e64 v171, v171, v119\n"
    "v_mfma_f32_16x16x32_bf16 a[68:71], v[228:231], v[152:155], a[68:71]\n"
    "s_waitcnt lgkmcnt(4)\n"
    "buffer_load_dwordx4 v190, s[44:47], s75 offen offset:0 lds\n"
    "v_mfma_f32_16x16x32_bf16 a[72:75], v[232:235], v[144:147], a[72:75]\n"
    "s_add_u32 s57, s57, s58\n"
    "ds_read_b64_tr_b16 v[228:229], v246 offset:288\n"
    "ds_read_b64_tr_b16 v[230:231], v246 offset:800\n"
    "v_mfma_f32_16x16x32_bf16 a[76:79], v[232:235], v[148:151], a[76:79]\n"
    "s_add_u32 s49, s49, 16896\n"
    "s_cmp_eq_u32 s49, s50\n"
    "s_cselect_b32 s49, s51, s49\n"
    "v_cmp_lt_f32 vcc, 0x41000000, v171\n"
    "v_add_f32_e64 v168, v168, v132\n"
    "v_mfma_f32_16x16x32_bf16 a[80:83], v[232:235], v[152:155], a[80:83]\n"
    "s_waitcnt lgkmcnt(4)\n"
    "v_add_f32_e64 v168, v168, v133\n"
    "v_add_f32_e64 v168, v168, v134\n"
    "v_mfma_f32_16x16x32_bf16 a[84:87], v[236:239], v[144:147], a[84:87]\n"
    "s_add_u32 m0, s49, s90\n"
    "s_add_u32 s74, s88, s94\n"
    "ds_read_b64_tr_b16 v[232:233], v246 offset:320\n"
    "ds_read_b64_tr_b16 v[234:235], v246 offset:832\n"
    "v_mfma_f32_16x16x32_bf16 a[88:91], v[236:239], v[148:151], a[88:91]\n"
    "buffer_load_dwordx4 v191, s[84:87], s74 offen offset:0 lds\n"
    "v_mfma_f32_16x16x32_bf16 a[92:95], v[236:239], v[152:155], a[92:95]\n"
    "s_waitcnt lgkmcnt(4)\n"
    "v_add_f32_e64 v168, v168, v135\n"
    "v_add_f32_e64 v169, v169, v124\n"
    "v_mfma_f32_16x16x32_bf16 a[96:99], v[224:227], v[144:147], a[96:99]\n"
    "s_add_u32 m0, s49, s91\n"
    "ds_read_b64_tr_b16 v[236:237], v246 offset:352\n"
    "ds_read_b64_tr_b16 v[238:239], v246 offset:864\n"
    "v_mfma_f32_16x16x32_bf16 a[100:103], v[224:227], v[148:151], a[100:103]\n"
    "s_add_u32 s75, s88, s95\n"
    "v_add_f32_e64 v169, v169, v125\n"
    "v_add_f32_e64 v169, v169, v126\n"
    "v_mfma_f32_16x16x32_bf16 a[104:107], v[224:227], v[152:155], a[104:107]\n"
    "s_waitcnt lgkmcnt(4)\n"
    "buffer_load_dwordx4 v191, s[84:87], s75 offen offset:0 lds\n"
    "v_mfma_f32_16x16x32_bf16 a[108:111], v[228:231], v[144:147], a[108:111]\n"
    "s_add_u32 m0, s49, s92\n"
    "ds_read_b64_tr_b16 v[224:225], v246 offset:384\n"
    "ds_read_b64_tr_b16 v[226:227], v246 offset:896\n"
    "v_mfma_f32_16x16x32_bf16 a[112:115], v[228:231], v[148:151], a[112:115]\n"
    "s_add_u32 s74, s88, s96\n"
    "v_add_f32_e64 v169, v169, v127\n"
    "v_add_f32_e64 v169, v169, v136\n"
    "v_mfma_f32_16x16x32_bf16 a[116:119], v[228:231], v[152:155], a[116:119]\n"
    "s_waitcnt lgkmcnt(4)\n"
    "v_add_f32_e64 v169, v169, v137\n"
    "v_add_f32_e64 v169, v169, v138\n"
    "v_mfma_f32_16x16x32_bf16 a[120:123], v[232:235], v[144:147], a[120:123]\n"
    "ds_read_b64_tr_b16 v[228:229], v246 offset:416\n"
    "ds_read_b64_tr_b16 v[230:231], v246 offset:928\n"
    "v_mfma_f32_16x16x32_bf16 a[124:127], v[232:235], v[148:151], a[124:127]\n"
    "buffer_load_dwordx4 v191, s[84:87], s74 offen offset:0 lds\n"
    "v_mfma_f32_16x16x32_bf16 a[128:131], v[232:235], v[152:155], a[128:131]\n"
    "s_waitcnt lgkmcnt(4)\n"
    "v_add_f32_e64 v169, v169, v139\n"
    "v_add_f32_e64 v170, v170, v128\n"
    "v_mfma_f32_16x16x32_bf16 a[132:135], v[236:239], v[144:147], a[132:135]\n"
    "s_add_u32 m0, s49, s93\n"
    "ds_read_b64_tr_b16 v[232:233], v246 offset:448\n"
    "ds_read_b64_tr_b16 v[234:235], v246 offset:960\n"
    "v_mfma_f32_16x16x32_bf16 a[136:139], v[236:239], v[148:151], a[136:139]\n"
    "s_add_u32 s75, s88, s97\n"
    "v_add_f32_e64 v170, v170, v129\n"
    "v_add_f32_e64 v170, v170, v130\n"
    "v_mfma_f32_16x16x32_bf16 a[140:143], v[236:239], v[152:155], a[140:143]\n"
    "s_waitcnt lgkmcnt(4)\n"
    "buffer_load_dwordx4 v191, s[84:87], s75 offen offset:0 lds\n"
    "v_mfma_f32_16x16x32_bf16 a[144:147], v[224:227], v[144:147], a[144:147]\n"
    "s_add_u32 s88, s88, s89\n"
    "ds_read_b64_tr_b16 v[236:237], v246 offset:480\n"
    "ds_read_b64_tr_b16 v[238:239], v246 offset:992\n"
    "v_mfma_f32_16x16x32_bf16 a[148:151], v[224:227], v[148:151], a[148:151]\n"
    "s_add_u32 s49, s49, 16896\n"
    "v_add_u32_e64 v245, s52, v254\n"
    "v_add_f32_e64 v170, v170, v131\n"
    "v_mfma_f32_16x16x32_bf16 a[152:155], v[224:227], v[152:155], a[152:155]\n"
    "s_waitcnt lgkmcnt(4)\n"
    "v_add_f32_e64 v170, v170, v140\n"
    "v_add_f32_e64 v170, v170, v141\n"
    "v_mfma_f32_16x16x32_bf16 a[156:159], v[228:231], v[144:147], a[156:159]\n"
    "s_add_u32 s52, s52, 16896\n"
    "ds_read_b128 v[224:227], v245 offset:0\n"
    "v_add_f32_e64 v170, v170, v142\n"
    "v_mfma_f32_16x16x32_bf16 a[160:163], v[228:231], v[148:151], a[160:163]\n"
    "s_cmp_eq_u32 s52, s53\n"
    "v_add_f32_e64 v170, v170, v143\n"
    "v_mfma_f32_16x16x32_bf16 a[164:167], v[228:231], v[152:155], a[164:167]\n"
    "s_waitcnt lgkmcnt(3)\n"
    "v_mfma_f32_16x16x32_bf16 a[168:171], v[232:235], v[144:147], a[168:171]\n"
    "s_cselect_b32 s52, s79, s52\n"
    "ds_read_b128 v[228:231], v245 offset:512\n"
    "v_mfma_f32_16x16x32_bf16 a[172:175], v[232:235], v[148:151], a[172:175]\n"
    "s_cmp_eq_u32 s49, s50\n"
    "v_mfma_f32_16x16x32_bf16 a[176:179], v[232:235], v[152:155], a[176:179]\n"
    "s_waitcnt lgkmcnt(2)\n"
    "v_mfma_f32_16x16x32_bf16 a[180:183], v[236:239], v[144:147], a[180:183]\n"
    "s_cselect_b32 s49, s51, s49\n"
    "ds_read_b128 v[232:235], v245 offset:64\n"
    "v_mfma_f32_16x16x32_bf16 a[184:187], v[236:239], v[148:151], a[184:187]\n"
    "v_mfma_f32_16x16x32_bf16 a[188:191], v[236:239], v[152:155], a[188:191]\n"
    "s_waitcnt lgkmcnt(2)\n"
    "s_cbranch_vccnz L_stub5_%=\n"
    "L_ret5_%=:\n"
    "v_mfma_f32_16x16x32_bf16 v[120:123], v[224:227], v[0:3], v[156:159]\n"
    "ds_read_b128 v[236:239], v245 offset:576\n"
    "v_mfma_f32_16x16x32_bf16 v[124:127], v[224:227], v[4:7], v[160:163]\n"
    "v_exp_f32_e64 v96, v96\n"
    "v_mfma_f32_16x16x32_bf16 v[128:131], v[224:227], v[8:11], v[164:167]\n"
    "s_waitcnt lgkmcnt(2)\n"
    "v_exp_f32_e64 v97, v97\n"
    "v_mfma_f32_16x16x32_bf16 v[132:135], v[228:231], v[0:3], v[156:159]\n"
    "ds_read_b128 v[224:227], v245 offset:128\n"
    "v_cvt_pk_bf16_f32 v144, v96, v97\n"
    "v_mfma_f32_16x16x32_bf16 v[136:139], v[228:231], v[4:7], v[160:163]\n"
    "v_exp_f32_e64 v98, v98\n"
    "v_mfma_f32_16x16x32_bf16 v[140:143], v[228:231], v[8:11], v[164:167]\n"
    "s_waitcnt lgkmcnt(2)\n"
    "v_exp_f32_e64 v99, v99\n"
    "v_mfma_f32_16x16x32_bf16 v[120:123], v[232:235], v[12:15], v[120:123]\n"
    "ds_read_b128 v[228:231], v245 offset:640\n"
    "v_cvt_pk_bf16_f32 v145, v98, v99\n"
    "v_mfma_f32_16x16x32_bf16 v[124:127], v[232:235], v[16:19], v[124:127]\n"
    "v_exp_f32_e64 v108, v108\n"
    "v_mfma_f32_16x16x32_bf16 v[128:131], v[232:235], v[20:23], v[128:131]\n"
    "s_waitcnt lgkmcnt(2)\n"
    "v_exp_f32_e64 v109, v109\n"
    "v_mfma_f32_16x16x32_bf16 v[132:135], v[236:239], v[12:15], v[132:135]\n"
    "ds_read_b128 v[232:235], v245 offset:192\n"
    "v_cvt_pk_bf16_f32 v146, v108, v109\n"
    "v_mfma_f32_16x16x32_bf16 v[136:139], v[236:239], v[16:19], v[136:139]\n"
    "v_exp_f32_e64 v110, v110\n"
    "v_mfma_f32_16x16x32_bf16 v[140:143], v[236:239], v[20:23], v[140:143]\n"
    "s_waitcnt lgkmcnt(2)\n"
    "v_exp_f32_e64 v111, v111\n"
    "v_mfma_f32_16x16x32_bf16 v[120:123], v[224:227], v[24:27], v[120:123]\n"
    "ds_read_b128 v[236:239], v245 offset:704\n"
    "v_cvt_pk_bf16_f32 v147, v110, v111\n"
    "v_mfma_f32_16x16x32_bf16 v[124:127], v[224:227], v[28:31], v[124:127]\n"
    "v_exp_f32_e64 v100, v100\n"
    "v_mfma_f32_16x16x32_bf16 v[128:131], v[224:227], v[32:35], v[128:131]\n"
    "s_waitcnt lgkmcnt(2)\n"
    "v_exp_f32_e64 v101, v101\n"
    "v_mfma_f32_16x16x32_bf16 v[132:135], v[228:231], v[24:27], v[132:135]\n"
    "ds_read_b128 v[224:227], v245 offset:256\n"
    "v_cvt_pk_bf16_f32 v148, v100, v101\n"
    "v_mfma_f32_16x16x32_bf16 v[136:139], v[228:231], v[28:31], v[136:139]\n"
    "v_exp_f32_e64 v102, v102\n"
    "v_mfma_f32_16x16x32_bf16 v[140:143], v[228:231], v[32:35], v[140:143]\n"
    "s_waitcnt lgkmcnt(2)\n"
    "v_exp_f32_e64 v103, v103\n"
    "v_mfma_f32_16x16x32_bf16 v[120:123], v[232:235], v[36:39], v[120:123]\n"
    "ds_read_b128 v[228:231], v245 offset:768\n"
    "v_cvt_pk_bf16_f32 v149, v102, v103\n"
    "v_mfma_f32_16x16x32_bf16 v[124:127], v[232:235], v[40:43], v[124:127]\n"
    "v_exp_f32_e64 v112, v112\n"
    "v_mfma_f32_16x16x32_bf16 v[128:131], v[232:235], v[44:47], v[128:131]\n"
    "s_waitcnt lgkmcnt(2)\n"
    "v_exp_f32_e64 v113, v113\n"
    "v_mfma_f32_16x16x32_bf16 v[132:135], v[236:239], v[36:39], v[132:135]\n"
    "ds_read_b128 v[232:235], v245 offset:320\n"
    "v_cvt_pk_bf16_f32 v150, v112, v113\n"
    "v_mfma_f32_16x16x32_bf16 v[136:139], v[236:239], v[40:43], v[136:139]\n"
    "v_exp_f32_e64 v114, v114\n"
    "v_mfma_f32_16x16x32_bf16 v[140:143], v[236:239], v[44:47], v[140:143]\n"
    "s_waitcnt lgkmcnt(2)\n"
    "v_exp_f32_e64 v115, v115\n"
    "v_mfma_f32_16x16x32_bf16 v[120:123], v[224:227], v[48:51], v[120:123]\n"
    "ds_read_b128 v[236:239], v245 offset:832\n"
    "v_cvt_pk_bf16_f32 v151, v114, v115\n"
    "v_mfma_f32_16x16x32_bf16 v[124:127], v[224:227], v[52:55], v[124:127]\n"
    "v_exp_f32_e64 v104, v104\n"
    "v_mfma_f32_16x16x32_bf16 v[128:131], v[224:227], v[56:59], v[128:131]\n"
    "s_waitcnt lgkmcnt(2)\n"
    "v_exp_f32_e64 v105, v105\n"
    "v_mfma_f32_16x16x32_bf16 v[132:135], v[228:231], v[48:51], v[132:135]\n"
    "ds_read_b128 v[224:227], v245 offset:384\n"
    "v_cvt_pk_bf16_f32 v152, v104, v105\n"
    "v_mfma_f32_16x16x32_bf16 v[136:139], v[228:231], v[52:55], v[136:139]\n"
    "v_exp_f32_e64 v106, v106\n"
    "v_mfma_f32_16x16x32_bf16 v[140:143], v[228:231], v[56:59], v[140:143]\n"
    "s_waitcnt lgkmcnt(2)\n"
    "v_exp_f32_e64 v107, v107\n"
    "v_mfma_f32_16x16x32_bf16 v[120:123], v[232:235], v[60:63], v[120:123]\n"
    "ds_read_b128 v[228:231], v245 offset:896\n"
    "v_cvt_pk_bf16_f32 v153, v106, v107\n"
    "v_mfma_f32_16x16x32_bf16 v[124:127], v[232:235], v[64:67], v[124:127]\n"
    "v_exp_f32_e64 v116, v116\n"
    "v_mfma_f32_16x16x32_bf16 v[128:131], v[232:235], v[68:71], v[128:131]\n"
    "s_waitcnt lgkmcnt(2)\n"
    "v_exp_f32_e64 v117, v117\n"
    "v_mfma_f32_16x16x32_bf16 v[132:135], v[236:239], v[60:63], v[132:135]\n"
    "ds_read_b128 v[232:235], v245 offset:448\n"
    "v_cvt_pk_bf16_f32 v154, v116, v117\n"
    "v_mfma_f32_16x16x32_bf16 v[136:139], v[236:239], v[64:67], v[136:139]\n"
    "v_exp_f32_e64 v118, v118\n"
    "v_mfma_f32_16x16x32_bf16 v[140:143], v[236:239], v[68:71], v[140:143]\n"
    "s_waitcnt lgkmcnt(2)\n"
    "v_exp_f32_e64 v119, v119\n"
    "v_mfma_f32_16x16x32_bf16 v[120:123], v[224:227], v[72:75], v[120:123]\n"
    "ds_read_b128 v[236:239], v245 offset:960\n"
    "v_cvt_pk_bf16_f32 v155, v118, v119\n"
    "v_mfma_f32_16x16x32_bf16 v[124:127], v[224:227], v[76:79], v[124:127]\n"
    "s_waitcnt vmcnt(22)\n"
    "v_add_u32_e64 v246, s52, v255\n"
    "v_mfma_f32_16x16x32_bf16 v[128:131], v[224:227], v[80:83], v[128:131]\n"
    "s_waitcnt lgkmcnt(2)\n"
    "v_mfma_f32_16x16x32_bf16 v[132:135], v[228:231], v[72:75], v[132:135]\n"
    "s_barrier\n"
    "ds_read_b64_tr_b16 v[224:225], v246 offset:0\n"
    "ds_read_b64_tr_b16 v[226:227], v246 offset:512\n"
    "v_mfma_f32_16x16x32_bf16 v[136:139], v[228:231], v[76:79], v[136:139]\n"
    "s_add_u32 s52, s52, 16896\n"
    "v_mfma_f32_16x16x32_bf16 v[140:143], v[228:231], v[80:83], v[140:143]\n"
    "s_waitcnt lgkmcnt(3)\n"
    "v_mfma_f32_16x16x32_bf16 v[120:123], v[232:235], v[84:87], v[120:123]\n"
    "s_cmp_eq_u32 s52, s53\n"
    "ds_read_b64_tr_b16 v[228:229], v246 offset:32\n"
    "ds_read_b64_tr_b16 v[230:231], v246 offset:544\n"
    "v_mfma_f32_16x16x32_bf16 v[124:127], v[232:235], v[88:91], v[124:127]\n"
    "s_cselect_b32 s52, s79, s52\n"
    "v_mfma_f32_16x16x32_bf16 v[128:131], v[232:235], v[92:95], v[128:131]\n"
    "s_waitcnt lgkmcnt(4)\n"
    "v_mfma_f32_16x16x32_bf16 v[132:135], v[236:239], v[84:87], v[132:135]\n"
    "ds_read_b64_tr_b16 v[232:233], v246 offset:64\n"
    "ds_read_b64_tr_b16 v[234:235], v246 offset:576\n"
    "v_mfma_f32_16x16x32_bf16 v[136:139], v[236:239], v[88:91], v[136:139]\n"
    "v_mfma_f32_16x16x32_bf16 v[140:143], v[236:239], v[92:95], v[140:143]\n"
    "s_waitcnt lgkmcnt(4)\n"
    "v_mfma_f32_16x16x32_bf16 a[0:3], v[224:227], v[144:147], a[0:3]\n"
    "s_add_u32 m0, s49, s59\n"
    "ds_read_b64_tr_b16 v[236:237], v246 offset:96\n"
    "ds_read_b64_tr_b16 v[238:239], v246 offset:608\n"
    "v_mfma_f32_16x16x32_bf16 a[4:7], v[224:227], v[148:151], a[4:7]\n"
    "s_add_u32 s74, s57, s63\n"
    "v_add_f32_e64 v168, v168, v96\n"
    "v_add_f32_e64 v168, v168, v97\n"
    "v_mfma_f32_16x16x32_bf16 a[8:11], v[224:227], v[152:155], a[8:11]\n"
    "s_waitcnt lgkmcnt(4)\n"
    "v_add_f32_e64 v168, v168, v98\n"
    "v_add_f32_e64 v168, v168, v99\n"
    "v_mfma_f32_16x16x32_bf16 a[12:15], v[228:231], v[144:147], a[12:15]\n"
    "ds_read_b64_tr_b16 v[224:225], v246 offset:128\n"
    "ds_read_b64_tr_b16 v[226:227], v246 offset:640\n"
    "v_mfma_f32_16x16x32_bf16 a[16:19], v[228:231], v[148:151], a[16:19]\n"
    "buffer_load_dwordx4 v190, s[44:47], s74 offen offset:0 lds\n"
    "v_mfma_f32_16x16x32_bf16 a[20:23], v[228:231], v[152:155], a[20:23]\n"
    "s_waitcnt lgkmcnt(4)\n"
    "v_max3_f32 v171, v120, v121, v122\n"
    "v_max3_f32 v171, v171, v123, v124\n"
    "v_mfma_f32_16x16x32_bf16 a[24:27], v[232:235], v[144:147], a[24:27]\n"
    "s_add_u32 m0, s49, s60\n"
    "ds_read_b64_tr_b16 v[228:229], v246 offset:160\n"
    "ds_read_b64_tr_b16 v[230:231], v246 offset:672\n"
    "v_mfma_f32_16x16x32_bf16 a[28:31], v[232:235], v[148:151], a[28:31]\n"
    "s_add_u32 s75, s57, s64\n"
    "v_max3_f32 v171, v171, v125, v126\n"
    "v_max3_f32 v171, v171, v127, v128\n"
    "v_mfma_f32_16x16x32_bf16 a[32:35], v[232:235], v[152:155], a[32:35]\n"
    "s_waitcnt lgkmcnt(4)\n"
    "buffer_load_dwordx4 v190, s[44:47], s75 offen offset:0 lds\n"
    "v_mfma_f32_16x16x32_bf16 a[36:39], v[236:239], v[144:147], a[36:39]\n"
    "s_add_u32 m0, s49, s61\n"
    "ds_read_b64_tr_b16 v[232:233], v246 offset:192\n"
    "ds_read_b64_tr_b16 v[234:235], v246 offset:704\n"
    "v_mfma_f32_16x16x32_bf16 a[40:43], v[236:239], v[148:151], a[40:43]\n"
    "s_add_u32 s74, s57, s65\n"
    "v_max3_f32 v171, v171, v129, v130\n"
    "v_max3_f32 v171, v171, v131, v132\n"
    "v_mfma_f32_16x16x32_bf16 a[44:47], v[236:239], v[152:155], a[44:47]\n"
    "s_waitcnt lgkmcnt(4)\n"
    "v_max3_f32 v171, v171, v133, v134\n"
    "v_max3_f32 v171, v171, v135, v136\n"
    "v_mfma_f32_16x16x32_bf16 a[48:51], v[224:227], v[144:147], a[48:51]\n"
    "ds_read_b64_tr_b16 v[236:237], v246 offset:224\n"
    "ds_read_b64_tr_b16 v[238:239], v246 offset:736\n"
    "v_mfma_f32_16x16x32_bf16 a[52:55], v[224:227], v[148:151], a[52:55]\n"
    "buffer_load_dwordx4 v190, s[44:47], s74 offen offset:0 lds\n"
    "v_mfma_f32_16x16x32_bf16 a[56:59], v[224:227], v[152:155], a[56:59]\n"
    "s_waitcnt lgkmcnt(4)\n"
    "v_max3_f32 v171, v171, v137, v138\n"
    "v_max3_f32 v171, v171, v139, v140\n"
    "v_mfma_f32_16x16x32_bf16 a[60:63], v[228:231], v[144:147], a[60:63]\n"
    "s_add_u32 m0, s49, s62\n"
    "ds_read_b64_tr_b16 v[224:225], v246 offset:256\n"
    "ds_read_b64_tr_b16 v[226:227], v246 offset:768\n"
    "v_mfma_f32_16x16x32_bf16 a[64:67], v[228:231], v[148:151], a[64:67]\n"
    "s_add_u32 s75, s57, s66\n"
    "v_max3_f32 v171, v171, v141, v142\n"
    "v_max_f32_e64 v171, v171, v143\n"
    "v_mfma_f32_16x16x32_bf16 a[68:71], v[228:231], v[152:155], a[68:71]\n"
    "s_waitcnt lgkmcnt(4)\n"
    "buffer_load_dwordx4 v190, s[44:47], s75 offen offset:0 lds\n"
    "v_mfma_f32_16x16x32_bf16 a[72:75], v[232:235], v[144:147], a[72:75]\n"
    "s_add_u32 s57, s57, s58\n"
    "ds_read_b64_tr_b16 v[228:229], v246 offset:288\n"
    "ds_read_b64_tr_b16 v[230:231], v246 offset:800\n"
    "v_mfma_f32_16x16x32_bf16 a[76:79], v[232:235], v[148:151], a[76:79]\n"
    "s_add_u32 s49, s49, 16896\n"
    "s_cmp_eq_u32 s49, s50\n"
    "s_cselect_b32 s49, s51, s49\n"
    "v_cmp_lt_f32 vcc, 0x41000000, v171\n"
    "v_add_f32_e64 v168, v168, v108\n"
    "v_mfma_f32_16x16x32_bf16 a[80:83], v[232:235], v[152:155], a[80:83]\n"
    "s_waitcnt lgkmcnt(4)\n"
    "v_add_f32_e64 v168, v168, v109\n"
    "v_add_f32_e64 v168, v168, v110\n"
    "v_mfma_f32_16x16x32_bf16 a[84:87], v[236:239], v[144:147], a[84:87]\n"
    "s_add_u32 m0, s49, s90\n"
    "s_add_u32 s74, s88, s94\n"
    "ds_read_b64_tr_b16 v[232:233], v246 offset:320\n"
    "ds_read_b64_tr_b16 v[234:235], v246 offset:832\n"
    "v_mfma_f32_16x16x32_bf16 a[88:91], v[236:239], v[148:151], a[88:91]\n"
    "buffer_load_dwordx4 v191, s[84:87], s74 offen offset:0 lds\n"
    "v_mfma_f32_16x16x32_bf16 a[92:95], v[236:239], v[152:155], a[92:95]\n"
    "s_waitcnt lgkmcnt(4)\n"
    "v_add_f32_e64 v168, v168, v111\n"
    "v_add_f32_e64 v169, v169, v100\n"
    "v_mfma_f32_16x16x32_bf16 a[96:99], v[224:227], v[144:147], a[96:99]\n"
    "s_add_u32 m0, s49, s91\n"
    "ds_read_b64_tr_b16 v[236:237], v246 offset:352\n"
    "ds_read_b64_tr_b16 v[238:239], v246 offset:864\n"
    "v_mfma_f32_16x16x32_bf16 a[100:103], v[224:227], v[148:151], a[100:103]\n"
    "s_add_u32 s75, s88, s95\n"
    "v_add_f32_e64 v169, v169, v101\n"
    "v_add_f32_e64 v169, v169, v102\n"
    "v_mfma_f32_16x16x32_bf16 a[104:107], v[224:227], v[152:155], a[104:107]\n"
    "s_waitcnt lgkmcnt(4)\n"
    "buffer_load_dwordx4 v191, s[84:87], s75 offen offset:0 lds\n"
    "v_mfma_f32_16x16x32_bf16 a[108:111], v[228:231], v[144:147], a[108:111]\n"
    "s_add_u32 m0, s49, s92\n"
    "ds_read_b64_tr_b16 v[224:225], v246 offset:384\n"
    "ds_read_b64_tr_b16 v[226:227], v246 offset:896\n"
    "v_mfma_f32_16x16x32_bf16 a[112:115], v[228:231], v[148:151], a[112:115]\n"
    "s_add_u32 s74, s88, s96\n"
    "v_add_f32_e64 v169, v169, v103\n"
    "v_add_f32_e64 v169, v169, v112\n"
    "v_mfma_f32_16x16x32_bf16 a[116:119], v[228:231], v[152:155], a[116:119]\n"
    "s_waitcnt lgkmcnt(4)\n"
    "v_add_f32_e64 v169, v169, v113\n"
    "v_add_f32_e64 v169, v169, v114\n"
    "v_mfma_f32_16x16x32_bf16 a[120:123], v[232:235], v[144:147], a[120:123]\n"
    "ds_read_b64_tr_b16 v[228:229], v246 offset:416\n"
    "ds_read_b64_tr_b16 v[230:231], v246 offset:928\n"
    "v_mfma_f32_16x16x32_bf16 a[124:127], v[232:235], v[148:151], a[124:127]\n"
    "buffer_load_dwordx4 v191, s[84:87], s74 offen offset:0 lds\n"
    "v_mfma_f32_16x16x32_bf16 a[128:131], v[232:235], v[152:155], a[128:131]\n"
    "s_waitcnt lgkmcnt(4)\n"
    "v_add_f32_e64 v169, v169, v115\n"
    "v_add_f32_e64 v170, v170, v104\n"
    "v_mfma_f32_16x16x32_bf16 a[132:135], v[236:239], v[144:147], a[132:135]\n"
    "s_add_u32 m0, s49, s93\n"
    "ds_read_b64_tr_b16 v[232:233], v246 offset:448\n"
    "ds_read_b64_tr_b16 v[234:235], v246 offset:960\n"
    "v_mfma_f32_16x16x32_bf16 a[136:139], v[236:239], v[148:151], a[136:139]\n"
    "s_add_u32 s75, s88, s97\n"
    "v_add_f32_e64 v170, v170, v105\n"
    "v_add_f32_e64 v170, v170, v106\n"
    "v_mfma_f32_16x16x32_bf16 a[140:143], v[236:239], v[152:155], a[140:143]\n"
    "s_waitcnt lgkmcnt(4)\n"
    "buffer_load_dwordx4 v191, s[84:87], s75 offen offset:0 lds\n"
    "v_mfma_f32_16x16x32_bf16 a[144:147], v[224:227], v[144:147], a[144:147]\n"
    "s_add_u32 s88, s88, s89\n"
    "ds_read_b64_tr_b16 v[236:237], v246 offset:480\n"
    "ds_read_b64_tr_b16 v[238:239], v246 offset:992\n"
    "v_mfma_f32_16x16x32_bf16 a[148:151], v[224:227], v[148:151], a[148:151]\n"
    "s_waitcnt vmcnt(26)\n"
    "v_add_u32_e64 v246, s52, v255\n"
    "v_add_f32_e64 v170, v170, v107\n"
    "v_mfma_f32_16x16x32_bf16 a[152:155], v[224:227], v[152:155], a[152:155]\n"
    "s_waitcnt lgkmcnt(4)\n"
    "v_add_f32_e64 v170, v170, v116\n"
    "v_add_f32_e64 v170, v170, v117\n"
    "v_mfma_f32_16x16x32_bf16 a[156:159], v[228:231], v[144:147], a[156:159]\n"
    "s_barrier\n"
    "ds_read_b64_tr_b16 v[224:225], v246 offset:0\n"
    "ds_read_b64_tr_b16 v[226:227], v246 offset:512\n"
    "v_mfma_f32_16x16x32_bf16 a[160:163], v[228:231], v[148:151], a[160:163]\n"
    "s_add_u32 s52, s52, 16896\n"
    "v_add_f32_e64 v170, v170, v118\n"
    "v_add_f32_e64 v170, v170, v119\n"
    "v_mfma_f32_16x16x32_bf16 a[164:167], v[228:231], v[152:155], a[164:167]\n"
    "s_waitcnt lgkmcnt(4)\n"
    "v_mfma_f32_16x16x32_bf16 a[168:171], v[232:235], v[144:147], a[168:171]\n"
    "s_cmp_eq_u32 s52, s53\n"
    "ds_read_b64_tr_b16 v[228:229], v246 offset:32\n"
    "ds_read_b64_tr_b16 v[230:231], v246 offset:544\n"
    "v_mfma_f32_16x16x32_bf16 a[172:175], v[232:235], v[148:151], a[172:175]\n"
    "s_cselect_b32 s52, s79, s52\n"
    "v_mfma_f32_16x16x32_bf16 a[176:179], v[232:235], v[152:155], a[176:179]\n"
    "s_waitcnt lgkmcnt(4)\n"
    "v_mfma_f32_16x16x32_bf16 a[180:183], v[236:239], v[144:147], a[180:183]\n"
    "s_add_u32 s49, s49, 16896\n"
    "s_cmp_eq_u32 s49, s50\n"
    "s_cselect_b32 s49, s51, s49\n"
    "ds_read_b64_tr_b16 v[232:233], v246 offset:64\n"
    "ds_read_b64_tr_b16 v[234:235], v246 offset:576\n"
    "v_mfma_f32_16x16x32_bf16 a[184:187], v[236:239], v[148:151], a[184:187]\n"
    "v_mfma_f32_16x16x32_bf16 a[188:191], v[236:239], v[152:155], a[188:191]\n"
    "s_waitcnt lgkmcnt(4)\n"
    "s_cbranch_vccnz L_stub6_%=\n"
    "L_ret6_%=:\n"
    ".p2align 3\n"
    "v_exp_f32_e64 v120, v120\n"
    "v_exp_f32_e64 v121, v121\n"
    "v_exp_f32_e64 v122, v122\n"
    "v_exp_f32_e64 v123, v123\n"
    "v_exp_f32_e64 v132, v132\n"
    "v_exp_f32_e64 v133, v133\n"
    "v_exp_f32_e64 v134, v134\n"
    "v_exp_f32_e64 v135, v135\n"
    "v_exp_f32_e64 v124, v124\n"
    "v_exp_f32_e64 v125, v125\n"
    "v_exp_f32_e64 v126, v126\n"
    "v_exp_f32_e64 v127, v127\n"
    "v_exp_f32_e64 v136, v136\n"
    "v_exp_f32_e64 v137, v137\n"
    "v_exp_f32_e64 v138, v138\n"
    "v_exp_f32_e64 v139, v139\n"
    "v_exp_f32_e64 v128, v128\n"
    "v_exp_f32_e64 v129, v129\n"
    "v_exp_f32_e64 v130, v130\n"
    "v_exp_f32_e64 v131, v131\n"
    "v_exp_f32_e64 v140, v140\n"
    "v_exp_f32_e64 v141, v141\n"
    "v_exp_f32_e64 v142, v142\n"
    "v_exp_f32_e64 v143, v143\n"
    "v_cvt_pk_bf16_f32 v144, v120, v121\n"
    "v_cvt_pk_bf16_f32 v145, v122, v123\n"
    "v_cvt_pk_bf16_f32 v146, v132, v133\n"
    "v_cvt_pk_bf16_f32 v147, v134, v135\n"
    "v_cvt_pk_bf16_f32 v148, v124, v125\n"
    "v_cvt_pk_bf16_f32 v149, v126, v127\n"
    "v_cvt_pk_bf16_f32 v150, v136, v137\n"
    "v_cvt_pk_bf16_f32 v151, v138, v139\n"
    "v_cvt_pk_bf16_f32 v152, v128, v129\n"
    "v_cvt_pk_bf16_f32 v153, v130, v131\n"
    "v_cvt_pk_bf16_f32 v154, v140, v141\n"
    "v_cvt_pk_bf16_f32 v155, v142, v143\n"
    "v_mfma_f32_16x16x32_bf16 a[0:3], v[224:227], v[144:147], a[0:3]\n"
    "ds_read_b64_tr_b16 v[236:237], v246 offset:96\n"
    "ds_read_b64_tr_b16 v[238:239], v246 offset:608\n"
    "v_mfma_f32_16x16x32_bf16 a[4:7], v[224:227], v[148:151], a[4:7]\n"
    "v_add_f32_e64 v168, v168, v120\n"
    "v_add_f32_e64 v168, v168, v121\n"
    "v_mfma_f32_16x16x32_bf16 a[8:11], v[224:227], v[152:155], a[8:11]\n"
    "s_waitcnt lgkmcnt(4)\n"
    "v_add_f32_e64 v168, v168, v122\n"
    "v_add_f32_e64 v168, v168, v123\n"
    "v_mfma_f32_16x16x32_bf16 a[12:15], v[228:231], v[144:147], a[12:15]\n"
    "s_lshr_b32 s46, s100, 10\n"
    "ds_read_b64_tr_b16 v[224:225], v246 offset:128\n"
    "ds_read_b64_tr_b16 v[226:227], v246 offset:640\n"
    "v_mfma_f32_16x16x32_bf16 a[16:19], v[228:231], v[148:151], a[16:19]\n"
    "s_mul_i32 s46, s46, 172032\n"
    "v_add_f32_e64 v168, v168, v132\n"
    "v_add_f32_e64 v168, v168, v133\n"
    "v_mfma_f32_16x16x32_bf16 a[20:23], v[228:231], v[152:155], a[20:23]\n"
    "s_waitcnt lgkmcnt(4)\n"
    "v_add_f32_e64 v168, v168, v134\n"
    "v_add_f32_e64 v168, v168, v135\n"
    "v_mfma_f32_16x16x32_bf16 a[24:27], v[232:235], v[144:147], a[24:27]\n"
    "s_sub_u32 s44, s40, s46\n"
    "ds_read_b64_tr_b16 v[228:229], v246 offset:160\n"
    "ds_read_b64_tr_b16 v[230:231], v246 offset:672\n"
    "v_mfma_f32_16x16x32_bf16 a[28:31], v[232:235], v[148:151], a[28:31]\n"
    "s_subb_u32 s45, s41, 0\n"
    "v_add_f32_e64 v169, v169, v124\n"
    "v_add_f32_e64 v169, v169, v125\n"
    "v_mfma_f32_16x16x32_bf16 a[32:35], v[232:235], v[152:155], a[32:35]\n"
    "s_waitcnt lgkmcnt(4)\n"
    "v_add_f32_e64 v169, v169, v126\n"
    "v_add_f32_e64 v169, v169, v127\n"
    "v_mfma_f32_16x16x32_bf16 a[36:39], v[236:239], v[144:147], a[36:39]\n"
    "s_add_u32 s44, s44, 1376256\n"
    "ds_read_b64_tr_b16 v[232:233], v246 offset:192\n"
    "ds_read_b64_tr_b16 v[234:235], v246 offset:704\n"
    "v_mfma_f32_16x16x32_bf16 a[40:43], v[236:239], v[148:151], a[40:43]\n"
    "s_addc_u32 s45, s45, 0\n"
    "v_add_f32_e64 v169, v169, v136\n"
    "v_add_f32_e64 v169, v169, v137\n"
    "v_mfma_f32_16x16x32_bf16 a[44:47], v[236:239], v[152:155], a[44:47]\n"
    "s_waitcnt lgkmcnt(4)\n"
    "v_add_f32_e64 v169, v169, v138\n"
    "v_add_f32_e64 v169, v169, v139\n"
    "v_mfma_f32_16x16x32_bf16 a[48:51], v[224:227], v[144:147], a[48:51]\n"
    "s_mov_b32 s46, 3072\n"
    "ds_read_b64_tr_b16 v[236:237], v246 offset:224\n"
    "ds_read_b64_tr_b16 v[238:239], v246 offset:736\n"
    "v_mfma_f32_16x16x32_bf16 a[52:55], v[224:227], v[148:151], a[52:55]\n"
    "s_mov_b32 s47, 131072\n"
    "v_add_f32_e64 v170, v170, v128\n"
    "v_add_f32_e64 v170, v170, v129\n"
    "v_mfma_f32_16x16x32_bf16 a[56:59], v[224:227], v[152:155], a[56:59]\n"
    "s_waitcnt lgkmcnt(4)\n"
    "v_add_f32_e64 v170, v170, v130\n"
    "v_add_f32_e64 v170, v170, v131\n"
    "v_mfma_f32_16x16x32_bf16 a[60:63], v[228:231], v[144:147], a[60:63]\n"
    "s_lshr_b32 s60, s100, 10\n"
    "ds_read_b64_tr_b16 v[224:225], v246 offset:256\n"
    "ds_read_b64_tr_b16 v[226:227], v246 offset:768\n"
    "v_mfma_f32_16x16x32_bf16 a[64:67], v[228:231], v[148:151], a[64:67]\n"
    "s_mul_i32 s60, s60, 4\n"
    "v_add_f32_e64 v170, v170, v140\n"
    "v_add_f32_e64 v170, v170, v141\n"
    "v_mfma_f32_16x16x32_bf16 a[68:71], v[228:231], v[152:155], a[68:71]\n"
    "s_waitcnt lgkmcnt(4)\n"
    "v_add_f32_e64 v170, v170, v142\n"
    "v_add_f32_e64 v170, v170, v143\n"
    "v_mfma_f32_16x16x32_bf16 a[72:75], v[232:235], v[144:147], a[72:75]\n"
    "s_add_u32 s60, s60, 0\n"
    "ds_read_b64_tr_b16 v[228:229], v246 offset:288\n"
    "ds_read_b64_tr_b16 v[230:231], v246 offset:800\n"
    "v_mfma_f32_16x16x32_bf16 a[76:79], v[232:235], v[148:151], a[76:79]\n"
    "s_lshr_b32 s61, s100, 10\n"
    "v_mbcnt_lo_u32_b32 v188, -1, 0\n"
    "v_mbcnt_hi_u32_b32 v188, -1, v188\n"
    "v_mfma_f32_16x16x32_bf16 a[80:83], v[232:235], v[152:155], a[80:83]\n"
    "s_waitcnt lgkmcnt(4)\n"
    "v_lshlrev_b32 v183, 7, v188\n"
    "v_cmp_gt_u32 vcc, 8, v188\n"
    "v_mfma_f32_16x16x32_bf16 a[84:87], v[236:239], v[144:147], a[84:87]\n"
    "s_mul_i32 s61, s61, 4\n"
    "ds_read_b64_tr_b16 v[232:233], v246 offset:320\n"
    "ds_read_b64_tr_b16 v[234:235], v246 offset:832\n"
    "v_mfma_f32_16x16x32_bf16 a[88:91], v[236:239], v[148:151], a[88:91]\n"
    "s_add_u32 s61, s61, 1024\n"
    "v_mov_b32 v188, 1048576\n"
    "v_cndmask_b32 v183, v188, v183, vcc\n"
    "v_mfma_f32_16x16x32_bf16 a[92:95], v[236:239], v[152:155], a[92:95]\n"
    "s_waitcnt lgkmcnt(4)\n"
    "v_mbcnt_lo_u32_b32 v188, -1, 0\n"
    "v_mbcnt_hi_u32_b32 v188, -1, v188\n"
    "v_mfma_f32_16x16x32_bf16 a[96:99], v[224:227], v[144:147], a[96:99]\n"
    "s_lshr_b32 s62, s100, 10\n"
    "ds_read_b64_tr_b16 v[236:237], v246 offset:352\n"
    "ds_read_b64_tr_b16 v[238:239], v246 offset:864\n"
    "v_mfma_f32_16x16x32_bf16 a[100:103], v[224:227], v[148:151], a[100:103]\n"
    "s_mul_i32 s62, s62, 4\n"
    "v_lshlrev_b32 v182, 2, v188\n"
    "v_cmp_gt_u32 vcc, 8, v188\n"
    "v_mfma_f32_16x16x32_bf16 a[104:107], v[224:227], v[152:155], a[104:107]\n"
    "s_waitcnt lgkmcnt(4)\n"
    "v_mov_b32 v188, 1048576\n"
    "v_cndmask_b32 v182, v188, v182, vcc\n"
    "v_mfma_f32_16x16x32_bf16 a[108:111], v[228:231], v[144:147], a[108:111]\n"
    "s_add_u32 s62, s62, 2048\n"
    "ds_read_b64_tr_b16 v[224:225], v246 offset:384\n"
    "ds_read_b64_tr_b16 v[226:227], v246 offset:896\n"
    "v_mfma_f32_16x16x32_bf16 a[112:115], v[228:231], v[148:151], a[112:115]\n"
    "s_lshr_b32 s63, s100, 10\n"
    "v_mfma_f32_16x16x32_bf16 a[116:119], v[228:231], v[152:155], a[116:119]\n"
    "s_waitcnt lgkmcnt(4)\n"
    "v_mfma_f32_16x16x32_bf16 a[120:123], v[232:235], v[144:147], a[120:123]\n"
    "s_mul_i32 s63, s63, 128\n"
    "ds_read_b64_tr_b16 v[228:229], v246 offset:416\n"
    "ds_read_b64_tr_b16 v[230:231], v246 offset:928\n"
    "v_mfma_f32_16x16x32_bf16 a[124:127], v[232:235], v[148:151], a[124:127]\n"
    "s_add_u32 s63, s63, 1024\n"
    "v_mfma_f32_16x16x32_bf16 a[128:131], v[232:235], v[152:155], a[128:131]\n"
    "s_waitcnt lgkmcnt(4)\n"
    "v_mfma_f32_16x16x32_bf16 a[132:135], v[236:239], v[144:147], a[132:135]\n"
    "s_lshr_b32 s64, s100, 10\n"
    "ds_read_b64_tr_b16 v[232:233], v246 offset:448\n"
    "ds_read_b64_tr_b16 v[234:235], v246 offset:960\n"
    "v_mfma_f32_16x16x32_bf16 a[136:139], v[236:239], v[148:151], a[136:139]\n"
    "s_mul_i32 s64, s64, 128\n"
    "v_mfma_f32_16x16x32_bf16 a[140:143], v[236:239], v[152:155], a[140:143]\n"
    "s_waitcnt lgkmcnt(4)\n"
    "v_mfma_f32_16x16x32_bf16 a[144:147], v[224:227], v[144:147], a[144:147]\n"
    "s_add_u32 s64, s64, 2048\n"
    "ds_read_b64_tr_b16 v[236:237], v246 offset:480\n"
    "ds_read_b64_tr_b16 v[238:239], v246 offset:992\n"
    "v_mfma_f32_16x16x32_bf16 a[148:151], v[224:227], v[148:151], a[148:151]\n"
    "v_mfma_f32_16x16x32_bf16 a[152:155], v[224:227], v[152:155], a[152:155]\n"
    "s_waitcnt lgkmcnt(4)\n"
    "v_mfma_f32_16x16x32_bf16 a[156:159], v[228:231], v[144:147], a[156:159]\n"
    "v_mfma_f32_16x16x32_bf16 a[160:163], v[228:231], v[148:151], a[160:163]\n"
    "v_mfma_f32_16x16x32_bf16 a[164:167], v[228:231], v[152:155], a[164:167]\n"
    "s_waitcnt lgkmcnt(2)\n"
    "v_mfma_f32_16x16x32_bf16 a[168:171], v[232:235], v[144:147], a[168:171]\n"
    "v_mfma_f32_16x16x32_bf16 a[172:175], v[232:235], v[148:151], a[172:175]\n"
    "v_mfma_f32_16x16x32_bf16 a[176:179], v[232:235], v[152:155], a[176:179]\n"
    "s_waitcnt lgkmcnt(0)\n"
    "v_mfma_f32_16x16x32_bf16 a[180:183], v[236:239], v[144:147], a[180:183]\n"
    "v_mfma_f32_16x16x32_bf16 a[184:187], v[236:239], v[148:151], a[184:187]\n"
    "v_mfma_f32_16x16x32_bf16 a[188:191], v[236:239], v[152:155], a[188:191]\n"
    "s_cmp_eq_u32 s71, 1\n"
    "s_cbranch_scc1 L_o1_%=\n"
    "s_waitcnt vmcnt(17)\n"
    "s_barrier\n"
    "v_add_u32_e64 v246, s52, v243\n"
    "s_add_u32 s52, s52, 16896\n"
    "s_cmp_eq_u32 s52, s53\n"
    "s_cselect_b32 s52, s79, s52\n"
    "v_add_u32_e64 v245, s52, v244\n"
    "s_add_u32 s52, s52, 16896\n"
    "s_cmp_eq_u32 s52, s53\n"
    "s_cselect_b32 s52, s79, s52\n"
    "ds_read_b64_tr_b16 v[224:225], v245 offset:0\n"
    "ds_read_b64_tr_b16 v[226:227], v245 offset:512\n"
    "ds_read_b64_tr_b16 v[228:229], v245 offset:8\n"
    "ds_read_b64_tr_b16 v[230:231], v245 offset:520\n"
    "ds_read_b64_tr_b16 v[232:233], v245 offset:64\n"
    "ds_read_b64_tr_b16 v[234:235], v245 offset:576\n"
    "ds_read_b128 v[192:195], v246 offset:0\n"
    "ds_read_b128 v[196:199], v246 offset:1056\n"
    "ds_read_b128 v[200:203], v246 offset:2112\n"
    "v_mov_b32_e64 v172, v168\n"
    "v_mov_b32_e64 v173, v169\n"
    "v_mov_b32_e64 v174, v170\n"
    "v_permlane16_swap_b32 v168, v172\n"
    "v_permlane16_swap_b32 v169, v173\n"
    "v_permlane16_swap_b32 v170, v174\n"
    "v_add_f32_e64 v168, v168, v172\n"
    "v_add_f32_e64 v169, v169, v173\n"
    "v_add_f32_e64 v170, v170, v174\n"
    "v_mov_b32_e64 v172, v168\n"
    "v_mov_b32_e64 v173, v169\n"
    "v_mov_b32_e64 v174, v170\n"
    "v_permlane32_swap_b32 v168, v172\n"
    "v_permlane32_swap_b32 v169, v173\n"
    "v_permlane32_swap_b32 v170, v174\n"
    "v_add_f32_e64 v168, v168, v172\n"
    "v_add_f32_e64 v169, v169, v173\n"
    "v_add_f32_e64 v170, v170, v174\n"
    "v_rcp_f32_e64 v247, v168\n"
    "v_rcp_f32_e64 v248, v169\n"
    "v_rcp_f32_e64 v249, v170\n"
    "s_mov_b32 s54, s88\n"
    "s_add_u32 s28, s28, 262144\n"
    "s_addc_u32 s29, s29, 0\n"
    "s_add_u32 s32, s32, 262144\n"
    "s_addc_u32 s33, s33, 0\n"
    "s_mov_b32 s71, 1\n"
    "s_branch L_qp_%=\n"
    "L_o1_%=:\n"
    "buffer_load_dword v181, v183, s[44:47], s60 offen sc1\n"
    "s_waitcnt vmcnt(18)\n"
    "s_barrier\n"
    "v_add_u32_e64 v245, s52, v255\n"
    "s_add_u32 s52, s52, 16896\n"
    "s_cmp_eq_u32 s52, s53\n"
    "s_cselect_b32 s52, s79, s52\n"
    "ds_read_b64_tr_b16 v[224:225], v245 offset:0\n"
    "ds_read_b64_tr_b16 v[226:227], v245 offset:224\n"
    "ds_read_b64_tr_b16 v[228:229], v245 offset:32\n"
    "ds_read_b64_tr_b16 v[230:231], v245 offset:256\n"
    "ds_read_b64_tr_b16 v[232:233], v245 offset:64\n"
    "ds_read_b64_tr_b16 v[234:235], v245 offset:288\n"
    "v_mov_b32_e64 v172, v168\n"
    "v_mov_b32_e64 v173, v169\n"
    "v_mov_b32_e64 v174, v170\n"
    "v_permlane16_swap_b32 v168, v172\n"
    "v_permlane16_swap_b32 v169, v173\n"
    "v_permlane16_swap_b32 v170, v174\n"
    "v_add_f32_e64 v168, v168, v172\n"
    "v_add_f32_e64 v169, v169, v173\n"
    "v_add_f32_e64 v170, v170, v174\n"
    "v_mov_b32_e64 v172, v168\n"
    "v_mov_b32_e64 v173, v169\n"
    "v_mov_b32_e64 v174, v170\n"
    "v_permlane32_swap_b32 v168, v172\n"
    "v_permlane32_swap_b32 v169, v173\n"
    "v_permlane32_swap_b32 v170, v174\n"
    "v_add_f32_e64 v168, v168, v172\n"
    "v_add_f32_e64 v169, v169, v173\n"
    "v_add_f32_e64 v170, v170, v174\n"
    "v_rcp_f32_e64 v174, v168\n"
    "v_rcp_f32_e64 v176, v169\n"
    "v_rcp_f32_e64 v178, v170\n"
    ".p2align 3\n"
    "v_accvgpr_read_b32 v96, a0\n"
    "v_accvgpr_read_b32 v97, a1\n"
    "v_accvgpr_read_b32 v98, a2\n"
    "v_accvgpr_read_b32 v99, a3\n"
    "v_accvgpr_read_b32 v100, a12\n"
    "v_accvgpr_read_b32 v101, a13\n"
    "v_accvgpr_read_b32 v102, a14\n"
    "v_accvgpr_read_b32 v103, a15\n"
    "v_pk_mul_f32 v[96:97], v[96:97], v[174:175] op_sel_hi:[1,0]\n"
    "v_pk_mul_f32 v[98:99], v[98:99], v[174:175] op_sel_hi:[1,0]\n"
    "v_pk_mul_f32 v[100:101], v[100:101], v[174:175] op_sel_hi:[1,0]\n"
    "v_pk_mul_f32 v[102:103], v[102:103], v[174:175] op_sel_hi:[1,0]\n"
    "v_cvt_pk_bf16_f32 v0, v96, v97\n"
    "v_cvt_pk_bf16_f32 v1, v98, v99\n"
    "v_cvt_pk_bf16_f32 v2, v100, v101\n"
    "v_cvt_pk_bf16_f32 v3, v102, v103\n"
    "v_accvgpr_read_b32 v104, a4\n"
    "v_accvgpr_read_b32 v105, a5\n"
    "v_accvgpr_read_b32 v106, a6\n"
    "v_accvgpr_read_b32 v107, a7\n"
    "v_accvgpr_read_b32 v108, a16\n"
    "v_accvgpr_read_b32 v109, a17\n"
    "v_accvgpr_read_b32 v110, a18\n"
    "v_accvgpr_read_b32 v111, a19\n"
    "v_pk_mul_f32 v[104:105], v[104:105], v[176:177] op_sel_hi:[1,0]\n"
    "v_pk_mul_f32 v[106:107], v[106:107], v[176:177] op_sel_hi:[1,0]\n"
    "v_pk_mul_f32 v[108:109], v[108:109], v[176:177] op_sel_hi:[1,0]\n"
    "v_pk_mul_f32 v[110:111], v[110:111], v[176:177] op_sel_hi:[1,0]\n"
    "v_cvt_pk_bf16_f32 v4, v104, v105\n"
    "v_cvt_pk_bf16_f32 v5, v106, v107\n"
    "v_cvt_pk_bf16_f32 v6, v108, v109\n"
    "v_cvt_pk_bf16_f32 v7, v110, v111\n"
    "v_accvgpr_read_b32 v112, a8\n"
    "v_accvgpr_read_b32 v113, a9\n"
    "v_accvgpr_read_b32 v114, a10\n"
    "v_accvgpr_read_b32 v115, a11\n"
    "v_accvgpr_read_b32 v116, a20\n"
    "v_accvgpr_read_b32 v117, a21\n"
    "v_accvgpr_read_b32 v118, a22\n"
    "v_accvgpr_read_b32 v119, a23\n"
    "v_pk_mul_f32 v[112:113], v[112:113], v[178:179] op_sel_hi:[1,0]\n"
    "v_pk_mul_f32 v[114:115], v[114:115], v[178:179] op_sel_hi:[1,0]\n"
    "v_pk_mul_f32 v[116:117], v[116:117], v[178:179] op_sel_hi:[1,0]\n"
    "v_pk_mul_f32 v[118:119], v[118:119], v[178:179] op_sel_hi:[1,0]\n"
    "v_cvt_pk_bf16_f32 v8, v112, v113\n"
    "v_cvt_pk_bf16_f32 v9, v114, v115\n"
    "v_cvt_pk_bf16_f32 v10, v116, v117\n"
    "v_cvt_pk_bf16_f32 v11, v118, v119\n"
    "v_accvgpr_read_b32 v120, a24\n"
    "v_accvgpr_read_b32 v121, a25\n"
    "v_accvgpr_read_b32 v122, a26\n"
    "v_accvgpr_read_b32 v123, a27\n"
    "v_accvgpr_read_b32 v124, a36\n"
    "v_accvgpr_read_b32 v125, a37\n"
    "v_accvgpr_read_b32 v126, a38\n"
    "v_accvgpr_read_b32 v127, a39\n"
    "v_pk_mul_f32 v[120:121], v[120:121], v[174:175] op_sel_hi:[1,0]\n"
    "v_pk_mul_f32 v[122:123], v[122:123], v[174:175] op_sel_hi:[1,0]\n"
    "v_pk_mul_f32 v[124:125], v[124:125], v[174:175] op_sel_hi:[1,0]\n"
    "v_pk_mul_f32 v[126:127], v[126:127], v[174:175] op_sel_hi:[1,0]\n"
    "v_cvt_pk_bf16_f32 v12, v120, v121\n"
    "v_cvt_pk_bf16_f32 v13, v122, v123\n"
    "v_cvt_pk_bf16_f32 v14, v124, v125\n"
    "v_cvt_pk_bf16_f32 v15, v126, v127\n"
    "v_accvgpr_read_b32 v96, a28\n"
    "v_accvgpr_read_b32 v97, a29\n"
    "v_accvgpr_read_b32 v98, a30\n"
    "v_accvgpr_read_b32 v99, a31\n"
    "v_accvgpr_read_b32 v100, a40\n"
    "v_accvgpr_read_b32 v101, a41\n"
    "v_accvgpr_read_b32 v102, a42\n"
    "v_accvgpr_read_b32 v103, a43\n"
    "v_pk_mul_f32 v[96:97], v[96:97], v[176:177] op_sel_hi:[1,0]\n"
    "v_pk_mul_f32 v[98:99], v[98:99], v[176:177] op_sel_hi:[1,0]\n"
    "v_pk_mul_f32 v[100:101], v[100:101], v[176:177] op_sel_hi:[1,0]\n"
    "v_pk_mul_f32 v[102:103], v[102:103], v[176:177] op_sel_hi:[1,0]\n"
    "v_cvt_pk_bf16_f32 v16, v96, v97\n"
    "v_cvt_pk_bf16_f32 v17, v98, v99\n"
    "v_cvt_pk_bf16_f32 v18, v100, v101\n"
    "v_cvt_pk_bf16_f32 v19, v102, v103\n"
    "v_accvgpr_read_b32 v104, a32\n"
    "v_accvgpr_read_b32 v105, a33\n"
    "v_accvgpr_read_b32 v106, a34\n"
    "v_accvgpr_read_b32 v107, a35\n"
    "v_accvgpr_read_b32 v108, a44\n"
    "v_accvgpr_read_b32 v109, a45\n"
    "v_accvgpr_read_b32 v110, a46\n"
    "v_accvgpr_read_b32 v111, a47\n"
    "v_pk_mul_f32 v[104:105], v[104:105], v[178:179] op_sel_hi:[1,0]\n"
    "v_pk_mul_f32 v[106:107], v[106:107], v[178:179] op_sel_hi:[1,0]\n"
    "v_pk_mul_f32 v[108:109], v[108:109], v[178:179] op_sel_hi:[1,0]\n"
    "v_pk_mul_f32 v[110:111], v[110:111], v[178:179] op_sel_hi:[1,0]\n"
    "v_cvt_pk_bf16_f32 v20, v104, v105\n"
    "v_cvt_pk_bf16_f32 v21, v106, v107\n"
    "v_cvt_pk_bf16_f32 v22, v108, v109\n"
    "v_cvt_pk_bf16_f32 v23, v110, v111\n"
    "v_accvgpr_read_b32 v112, a48\n"
    "v_accvgpr_read_b32 v113, a49\n"
    "v_accvgpr_read_b32 v114, a50\n"
    "v_accvgpr_read_b32 v115, a51\n"
    "v_accvgpr_read_b32 v116, a60\n"
    "v_accvgpr_read_b32 v117, a61\n"
    "v_accvgpr_read_b32 v118, a62\n"
    "v_accvgpr_read_b32 v119, a63\n"
    "v_pk_mul_f32 v[112:113], v[112:113], v[174:175] op_sel_hi:[1,0]\n"
    "v_pk_mul_f32 v[114:115], v[114:115], v[174:175] op_sel_hi:[1,0]\n"
    "v_pk_mul_f32 v[116:117], v[116:117], v[174:175] op_sel_hi:[1,0]\n"
    "v_pk_mul_f32 v[118:119], v[118:119], v[174:175] op_sel_hi:[1,0]\n"
    "v_cvt_pk_bf16_f32 v24, v112, v113\n"
    "v_cvt_pk_bf16_f32 v25, v114, v115\n"
    "v_cvt_pk_bf16_f32 v26, v116, v117\n"
    "v_cvt_pk_bf16_f32 v27, v118, v119\n"
    "v_accvgpr_read_b32 v120, a52\n"
    "v_accvgpr_read_b32 v121, a53\n"
    "v_accvgpr_read_b32 v122, a54\n"
    "v_accvgpr_read_b32 v123, a55\n"
    "v_accvgpr_read_b32 v124, a64\n"
    "v_accvgpr_read_b32 v125, a65\n"
    "v_accvgpr_read_b32 v126, a66\n"
    "v_accvgpr_read_b32 v127, a67\n"
    "v_pk_mul_f32 v[120:121], v[120:121], v[176:177] op_sel_hi:[1,0]\n"
    "v_pk_mul_f32 v[122:123], v[122:123], v[176:177] op_sel_hi:[1,0]\n"
    "v_pk_mul_f32 v[124:125], v[124:125], v[176:177] op_sel_hi:[1,0]\n"
    "v_pk_mul_f32 v[126:127], v[126:127], v[176:177] op_sel_hi:[1,0]\n"
    "v_cvt_pk_bf16_f32 v28, v120, v121\n"
    "v_cvt_pk_bf16_f32 v29, v122, v123\n"
    "v_cvt_pk_bf16_f32 v30, v124, v125\n"
    "v_cvt_pk_bf16_f32 v31, v126, v127\n"
    "v_accvgpr_read_b32 v96, a56\n"
    "v_accvgpr_read_b32 v97, a57\n"
    "v_accvgpr_read_b32 v98, a58\n"
    "v_accvgpr_read_b32 v99, a59\n"
    "v_accvgpr_read_b32 v100, a68\n"
    "v_accvgpr_read_b32 v101, a69\n"
    "v_accvgpr_read_b32 v102, a70\n"
    "v_accvgpr_read_b32 v103, a71\n"
    "v_pk_mul_f32 v[96:97], v[96:97], v[178:179] op_sel_hi:[1,0]\n"
    "v_pk_mul_f32 v[98:99], v[98:99], v[178:179] op_sel_hi:[1,0]\n"
    "v_pk_mul_f32 v[100:101], v[100:101], v[178:179] op_sel_hi:[1,0]\n"
    "v_pk_mul_f32 v[102:103], v[102:103], v[178:179] op_sel_hi:[1,0]\n"
    "v_cvt_pk_bf16_f32 v32, v96, v97\n"
    "v_cvt_pk_bf16_f32 v33, v98, v99\n"
    "v_cvt_pk_bf16_f32 v34, v100, v101\n"
    "v_cvt_pk_bf16_f32 v35, v102, v103\n"
    "v_accvgpr_read_b32 v104, a72\n"
    "v_accvgpr_read_b32 v105, a73\n"
    "v_accvgpr_read_b32 v106, a74\n"
    "v_accvgpr_read_b32 v107, a75\n"
    "v_accvgpr_read_b32 v108, a84\n"
    "v_accvgpr_read_b32 v109, a85\n"
    "v_accvgpr_read_b32 v110, a86\n"
    "v_accvgpr_read_b32 v111, a87\n"
    "v_pk_mul_f32 v[104:105], v[104:105], v[174:175] op_sel_hi:[1,0]\n"
    "v_pk_mul_f32 v[106:107], v[106:107], v[174:175] op_sel_hi:[1,0]\n"
    "v_pk_mul_f32 v[108:109], v[108:109], v[174:175] op_sel_hi:[1,0]\n"
    "v_pk_mul_f32 v[110:111], v[110:111], v[174:175] op_sel_hi:[1,0]\n"
    "v_cvt_pk_bf16_f32 v36, v104, v105\n"
    "v_cvt_pk_bf16_f32 v37, v106, v107\n"
    "v_cvt_pk_bf16_f32 v38, v108, v109\n"
    "v_cvt_pk_bf16_f32 v39, v110, v111\n"
    "v_accvgpr_read_b32 v112, a76\n"
    "v_accvgpr_read_b32 v113, a77\n"
    "v_accvgpr_read_b32 v114, a78\n"
    "v_accvgpr_read_b32 v115, a79\n"
    "v_accvgpr_read_b32 v116, a88\n"
    "v_accvgpr_read_b32 v117, a89\n"
    "v_accvgpr_read_b32 v118, a90\n"
    "v_accvgpr_read_b32 v119, a91\n"
    "v_pk_mul_f32 v[112:113], v[112:113], v[176:177] op_sel_hi:[1,0]\n"
    "v_pk_mul_f32 v[114:115], v[114:115], v[176:177] op_sel_hi:[1,0]\n"
    "v_pk_mul_f32 v[116:117], v[116:117], v[176:177] op_sel_hi:[1,0]\n"
    "v_pk_mul_f32 v[118:119], v[118:119], v[176:177] op_sel_hi:[1,0]\n"
    "v_cvt_pk_bf16_f32 v40, v112, v113\n"
    "v_cvt_pk_bf16_f32 v41, v114, v115\n"
    "v_cvt_pk_bf16_f32 v42, v116, v117\n"
    "v_cvt_pk_bf16_f32 v43, v118, v119\n"
    "v_accvgpr_read_b32 v120, a80\n"
    "v_accvgpr_read_b32 v121, a81\n"
    "v_accvgpr_read_b32 v122, a82\n"
    "v_accvgpr_read_b32 v123, a83\n"
    "v_accvgpr_read_b32 v124, a92\n"
    "v_accvgpr_read_b32 v125, a93\n"
    "v_accvgpr_read_b32 v126, a94\n"
    "v_accvgpr_read_b32 v127, a95\n"
    "v_pk_mul_f32 v[120:121], v[120:121], v[178:179] op_sel_hi:[1,0]\n"
    "v_pk_mul_f32 v[122:123], v[122:123], v[178:179] op_sel_hi:[1,0]\n"
    "v_pk_mul_f32 v[124:125], v[124:125], v[178:179] op_sel_hi:[1,0]\n"
    "v_pk_mul_f32 v[126:127], v[126:127], v[178:179] op_sel_hi:[1,0]\n"
    "v_cvt_pk_bf16_f32 v44, v120, v121\n"
    "v_cvt_pk_bf16_f32 v45, v122, v123\n"
    "v_cvt_pk_bf16_f32 v46, v124, v125\n"
    "v_cvt_pk_bf16_f32 v47, v126, v127\n"
    "v_accvgpr_read_b32 v96, a96\n"
    "v_accvgpr_read_b32 v97, a97\n"
    "v_accvgpr_read_b32 v98, a98\n"
    "v_accvgpr_read_b32 v99, a99\n"
    "v_accvgpr_read_b32 v100, a108\n"
    "v_accvgpr_read_b32 v101, a109\n"
    "v_accvgpr_read_b32 v102, a110\n"
    "v_accvgpr_read_b32 v103, a111\n"
    "v_pk_mul_f32 v[96:97], v[96:97], v[174:175] op_sel_hi:[1,0]\n"
    "v_pk_mul_f32 v[98:99], v[98:99], v[174:175] op_sel_hi:[1,0]\n"
    "v_pk_mul_f32 v[100:101], v[100:101], v[174:175] op_sel_hi:[1,0]\n"
    "v_pk_mul_f32 v[102:103], v[102:103], v[174:175] op_sel_hi:[1,0]\n"
    "v_cvt_pk_bf16_f32 v48, v96, v97\n"
    "v_cvt_pk_bf16_f32 v49, v98, v99\n"
    "v_cvt_pk_bf16_f32 v50, v100, v101\n"
    "v_cvt_pk_bf16_f32 v51, v102, v103\n"
    "v_accvgpr_read_b32 v104, a100\n"
    "v_accvgpr_read_b32 v105, a101\n"
    "v_accvgpr_read_b32 v106, a102\n"
    "v_accvgpr_read_b32 v107, a103\n"
    "v_accvgpr_read_b32 v108, a112\n"
    "v_accvgpr_read_b32 v109, a113\n"
    "v_accvgpr_read_b32 v110, a114\n"
    "v_accvgpr_read_b32 v111, a115\n"
    "v_pk_mul_f32 v[104:105], v[104:105], v[176:177] op_sel_hi:[1,0]\n"
    "v_pk_mul_f32 v[106:107], v[106:107], v[176:177] op_sel_hi:[1,0]\n"
    "v_pk_mul_f32 v[108:109], v[108:109], v[176:177] op_sel_hi:[1,0]\n"
    "v_pk_mul_f32 v[110:111], v[110:111], v[176:177] op_sel_hi:[1,0]\n"
    "v_cvt_pk_bf16_f32 v52, v104, v105\n"
    "v_cvt_pk_bf16_f32 v53, v106, v107\n"
    "v_cvt_pk_bf16_f32 v54, v108, v109\n"
    "v_cvt_pk_bf16_f32 v55, v110, v111\n"
    "v_accvgpr_read_b32 v112, a104\n"
    "v_accvgpr_read_b32 v113, a105\n"
    "v_accvgpr_read_b32 v114, a106\n"
    "v_accvgpr_read_b32 v115, a107\n"
    "v_accvgpr_read_b32 v116, a116\n"
    "v_accvgpr_read_b32 v117, a117\n"
    "v_accvgpr_read_b32 v118, a118\n"
    "v_accvgpr_read_b32 v119, a119\n"
    "v_pk_mul_f32 v[112:113], v[112:113], v[178:179] op_sel_hi:[1,0]\n"
    "v_pk_mul_f32 v[114:115], v[114:115], v[178:179] op_sel_hi:[1,0]\n"
    "v_pk_mul_f32 v[116:117], v[116:117], v[178:179] op_sel_hi:[1,0]\n"
    "v_pk_mul_f32 v[118:119], v[118:119], v[178:179] op_sel_hi:[1,0]\n"
    "v_cvt_pk_bf16_f32 v56, v112, v113\n"
    "v_cvt_pk_bf16_f32 v57, v114, v115\n"
    "v_cvt_pk_bf16_f32 v58, v116, v117\n"
    "v_cvt_pk_bf16_f32 v59, v118, v119\n"
    "v_accvgpr_read_b32 v120, a120\n"
    "v_accvgpr_read_b32 v121, a121\n"
    "v_accvgpr_read_b32 v122, a122\n"
    "v_accvgpr_read_b32 v123, a123\n"
    "v_accvgpr_read_b32 v124, a132\n"
    "v_accvgpr_read_b32 v125, a133\n"
    "v_accvgpr_read_b32 v126, a134\n"
    "v_accvgpr_read_b32 v127, a135\n"
    "v_pk_mul_f32 v[120:121], v[120:121], v[174:175] op_sel_hi:[1,0]\n"
    "v_pk_mul_f32 v[122:123], v[122:123], v[174:175] op_sel_hi:[1,0]\n"
    "v_pk_mul_f32 v[124:125], v[124:125], v[174:175] op_sel_hi:[1,0]\n"
    "v_pk_mul_f32 v[126:127], v[126:127], v[174:175] op_sel_hi:[1,0]\n"
    "v_cvt_pk_bf16_f32 v60, v120, v121\n"
    "v_cvt_pk_bf16_f32 v61, v122, v123\n"
    "v_cvt_pk_bf16_f32 v62, v124, v125\n"
    "v_cvt_pk_bf16_f32 v63, v126, v127\n"
    "v_accvgpr_read_b32 v96, a124\n"
    "v_accvgpr_read_b32 v97, a125\n"
    "v_accvgpr_read_b32 v98, a126\n"
    "v_accvgpr_read_b32 v99, a127\n"
    "v_accvgpr_read_b32 v100, a136\n"
    "v_accvgpr_read_b32 v101, a137\n"
    "v_accvgpr_read_b32 v102, a138\n"
    "v_accvgpr_read_b32 v103, a139\n"
    "v_pk_mul_f32 v[96:97], v[96:97], v[176:177] op_sel_hi:[1,0]\n"
    "v_pk_mul_f32 v[98:99], v[98:99], v[176:177] op_sel_hi:[1,0]\n"
    "v_pk_mul_f32 v[100:101], v[100:101], v[176:177] op_sel_hi:[1,0]\n"
    "v_pk_mul_f32 v[102:103], v[102:103], v[176:177] op_sel_hi:[1,0]\n"
    "v_cvt_pk_bf16_f32 v64, v96, v97\n"
    "v_cvt_pk_bf16_f32 v65, v98, v99\n"
    "v_cvt_pk_bf16_f32 v66, v100, v101\n"
    "v_cvt_pk_bf16_f32 v67, v102, v103\n"
    "v_accvgpr_read_b32 v104, a128\n"
    "v_accvgpr_read_b32 v105, a129\n"
    "v_accvgpr_read_b32 v106, a130\n"
    "v_accvgpr_read_b32 v107, a131\n"
    "v_accvgpr_read_b32 v108, a140\n"
    "v_accvgpr_read_b32 v109, a141\n"
    "v_accvgpr_read_b32 v110, a142\n"
    "v_accvgpr_read_b32 v111, a143\n"
    "v_pk_mul_f32 v[104:105], v[104:105], v[178:179] op_sel_hi:[1,0]\n"
    "v_pk_mul_f32 v[106:107], v[106:107], v[178:179] op_sel_hi:[1,0]\n"
    "v_pk_mul_f32 v[108:109], v[108:109], v[178:179] op_sel_hi:[1,0]\n"
    "v_pk_mul_f32 v[110:111], v[110:111], v[178:179] op_sel_hi:[1,0]\n"
    "v_cvt_pk_bf16_f32 v68, v104, v105\n"
    "v_cvt_pk_bf16_f32 v69, v106, v107\n"
    "v_cvt_pk_bf16_f32 v70, v108, v109\n"
    "v_cvt_pk_bf16_f32 v71, v110, v111\n"
    "v_accvgpr_read_b32 v112, a144\n"
    "v_accvgpr_read_b32 v113, a145\n"
    "v_accvgpr_read_b32 v114, a146\n"
    "v_accvgpr_read_b32 v115, a147\n"
    "v_accvgpr_read_b32 v116, a156\n"
    "v_accvgpr_read_b32 v117, a157\n"
    "v_accvgpr_read_b32 v118, a158\n"
    "v_accvgpr_read_b32 v119, a159\n"
    "v_pk_mul_f32 v[112:113], v[112:113], v[174:175] op_sel_hi:[1,0]\n"
    "v_pk_mul_f32 v[114:115], v[114:115], v[174:175] op_sel_hi:[1,0]\n"
    "v_pk_mul_f32 v[116:117], v[116:117], v[174:175] op_sel_hi:[1,0]\n"
    "v_pk_mul_f32 v[118:119], v[118:119], v[174:175] op_sel_hi:[1,0]\n"
    "v_cvt_pk_bf16_f32 v72, v112, v113\n"
    "v_cvt_pk_bf16_f32 v73, v114, v115\n"
    "v_cvt_pk_bf16_f32 v74, v116, v117\n"
    "v_cvt_pk_bf16_f32 v75, v118, v119\n"
    "v_accvgpr_read_b32 v120, a148\n"
    "v_accvgpr_read_b32 v121, a149\n"
    "v_accvgpr_read_b32 v122, a150\n"
    "v_accvgpr_read_b32 v123, a151\n"
    "v_accvgpr_read_b32 v124, a160\n"
    "v_accvgpr_read_b32 v125, a161\n"
    "v_accvgpr_read_b32 v126, a162\n"
    "v_accvgpr_read_b32 v127, a163\n"
    "v_pk_mul_f32 v[120:121], v[120:121], v[176:177] op_sel_hi:[1,0]\n"
    "v_pk_mul_f32 v[122:123], v[122:123], v[176:177] op_sel_hi:[1,0]\n"
    "v_pk_mul_f32 v[124:125], v[124:125], v[176:177] op_sel_hi:[1,0]\n"
    "v_pk_mul_f32 v[126:127], v[126:127], v[176:177] op_sel_hi:[1,0]\n"
    "v_cvt_pk_bf16_f32 v76, v120, v121\n"
    "v_cvt_pk_bf16_f32 v77, v122, v123\n"
    "v_cvt_pk_bf16_f32 v78, v124, v125\n"
    "v_cvt_pk_bf16_f32 v79, v126, v127\n"
    "v_accvgpr_read_b32 v96, a152\n"
    "v_accvgpr_read_b32 v97, a153\n"
    "v_accvgpr_read_b32 v98, a154\n"
    "v_accvgpr_read_b32 v99, a155\n"
    "v_accvgpr_read_b32 v100, a164\n"
    "v_accvgpr_read_b32 v101, a165\n"
    "v_accvgpr_read_b32 v102, a166\n"
    "v_accvgpr_read_b32 v103, a167\n"
    "v_pk_mul_f32 v[96:97], v[96:97], v[178:179] op_sel_hi:[1,0]\n"
    "v_pk_mul_f32 v[98:99], v[98:99], v[178:179] op_sel_hi:[1,0]\n"
    "v_pk_mul_f32 v[100:101], v[100:101], v[178:179] op_sel_hi:[1,0]\n"
    "v_pk_mul_f32 v[102:103], v[102:103], v[178:179] op_sel_hi:[1,0]\n"
    "v_cvt_pk_bf16_f32 v80, v96, v97\n"
    "v_cvt_pk_bf16_f32 v81, v98, v99\n"
    "v_cvt_pk_bf16_f32 v82, v100, v101\n"
    "v_cvt_pk_bf16_f32 v83, v102, v103\n"
    "v_accvgpr_read_b32 v104, a168\n"
    "v_accvgpr_read_b32 v105, a169\n"
    "v_accvgpr_read_b32 v106, a170\n"
    "v_accvgpr_read_b32 v107, a171\n"
    "v_accvgpr_read_b32 v108, a180\n"
    "v_accvgpr_read_b32 v109, a181\n"
    "v_accvgpr_read_b32 v110, a182\n"
    "v_accvgpr_read_b32 v111, a183\n"
    "v_pk_mul_f32 v[104:105], v[104:105], v[174:175] op_sel_hi:[1,0]\n"
    "v_pk_mul_f32 v[106:107], v[106:107], v[174:175] op_sel_hi:[1,0]\n"
    "v_pk_mul_f32 v[108:109], v[108:109], v[174:175] op_sel_hi:[1,0]\n"
    "v_pk_mul_f32 v[110:111], v[110:111], v[174:175] op_sel_hi:[1,0]\n"
    "v_cvt_pk_bf16_f32 v84, v104, v105\n"
    "v_cvt_pk_bf16_f32 v85, v106, v107\n"
    "v_cvt_pk_bf16_f32 v86, v108, v109\n"
    "v_cvt_pk_bf16_f32 v87, v110, v111\n"
    "v_accvgpr_read_b32 v112, a172\n"
    "v_accvgpr_read_b32 v113, a173\n"
    "v_accvgpr_read_b32 v114, a174\n"
    "v_accvgpr_read_b32 v115, a175\n"
    "v_accvgpr_read_b32 v116, a184\n"
    "v_accvgpr_read_b32 v117, a185\n"
    "v_accvgpr_read_b32 v118, a186\n"
    "v_accvgpr_read_b32 v119, a187\n"
    "v_pk_mul_f32 v[112:113], v[112:113], v[176:177] op_sel_hi:[1,0]\n"
    "v_pk_mul_f32 v[114:115], v[114:115], v[176:177] op_sel_hi:[1,0]\n"
    "v_pk_mul_f32 v[116:117], v[116:117], v[176:177] op_sel_hi:[1,0]\n"
    "v_pk_mul_f32 v[118:119], v[118:119], v[176:177] op_sel_hi:[1,0]\n"
    "v_cvt_pk_bf16_f32 v88, v112, v113\n"
    "v_cvt_pk_bf16_f32 v89, v114, v115\n"
    "v_cvt_pk_bf16_f32 v90, v116, v117\n"
    "v_cvt_pk_bf16_f32 v91, v118, v119\n"
    "v_accvgpr_read_b32 v120, a176\n"
    "v_accvgpr_read_b32 v121, a177\n"
    "v_accvgpr_read_b32 v122, a178\n"
    "v_accvgpr_read_b32 v123, a179\n"
    "v_accvgpr_read_b32 v124, a188\n"
    "v_accvgpr_read_b32 v125, a189\n"
    "v_accvgpr_read_b32 v126, a190\n"
    "v_accvgpr_read_b32 v127, a191\n"
    "v_pk_mul_f32 v[120:121], v[120:121], v[178:179] op_sel_hi:[1,0]\n"
    "v_pk_mul_f32 v[122:123], v[122:123], v[178:179] op_sel_hi:[1,0]\n"
    "v_pk_mul_f32 v[124:125], v[124:125], v[178:179] op_sel_hi:[1,0]\n"
    "v_pk_mul_f32 v[126:127], v[126:127], v[178:179] op_sel_hi:[1,0]\n"
    "v_cvt_pk_bf16_f32 v92, v120, v121\n"
    "v_cvt_pk_bf16_f32 v93, v122, v123\n"
    "v_cvt_pk_bf16_f32 v94, v124, v125\n"
    "v_cvt_pk_bf16_f32 v95, v126, v127\n"
    "v_mbcnt_lo_u32_b32 v241, -1, 0\n"
    "v_mbcnt_hi_u32_b32 v241, -1, v241\n"
    "v_lshlrev_b32 v240, 4, v241\n"
    "v_lshlrev_b32 v241, 3, v241\n"
    "s_mov_b32 s72, s57\n"
    "s_mov_b32 s70, 4\n"
    "s_mov_b32 s42, 0\n"
    "s_mul_i32 s73, s48, 43008\n"
    "s_sub_u32 s73, s73, 10752\n"
    "s_waitcnt lgkmcnt(4)\n"
    "L_outp_%=:\n"
    "v_mfma_f32_16x16x32_bf16 v[96:99], v[224:227], a[192:195], 0\n"
    "s_mov_b32 m0, s49\n"
    "ds_read_b64_tr_b16 v[236:237], v245 offset:96\n"
    "ds_read_b64_tr_b16 v[238:239], v245 offset:320\n"
    "v_mfma_f32_16x16x32_bf16 v[100:103], v[224:227], a[196:199], 0\n"
    "v_mfma_f32_16x16x32_bf16 v[104:107], v[224:227], a[200:203], 0\n"
    "s_waitcnt lgkmcnt(4)\n"
    "v_cvt_pk_bf16_f32 v252, v164, v165\n"
    "v_cvt_pk_bf16_f32 v253, v166, v167\n"
    "v_mfma_f32_16x16x32_bf16 v[108:111], v[228:231], a[192:195], 0\n"
    "ds_read_b64_tr_b16 v[224:225], v245 offset:128\n"
    "ds_read_b64_tr_b16 v[226:227], v245 offset:352\n"
    "v_mfma_f32_16x16x32_bf16 v[112:115], v[228:231], a[196:199], 0\n"
    "buffer_load_dwordx4 v190, s[36:39], s72 offen offset:0 lds\n"
    "v_mfma_f32_16x16x32_bf16 v[116:119], v[228:231], a[200:203], 0\n"
    "s_waitcnt lgkmcnt(4)\n"
    "v_mfma_f32_16x16x32_bf16 v[120:123], v[232:235], a[192:195], 0\n"
    "s_add_u32 m0, s49, 160\n"
    "ds_read_b64_tr_b16 v[228:229], v245 offset:160\n"
    "ds_read_b64_tr_b16 v[230:231], v245 offset:384\n"
    "v_mfma_f32_16x16x32_bf16 v[124:127], v[232:235], a[196:199], 0\n"
    "v_accvgpr_write_b32 a34, v252\n"
    "v_accvgpr_write_b32 a35, v253\n"
    "v_mfma_f32_16x16x32_bf16 v[128:131], v[232:235], a[200:203], 0\n"
    "s_waitcnt lgkmcnt(4)\n"
    "v_cvt_pk_bf16_f32 v250, v168, v169\n"
    "v_cvt_pk_bf16_f32 v251, v170, v171\n"
    "v_mfma_f32_16x16x32_bf16 v[132:135], v[236:239], a[192:195], 0\n"
    "ds_read_b64_tr_b16 v[232:233], v245 offset:192\n"
    "ds_read_b64_tr_b16 v[234:235], v245 offset:416\n"
    "v_mfma_f32_16x16x32_bf16 v[136:139], v[236:239], a[196:199], 0\n"
    "v_accvgpr_write_b32 a36, v250\n"
    "v_accvgpr_write_b32 a37, v251\n"
    "v_mfma_f32_16x16x32_bf16 v[140:143], v[236:239], a[200:203], 0\n"
    "s_waitcnt lgkmcnt(4)\n"
    "v_cvt_pk_bf16_f32 v252, v172, v173\n"
    "v_cvt_pk_bf16_f32 v253, v174, v175\n"
    "v_mfma_f32_16x16x32_bf16 v[144:147], v[224:227], a[192:195], 0\n"
    "ds_read_b64_tr_b16 v[236:237], v245 offset:448\n"
    "ds_read_b64_tr_b16 v[238:239], v245 offset:672\n"
    "v_mfma_f32_16x16x32_bf16 v[148:151], v[224:227], a[196:199], 0\n"
    "buffer_load_dwordx4 v190, s[36:39], s72 offen offset:896 lds\n"
    "v_mfma_f32_16x16x32_bf16 v[152:155], v[224:227], a[200:203], 0\n"
    "s_waitcnt lgkmcnt(4)\n"
    "v_mfma_f32_16x16x32_bf16 v[156:159], v[228:231], a[192:195], 0\n"
    "s_add_u32 m0, s49, 320\n"
    "ds_read_b64_tr_b16 v[224:225], v245 offset:480\n"
    "ds_read_b64_tr_b16 v[226:227], v245 offset:704\n"
    "v_mfma_f32_16x16x32_bf16 v[160:163], v[228:231], a[196:199], 0\n"
    "v_accvgpr_write_b32 a38, v252\n"
    "v_accvgpr_write_b32 a39, v253\n"
    "v_mfma_f32_16x16x32_bf16 v[164:167], v[228:231], a[200:203], 0\n"
    "s_waitcnt lgkmcnt(4)\n"
    "v_cvt_pk_bf16_f32 v250, v176, v177\n"
    "v_cvt_pk_bf16_f32 v251, v178, v179\n"
    "v_mfma_f32_16x16x32_bf16 v[168:171], v[232:235], a[192:195], 0\n"
    "ds_read_b64_tr_b16 v[228:229], v245 offset:512\n"
    "ds_read_b64_tr_b16 v[230:231], v245 offset:736\n"
    "v_mfma_f32_16x16x32_bf16 v[172:175], v[232:235], a[196:199], 0\n"
    "v_accvgpr_write_b32 a40, v250\n"
    "v_accvgpr_write_b32 a41, v251\n"
    "v_mfma_f32_16x16x32_bf16 v[176:179], v[232:235], a[200:203], 0\n"
    "s_waitcnt lgkmcnt(4)\n"
    "v_mfma_f32_16x16x32_bf16 v[96:99], v[236:239], a[204:207], v[96:99]\n"
    "ds_read_b64_tr_b16 v[232:233], v245 offset:544\n"
    "ds_read_b64_tr_b16 v[234:235], v245 offset:768\n"
    "v_mfma_f32_16x16x32_bf16 v[100:103], v[236:239], a[208:211], v[100:103]\n"
    "buffer_load_dwordx4 v190, s[36:39], s72 offen offset:1792 lds\n"
    "v_mfma_f32_16x16x32_bf16 v[104:107], v[236:239], a[212:215], v[104:107]\n"
    "s_waitcnt lgkmcnt(4)\n"
    "v_mfma_f32_16x16x32_bf16 v[108:111], v[224:227], a[204:207], v[108:111]\n"
    "s_add_u32 m0, s49, 480\n"
    "ds_read_b64_tr_b16 v[236:237], v245 offset:576\n"
    "ds_read_b64_tr_b16 v[238:239], v245 offset:800\n"
    "v_mfma_f32_16x16x32_bf16 v[112:115], v[224:227], a[208:211], v[112:115]\n"
    "v_add_u32_e64 v246, s52, v255\n"
    "v_mfma_f32_16x16x32_bf16 v[116:119], v[224:227], a[212:215], v[116:119]\n"
    "s_waitcnt lgkmcnt(4)\n"
    "v_mfma_f32_16x16x32_bf16 v[120:123], v[228:231], a[204:207], v[120:123]\n"
    "s_add_u32 s52, s52, 16896\n"
    "ds_read_b64_tr_b16 v[224:225], v245 offset:608\n"
    "ds_read_b64_tr_b16 v[226:227], v245 offset:832\n"
    "v_mfma_f32_16x16x32_bf16 v[124:127], v[228:231], a[208:211], v[124:127]\n"
    "s_cmp_eq_u32 s52, s53\n"
    "buffer_store_dwordx4 a[0:3], v240, s[40:43], s73 offen offset:0 sc1\n"
    "v_mfma_f32_16x16x32_bf16 v[128:131], v[228:231], a[212:215], v[128:131]\n"
    "s_waitcnt lgkmcnt(4)\n"
    "v_mfma_f32_16x16x32_bf16 v[132:135], v[232:235], a[204:207], v[132:135]\n"
    "s_cselect_b32 s52, s79, s52\n"
    "ds_read_b64_tr_b16 v[228:229], v245 offset:640\n"
    "ds_read_b64_tr_b16 v[230:231], v245 offset:864\n"
    "v_mfma_f32_16x16x32_bf16 v[136:139], v[232:235], a[208:211], v[136:139]\n"
    "buffer_load_dwordx4 v190, s[36:39], s72 offen offset:2688 lds\n"
    "v_mfma_f32_16x16x32_bf16 v[140:143], v[232:235], a[212:215], v[140:143]\n"
    "s_waitcnt lgkmcnt(4)\n"
    "v_mfma_f32_16x16x32_bf16 v[144:147], v[236:239], a[204:207], v[144:147]\n"
    "s_add_u32 s72, s72, 57344\n"
    "ds_read_b64_tr_b16 v[232:233], v246 offset:0\n"
    "ds_read_b64_tr_b16 v[234:235], v246 offset:224\n"
    "v_mfma_f32_16x16x32_bf16 v[148:151], v[236:239], a[208:211], v[148:151]\n"
    "s_add_u32 s49, s49, 16896\n"
    "v_mfma_f32_16x16x32_bf16 v[152:155], v[236:239], a[212:215], v[152:155]\n"
    "s_waitcnt lgkmcnt(4)\n"
    "v_mfma_f32_16x16x32_bf16 v[156:159], v[224:227], a[204:207], v[156:159]\n"
    "s_cmp_eq_u32 s49, s50\n"
    "ds_read_b64_tr_b16 v[236:237], v246 offset:32\n"
    "ds_read_b64_tr_b16 v[238:239], v246 offset:256\n"
    "v_mfma_f32_16x16x32_bf16 v[160:163], v[224:227], a[208:211], v[160:163]\n"
    "s_cselect_b32 s49, s51, s49\n"
    "v_mfma_f32_16x16x32_bf16 v[164:167], v[224:227], a[212:215], v[164:167]\n"
    "s_waitcnt lgkmcnt(4)\n"
    "v_mfma_f32_16x16x32_bf16 v[168:171], v[228:231], a[204:207], v[168:171]\n"
    "s_mov_b32 m0, s49\n"
    "ds_read_b64_tr_b16 v[224:225], v246 offset:64\n"
    "ds_read_b64_tr_b16 v[226:227], v246 offset:288\n"
    "v_mfma_f32_16x16x32_bf16 v[172:175], v[228:231], a[208:211], v[172:175]\n"
    "v_mfma_f32_16x16x32_bf16 v[176:179], v[228:231], a[212:215], v[176:179]\n"
    "s_waitcnt lgkmcnt(4)\n"
    "v_mfma_f32_16x16x32_bf16 v[96:99], v[232:235], a[216:219], v[96:99]\n"
    "ds_read_b64_tr_b16 v[228:229], v246 offset:96\n"
    "ds_read_b64_tr_b16 v[230:231], v246 offset:320\n"
    "v_mfma_f32_16x16x32_bf16 v[100:103], v[232:235], a[220:223], v[100:103]\n"
    "buffer_load_dwordx4 v190, s[36:39], s72 offen offset:0 lds\n"
    "v_mfma_f32_16x16x32_bf16 v[104:107], v[232:235], a[224:227], v[104:107]\n"
    "s_waitcnt lgkmcnt(4)\n"
    "v_mfma_f32_16x16x32_bf16 v[108:111], v[236:239], a[216:219], v[108:111]\n"
    "s_add_u32 m0, s49, 160\n"
    "ds_read_b64_tr_b16 v[232:233], v246 offset:128\n"
    "ds_read_b64_tr_b16 v[234:235], v246 offset:352\n"
    "v_mfma_f32_16x16x32_bf16 v[112:115], v[236:239], a[220:223], v[112:115]\n"
    "buffer_store_dwordx4 a[4:7], v240, s[40:43], s73 offen offset:1024 sc1\n"
    "v_mfma_f32_16x16x32_bf16 v[116:119], v[236:239], a[224:227], v[116:119]\n"
    "s_waitcnt lgkmcnt(4)\n"
    "v_mfma_f32_16x16x32_bf16 v[120:123], v[224:227], a[216:219], v[120:123]\n"
    "ds_read_b64_tr_b16 v[236:237], v246 offset:160\n"
    "ds_read_b64_tr_b16 v[238:239], v246 offset:384\n"
    "v_mfma_f32_16x16x32_bf16 v[124:127], v[224:227], a[220:223], v[124:127]\n"
    "v_mfma_f32_16x16x32_bf16 v[128:131], v[224:227], a[224:227], v[128:131]\n"
    "s_waitcnt lgkmcnt(4)\n"
    "v_mfma_f32_16x16x32_bf16 v[132:135], v[228:231], a[216:219], v[132:135]\n"
    "ds_read_b64_tr_b16 v[224:225], v246 offset:192\n"
    "ds_read_b64_tr_b16 v[226:227], v246 offset:416\n"
    "v_mfma_f32_16x16x32_bf16 v[136:139], v[228:231], a[220:223], v[136:139]\n"
    "buffer_load_dwordx4 v190, s[36:39], s72 offen offset:896 lds\n"
    "v_mfma_f32_16x16x32_bf16 v[140:143], v[228:231], a[224:227], v[140:143]\n"
    "s_waitcnt lgkmcnt(4)\n"
    "v_mfma_f32_16x16x32_bf16 v[144:147], v[232:235], a[216:219], v[144:147]\n"
    "s_add_u32 m0, s49, 320\n"
    "ds_read_b64_tr_b16 v[228:229], v246 offset:448\n"
    "ds_read_b64_tr_b16 v[230:231], v246 offset:672\n"
    "v_mfma_f32_16x16x32_bf16 v[148:151], v[232:235], a[220:223], v[148:151]\n"
    "v_mfma_f32_16x16x32_bf16 v[152:155], v[232:235], a[224:227], v[152:155]\n"
    "s_waitcnt lgkmcnt(4)\n"
    "v_mfma_f32_16x16x32_bf16 v[156:159], v[236:239], a[216:219], v[156:159]\n"
    "ds_read_b64_tr_b16 v[232:233], v246 offset:480\n"
    "ds_read_b64_tr_b16 v[234:235], v246 offset:704\n"
    "v_mfma_f32_16x16x32_bf16 v[160:163], v[236:239], a[220:223], v[160:163]\n"
    "v_mfma_f32_16x16x32_bf16 v[164:167], v[236:239], a[224:227], v[164:167]\n"
    "s_waitcnt lgkmcnt(4)\n"
    "v_mfma_f32_16x16x32_bf16 v[168:171], v[224:227], a[216:219], v[168:171]\n"
    "ds_read_b64_tr_b16 v[236:237], v246 offset:512\n"
    "ds_read_b64_tr_b16 v[238:239], v246 offset:736\n"
    "v_mfma_f32_16x16x32_bf16 v[172:175], v[224:227], a[220:223], v[172:175]\n"
    "buffer_load_dwordx4 v190, s[36:39], s72 offen offset:1792 lds\n"
    "v_mfma_f32_16x16x32_bf16 v[176:179], v[224:227], a[224:227], v[176:179]\n"
    "s_waitcnt lgkmcnt(4)\n"
    "v_mfma_f32_16x16x32_bf16 v[96:99], v[228:231], a[228:231], v[96:99]\n"
    "s_add_u32 m0, s49, 480\n"
    "ds_read_b64_tr_b16 v[224:225], v246 offset:544\n"
    "ds_read_b64_tr_b16 v[226:227], v246 offset:768\n"
    "v_mfma_f32_16x16x32_bf16 v[100:103], v[228:231], a[232:235], v[100:103]\n"
    "v_add_u32_e64 v245, s52, v255\n"
    "v_mfma_f32_16x16x32_bf16 v[104:107], v[228:231], a[236:239], v[104:107]\n"
    "s_waitcnt lgkmcnt(4)\n"
    "v_mfma_f32_16x16x32_bf16 v[108:111], v[232:235], a[228:231], v[108:111]\n"
    "s_add_u32 s52, s52, 16896\n"
    "ds_read_b64_tr_b16 v[228:229], v246 offset:576\n"
    "ds_read_b64_tr_b16 v[230:231], v246 offset:800\n"
    "v_mfma_f32_16x16x32_bf16 v[112:115], v[232:235], a[232:235], v[112:115]\n"
    "s_cmp_eq_u32 s52, s53\n"
    "buffer_store_dwordx4 a[8:11], v240, s[40:43], s73 offen offset:2048 sc1\n"
    "v_mfma_f32_16x16x32_bf16 v[116:119], v[232:235], a[236:239], v[116:119]\n"
    "s_waitcnt lgkmcnt(4)\n"
    "v_mfma_f32_16x16x32_bf16 v[120:123], v[236:239], a[228:231], v[120:123]\n"
    "s_cselect_b32 s52, s79, s52\n"
    "ds_read_b64_tr_b16 v[232:233], v246 offset:608\n"
    "ds_read_b64_tr_b16 v[234:235], v246 offset:832\n"
    "v_mfma_f32_16x16x32_bf16 v[124:127], v[236:239], a[232:235], v[124:127]\n"
    "buffer_load_dwordx4 v190, s[36:39], s72 offen offset:2688 lds\n"
    "v_mfma_f32_16x16x32_bf16 v[128:131], v[236:239], a[236:239], v[128:131]\n"
    "s_waitcnt lgkmcnt(4)\n"
    "v_mfma_f32_16x16x32_bf16 v[132:135], v[224:227], a[228:231], v[132:135]\n"
    "s_add_u32 s72, s72, 4294566112\n"
    "ds_read_b64_tr_b16 v[236:237], v246 offset:640\n"
    "ds_read_b64_tr_b16 v[238:239], v246 offset:864\n"
    "v_mfma_f32_16x16x32_bf16 v[136:139], v[224:227], a[232:235], v[136:139]\n"
    "s_waitcnt vmcnt(19)\n"
    "v_mfma_f32_16x16x32_bf16 v[140:143], v[224:227], a[236:239], v[140:143]\n"
    "s_waitcnt lgkmcnt(4)\n"
    "v_mfma_f32_16x16x32_bf16 v[144:147], v[228:231], a[228:231], v[144:147]\n"
    "s_barrier\n"
    "ds_read_b64_tr_b16 v[224:225], v245 offset:0\n"
    "ds_read_b64_tr_b16 v[226:227], v245 offset:224\n"
    "v_mfma_f32_16x16x32_bf16 v[148:151], v[228:231], a[232:235], v[148:151]\n"
    "s_add_u32 s49, s49, 16896\n"
    "v_mfma_f32_16x16x32_bf16 v[152:155], v[228:231], a[236:239], v[152:155]\n"
    "s_waitcnt lgkmcnt(4)\n"
    "v_mfma_f32_16x16x32_bf16 v[156:159], v[232:235], a[228:231], v[156:159]\n"
    "s_cmp_eq_u32 s49, s50\n"
    "ds_read_b64_tr_b16 v[228:229], v245 offset:32\n"
    "ds_read_b64_tr_b16 v[230:231], v245 offset:256\n"
    "v_mfma_f32_16x16x32_bf16 v[160:163], v[232:235], a[232:235], v[160:163]\n"
    "s_cselect_b32 s49, s51, s49\n"
    "v_mfma_f32_16x16x32_bf16 v[164:167], v[232:235], a[236:239], v[164:167]\n"
    "s_waitcnt lgkmcnt(4)\n"
    "v_mfma_f32_16x16x32_bf16 v[168:171], v[236:239], a[228:231], v[168:171]\n"
    "s_cmp_lg_u32 s70, 1\n"
    "s_cbranch_scc1 L_sa_%=\n"
    "buffer_store_dword v181, v182, s[44:47], s63 offen sc1\n"
    "L_sa_%=:\n"
    "ds_read_b64_tr_b16 v[232:233], v245 offset:64\n"
    "ds_read_b64_tr_b16 v[234:235], v245 offset:288\n"
    "v_mfma_f32_16x16x32_bf16 v[172:175], v[236:239], a[232:235], v[172:175]\n"
    "s_cmp_eq_u32 s70, 1\n"
    "s_cselect_b32 s38, 0, s38\n"
    "v_mfma_f32_16x16x32_bf16 v[176:179], v[236:239], a[236:239], v[176:179]\n"
    "s_waitcnt lgkmcnt(4)\n"
    "v_mfma_f32_16x16x32_bf16 v[96:99], v[224:227], a[240:243], v[96:99]\n"
    "s_mov_b32 m0, s49\n"
    "ds_read_b64_tr_b16 v[236:237], v245 offset:96\n"
    "ds_read_b64_tr_b16 v[238:239], v245 offset:320\n"
    "v_mfma_f32_16x16x32_bf16 v[100:103], v[224:227], a[244:247], v[100:103]\n"
    "buffer_store_dwordx4 a[12:15], v240, s[40:43], s73 offen offset:3072 sc1\n"
    "v_mfma_f32_16x16x32_bf16 v[104:107], v[224:227], a[248:251], v[104:107]\n"
    "s_waitcnt lgkmcnt(4)\n"
    "v_mfma_f32_16x16x32_bf16 v[108:111], v[228:231], a[240:243], v[108:111]\n"
    "ds_read_b64_tr_b16 v[224:225], v245 offset:128\n"
    "ds_read_b64_tr_b16 v[226:227], v245 offset:352\n"
    "v_mfma_f32_16x16x32_bf16 v[112:115], v[228:231], a[244:247], v[112:115]\n"
    "buffer_load_dwordx4 v190, s[36:39], s72 offen offset:0 lds\n"
    "v_mfma_f32_16x16x32_bf16 v[116:119], v[228:231], a[248:251], v[116:119]\n"
    "s_waitcnt lgkmcnt(4)\n"
    "v_mfma_f32_16x16x32_bf16 v[120:123], v[232:235], a[240:243], v[120:123]\n"
    "s_add_u32 m0, s49, 160\n"
    "ds_read_b64_tr_b16 v[228:229], v245 offset:160\n"
    "ds_read_b64_tr_b16 v[230:231], v245 offset:384\n"
    "v_mfma_f32_16x16x32_bf16 v[124:127], v[232:235], a[244:247], v[124:127]\n"
    "v_mfma_f32_16x16x32_bf16 v[128:131], v[232:235], a[248:251], v[128:131]\n"
    "s_waitcnt lgkmcnt(4)\n"
    "v_mfma_f32_16x16x32_bf16 v[132:135], v[236:239], a[240:243], v[132:135]\n"
    "ds_read_b64_tr_b16 v[232:233], v245 offset:192\n"
    "ds_read_b64_tr_b16 v[234:235], v245 offset:416\n"
    "v_mfma_f32_16x16x32_bf16 v[136:139], v[236:239], a[244:247], v[136:139]\n"
    "v_mfma_f32_16x16x32_bf16 v[140:143], v[236:239], a[248:251], v[140:143]\n"
    "s_waitcnt lgkmcnt(4)\n"
    "v_mfma_f32_16x16x32_bf16 v[144:147], v[224:227], a[240:243], v[144:147]\n"
    "ds_read_b64_tr_b16 v[236:237], v245 offset:448\n"
    "ds_read_b64_tr_b16 v[238:239], v245 offset:672\n"
    "v_mfma_f32_16x16x32_bf16 v[148:151], v[224:227], a[244:247], v[148:151]\n"
    "buffer_load_dwordx4 v190, s[36:39], s72 offen offset:896 lds\n"
    "v_mfma_f32_16x16x32_bf16 v[152:155], v[224:227], a[248:251], v[152:155]\n"
    "s_waitcnt lgkmcnt(4)\n"
    "v_mfma_f32_16x16x32_bf16 v[156:159], v[228:231], a[240:243], v[156:159]\n"
    "s_add_u32 m0, s49, 320\n"
    "ds_read_b64_tr_b16 v[224:225], v245 offset:480\n"
    "ds_read_b64_tr_b16 v[226:227], v245 offset:704\n"
    "v_mfma_f32_16x16x32_bf16 v[160:163], v[228:231], a[244:247], v[160:163]\n"
    "v_mfma_f32_16x16x32_bf16 v[164:167], v[228:231], a[248:251], v[164:167]\n"
    "s_waitcnt lgkmcnt(4)\n"
    "v_mfma_f32_16x16x32_bf16 v[168:171], v[232:235], a[240:243], v[168:171]\n"
    "ds_read_b64_tr_b16 v[228:229], v245 offset:512\n"
    "ds_read_b64_tr_b16 v[230:231], v245 offset:736\n"
    "v_mfma_f32_16x16x32_bf16 v[172:175], v[232:235], a[244:247], v[172:175]\n"
    "v_mfma_f32_16x16x32_bf16 v[176:179], v[232:235], a[248:251], v[176:179]\n"
    "s_waitcnt lgkmcnt(4)\n"
    "v_mfma_f32_16x16x32_bf16 v[96:99], v[236:239], a[252:255], v[96:99]\n"
    "s_add_u32 s81, s73, 4096\n"
    "ds_read_b64_tr_b16 v[232:233], v245 offset:544\n"
    "ds_read_b64_tr_b16 v[234:235], v245 offset:768\n"
    "v_mfma_f32_16x16x32_bf16 v[100:103], v[236:239], v[192:195], v[100:103]\n"
    "buffer_load_dwordx4 v190, s[36:39], s72 offen offset:1792 lds\n"
    "v_mfma_f32_16x16x32_bf16 v[104:107], v[236:239], v[196:199], v[104:107]\n"
    "s_waitcnt lgkmcnt(4)\n"
    "v_mfma_f32_16x16x32_bf16 v[108:111], v[224:227], a[252:255], v[108:111]\n"
    "s_add_u32 m0, s49, 480\n"
    "ds_read_b64_tr_b16 v[236:237], v245 offset:576\n"
    "ds_read_b64_tr_b16 v[238:239], v245 offset:800\n"
    "v_mfma_f32_16x16x32_bf16 v[112:115], v[224:227], v[192:195], v[112:115]\n"
    "v_add_u32_e64 v246, s52, v255\n"
    "v_mfma_f32_16x16x32_bf16 v[116:119], v[224:227], v[196:199], v[116:119]\n"
    "s_waitcnt lgkmcnt(4)\n"
    "v_mfma_f32_16x16x32_bf16 v[120:123], v[228:231], a[252:255], v[120:123]\n"
    "s_add_u32 s52, s52, 16896\n"
    "ds_read_b64_tr_b16 v[224:225], v245 offset:608\n"
    "ds_read_b64_tr_b16 v[226:227], v245 offset:832\n"
    "v_mfma_f32_16x16x32_bf16 v[124:127], v[228:231], v[192:195], v[124:127]\n"
    "s_cmp_eq_u32 s52, s53\n"
    "buffer_store_dwordx4 a[16:19], v240, s[40:43], s81 offen offset:0 sc1\n"
    "v_mfma_f32_16x16x32_bf16 v[128:131], v[228:231], v[196:199], v[128:131]\n"
    "s_waitcnt lgkmcnt(4)\n"
    "v_mfma_f32_16x16x32_bf16 v[132:135], v[232:235], a[252:255], v[132:135]\n"
    "s_cselect_b32 s52, s79, s52\n"
    "ds_read_b64_tr_b16 v[228:229], v245 offset:640\n"
    "ds_read_b64_tr_b16 v[230:231], v245 offset:864\n"
    "v_mfma_f32_16x16x32_bf16 v[136:139], v[232:235], v[192:195], v[136:139]\n"
    "buffer_load_dwordx4 v190, s[36:39], s72 offen offset:2688 lds\n"
    "v_mfma_f32_16x16x32_bf16 v[140:143], v[232:235], v[196:199], v[140:143]\n"
    "s_waitcnt lgkmcnt(4)\n"
    "v_mfma_f32_16x16x32_bf16 v[144:147], v[236:239], a[252:255], v[144:147]\n"
    "s_add_u32 s72, s72, 57344\n"
    "ds_read_b64_tr_b16 v[232:233], v246 offset:0\n"
    "ds_read_b64_tr_b16 v[234:235], v246 offset:224\n"
    "v_mfma_f32_16x16x32_bf16 v[148:151], v[236:239], v[192:195], v[148:151]\n"
    "s_add_u32 s49, s49, 16896\n"
    "v_mfma_f32_16x16x32_bf16 v[152:155], v[236:239], v[196:199], v[152:155]\n"
    "s_waitcnt lgkmcnt(4)\n"
    "v_mfma_f32_16x16x32_bf16 v[156:159], v[224:227], a[252:255], v[156:159]\n"
    "s_cmp_eq_u32 s49, s50\n"
    "ds_read_b64_tr_b16 v[236:237], v246 offset:32\n"
    "ds_read_b64_tr_b16 v[238:239], v246 offset:256\n"
    "v_mfma_f32_16x16x32_bf16 v[160:163], v[224:227], v[192:195], v[160:163]\n"
    "s_cselect_b32 s49, s51, s49\n"
    "v_mfma_f32_16x16x32_bf16 v[164:167], v[224:227], v[196:199], v[164:167]\n"
    "s_waitcnt lgkmcnt(4)\n"
    "v_mfma_f32_16x16x32_bf16 v[168:171], v[228:231], a[252:255], v[168:171]\n"
    "s_mov_b32 m0, s49\n"
    "ds_read_b64_tr_b16 v[224:225], v246 offset:64\n"
    "ds_read_b64_tr_b16 v[226:227], v246 offset:288\n"
    "v_mfma_f32_16x16x32_bf16 v[172:175], v[228:231], v[192:195], v[172:175]\n"
    "v_mfma_f32_16x16x32_bf16 v[176:179], v[228:231], v[196:199], v[176:179]\n"
    "s_waitcnt lgkmcnt(4)\n"
    "v_mfma_f32_16x16x32_bf16 v[96:99], v[232:235], v[200:203], v[96:99]\n"
    "ds_read_b64_tr_b16 v[228:229], v246 offset:96\n"
    "ds_read_b64_tr_b16 v[230:231], v246 offset:320\n"
    "v_mfma_f32_16x16x32_bf16 v[100:103], v[232:235], v[204:207], v[100:103]\n"
    "buffer_load_dwordx4 v190, s[36:39], s72 offen offset:0 lds\n"
    "v_mfma_f32_16x16x32_bf16 v[104:107], v[232:235], v[208:211], v[104:107]\n"
    "s_waitcnt lgkmcnt(4)\n"
    "v_mfma_f32_16x16x32_bf16 v[108:111], v[236:239], v[200:203], v[108:111]\n"
    "s_add_u32 m0, s49, 160\n"
    "ds_read_b64_tr_b16 v[232:233], v246 offset:128\n"
    "ds_read_b64_tr_b16 v[234:235], v246 offset:352\n"
    "v_mfma_f32_16x16x32_bf16 v[112:115], v[236:239], v[204:207], v[112:115]\n"
    "buffer_store_dwordx4 a[20:23], v240, s[40:43], s81 offen offset:1024 sc1\n"
    "v_mfma_f32_16x16x32_bf16 v[116:119], v[236:239], v[208:211], v[116:119]\n"
    "s_waitcnt lgkmcnt(4)\n"
    "v_mfma_f32_16x16x32_bf16 v[120:123], v[224:227], v[200:203], v[120:123]\n"
    "ds_read_b64_tr_b16 v[236:237], v246 offset:160\n"
    "ds_read_b64_tr_b16 v[238:239], v246 offset:384\n"
    "v_mfma_f32_16x16x32_bf16 v[124:127], v[224:227], v[204:207], v[124:127]\n"
    "v_mfma_f32_16x16x32_bf16 v[128:131], v[224:227], v[208:211], v[128:131]\n"
    "s_waitcnt lgkmcnt(4)\n"
    "v_mfma_f32_16x16x32_bf16 v[132:135], v[228:231], v[200:203], v[132:135]\n"
    "ds_read_b64_tr_b16 v[224:225], v246 offset:192\n"
    "ds_read_b64_tr_b16 v[226:227], v246 offset:416\n"
    "v_mfma_f32_16x16x32_bf16 v[136:139], v[228:231], v[204:207], v[136:139]\n"
    "buffer_load_dwordx4 v190, s[36:39], s72 offen offset:896 lds\n"
    "v_mfma_f32_16x16x32_bf16 v[140:143], v[228:231], v[208:211], v[140:143]\n"
    "s_waitcnt lgkmcnt(4)\n"
    "v_mfma_f32_16x16x32_bf16 v[144:147], v[232:235], v[200:203], v[144:147]\n"
    "s_add_u32 m0, s49, 320\n"
    "ds_read_b64_tr_b16 v[228:229], v246 offset:448\n"
    "ds_read_b64_tr_b16 v[230:231], v246 offset:672\n"
    "v_mfma_f32_16x16x32_bf16 v[148:151], v[232:235], v[204:207], v[148:151]\n"
    "v_mfma_f32_16x16x32_bf16 v[152:155], v[232:235], v[208:211], v[152:155]\n"
    "s_waitcnt lgkmcnt(4)\n"
    "v_mfma_f32_16x16x32_bf16 v[156:159], v[236:239], v[200:203], v[156:159]\n"
    "ds_read_b64_tr_b16 v[232:233], v246 offset:480\n"
    "ds_read_b64_tr_b16 v[234:235], v246 offset:704\n"
    "v_mfma_f32_16x16x32_bf16 v[160:163], v[236:239], v[204:207], v[160:163]\n"
    "v_mfma_f32_16x16x32_bf16 v[164:167], v[236:239], v[208:211], v[164:167]\n"
    "s_waitcnt lgkmcnt(4)\n"
    "v_mfma_f32_16x16x32_bf16 v[168:171], v[224:227], v[200:203], v[168:171]\n"
    "ds_read_b64_tr_b16 v[236:237], v246 offset:512\n"
    "ds_read_b64_tr_b16 v[238:239], v246 offset:736\n"
    "v_mfma_f32_16x16x32_bf16 v[172:175], v[224:227], v[204:207], v[172:175]\n"
    "buffer_load_dwordx4 v190, s[36:39], s72 offen offset:1792 lds\n"
    "v_mfma_f32_16x16x32_bf16 v[176:179], v[224:227], v[208:211], v[176:179]\n"
    "s_waitcnt lgkmcnt(4)\n"
    "v_mfma_f32_16x16x32_bf16 v[96:99], v[228:231], v[212:215], v[96:99]\n"
    "s_add_u32 m0, s49, 480\n"
    "ds_read_b64_tr_b16 v[224:225], v246 offset:544\n"
    "ds_read_b64_tr_b16 v[226:227], v246 offset:768\n"
    "v_mfma_f32_16x16x32_bf16 v[100:103], v[228:231], v[216:219], v[100:103]\n"
    "v_add_u32_e64 v245, s52, v255\n"
    "v_mfma_f32_16x16x32_bf16 v[104:107], v[228:231], v[220:223], v[104:107]\n"
    "s_waitcnt lgkmcnt(4)\n"
    "v_mfma_f32_16x16x32_bf16 v[108:111], v[232:235], v[212:215], v[108:111]\n"
    "s_add_u32 s52, s52, 16896\n"
    "ds_read_b64_tr_b16 v[228:229], v246 offset:576\n"
    "ds_read_b64_tr_b16 v[230:231], v246 offset:800\n"
    "v_mfma_f32_16x16x32_bf16 v[112:115], v[232:235], v[216:219], v[112:115]\n"
    "s_cmp_eq_u32 s52, s53\n"
    "buffer_store_dwordx4 a[24:27], v240, s[40:43], s81 offen offset:2048 sc1\n"
    "v_mfma_f32_16x16x32_bf16 v[116:119], v[232:235], v[220:223], v[116:119]\n"
    "s_waitcnt lgkmcnt(4)\n"
    "v_mfma_f32_16x16x32_bf16 v[120:123], v[236:239], v[212:215], v[120:123]\n"
    "s_cselect_b32 s52, s79, s52\n"
    "ds_read_b64_tr_b16 v[232:233], v246 offset:608\n"
    "ds_read_b64_tr_b16 v[234:235], v246 offset:832\n"
    "v_mfma_f32_16x16x32_bf16 v[124:127], v[236:239], v[216:219], v[124:127]\n"
    "buffer_load_dwordx4 v190, s[36:39], s72 offen offset:2688 lds\n"
    "v_mfma_f32_16x16x32_bf16 v[128:131], v[236:239], v[220:223], v[128:131]\n"
    "s_waitcnt lgkmcnt(4)\n"
    "v_mfma_f32_16x16x32_bf16 v[132:135], v[224:227], v[212:215], v[132:135]\n"
    "s_add_u32 s72, s72, 57344\n"
    "ds_read_b64_tr_b16 v[236:237], v246 offset:640\n"
    "ds_read_b64_tr_b16 v[238:239], v246 offset:864\n"
    "v_mfma_f32_16x16x32_bf16 v[136:139], v[224:227], v[216:219], v[136:139]\n"
    "s_waitcnt vmcnt(23)\n"
    "v_mfma_f32_16x16x32_bf16 v[140:143], v[224:227], v[220:223], v[140:143]\n"
    "s_waitcnt lgkmcnt(4)\n"
    "v_mfma_f32_16x16x32_bf16 v[144:147], v[228:231], v[212:215], v[144:147]\n"
    "s_barrier\n"
    "ds_read_b64_tr_b16 v[224:225], v245 offset:0\n"
    "ds_read_b64_tr_b16 v[226:227], v245 offset:224\n"
    "v_mfma_f32_16x16x32_bf16 v[148:151], v[228:231], v[216:219], v[148:151]\n"
    "s_add_u32 s49, s49, 16896\n"
    "v_mfma_f32_16x16x32_bf16 v[152:155], v[228:231], v[220:223], v[152:155]\n"
    "s_waitcnt lgkmcnt(4)\n"
    "v_mfma_f32_16x16x32_bf16 v[156:159], v[232:235], v[212:215], v[156:159]\n"
    "s_cmp_eq_u32 s49, s50\n"
    "ds_read_b64_tr_b16 v[228:229], v245 offset:32\n"
    "ds_read_b64_tr_b16 v[230:231], v245 offset:256\n"
    "v_mfma_f32_16x16x32_bf16 v[160:163], v[232:235], v[216:219], v[160:163]\n"
    "s_cselect_b32 s49, s51, s49\n"
    "v_mfma_f32_16x16x32_bf16 v[164:167], v[232:235], v[220:223], v[164:167]\n"
    "s_waitcnt lgkmcnt(4)\n"
    "v_mfma_f32_16x16x32_bf16 v[168:171], v[236:239], v[212:215], v[168:171]\n"
    "ds_read_b64_tr_b16 v[232:233], v245 offset:64\n"
    "ds_read_b64_tr_b16 v[234:235], v245 offset:288\n"
    "v_mfma_f32_16x16x32_bf16 v[172:175], v[236:239], v[216:219], v[172:175]\n"
    "v_mfma_f32_16x16x32_bf16 v[176:179], v[236:239], v[220:223], v[176:179]\n"
    "s_waitcnt lgkmcnt(4)\n"
    "v_mfma_f32_16x16x32_bf16 v[96:99], v[224:227], v[0:3], v[96:99]\n"
    "s_mov_b32 m0, s49\n"
    "ds_read_b64_tr_b16 v[236:237], v245 offset:96\n"
    "ds_read_b64_tr_b16 v[238:239], v245 offset:320\n"
    "v_mfma_f32_16x16x32_bf16 v[100:103], v[224:227], v[4:7], v[100:103]\n"
    "buffer_store_dwordx4 a[28:31], v240, s[40:43], s81 offen offset:3072 sc1\n"
    "v_mfma_f32_16x16x32_bf16 v[104:107], v[224:227], v[8:11], v[104:107]\n"
    "s_waitcnt lgkmcnt(4)\n"
    "v_mfma_f32_16x16x32_bf16 v[108:111], v[228:231], v[0:3], v[108:111]\n"
    "ds_read_b64_tr_b16 v[224:225], v245 offset:128\n"
    "ds_read_b64_tr_b16 v[226:227], v245 offset:352\n"
    "v_mfma_f32_16x16x32_bf16 v[112:115], v[228:231], v[4:7], v[112:115]\n"
    "buffer_load_dwordx4 v190, s[36:39], s72 offen offset:0 lds\n"
    "v_mfma_f32_16x16x32_bf16 v[116:119], v[228:231], v[8:11], v[116:119]\n"
    "s_waitcnt lgkmcnt(4)\n"
    "v_mfma_f32_16x16x32_bf16 v[120:123], v[232:235], v[0:3], v[120:123]\n"
    "s_add_u32 m0, s49, 160\n"
    "ds_read_b64_tr_b16 v[228:229], v245 offset:160\n"
    "ds_read_b64_tr_b16 v[230:231], v245 offset:384\n"
    "v_mfma_f32_16x16x32_bf16 v[124:127], v[232:235], v[4:7], v[124:127]\n"
    "v_mfma_f32_16x16x32_bf16 v[128:131], v[232:235], v[8:11], v[128:131]\n"
    "s_waitcnt lgkmcnt(4)\n"
    "v_mfma_f32_16x16x32_bf16 v[132:135], v[236:239], v[0:3], v[132:135]\n"
    "ds_read_b64_tr_b16 v[232:233], v245 offset:192\n"
    "ds_read_b64_tr_b16 v[234:235], v245 offset:416\n"
    "v_mfma_f32_16x16x32_bf16 v[136:139], v[236:239], v[4:7], v[136:139]\n"
    "v_mfma_f32_16x16x32_bf16 v[140:143], v[236:239], v[8:11], v[140:143]\n"
    "s_waitcnt lgkmcnt(4)\n"
    "v_mfma_f32_16x16x32_bf16 v[144:147], v[224:227], v[0:3], v[144:147]\n"
    "ds_read_b64_tr_b16 v[236:237], v245 offset:448\n"
    "ds_read_b64_tr_b16 v[238:239], v245 offset:672\n"
    "v_mfma_f32_16x16x32_bf16 v[148:151], v[224:227], v[4:7], v[148:151]\n"
    "buffer_load_dwordx4 v190, s[36:39], s72 offen offset:896 lds\n"
    "v_mfma_f32_16x16x32_bf16 v[152:155], v[224:227], v[8:11], v[152:155]\n"
    "s_waitcnt lgkmcnt(4)\n"
    "v_mfma_f32_16x16x32_bf16 v[156:159], v[228:231], v[0:3], v[156:159]\n"
    "s_add_u32 m0, s49, 320\n"
    "ds_read_b64_tr_b16 v[224:225], v245 offset:480\n"
    "ds_read_b64_tr_b16 v[226:227], v245 offset:704\n"
    "v_mfma_f32_16x16x32_bf16 v[160:163], v[228:231], v[4:7], v[160:163]\n"
    "v_mfma_f32_16x16x32_bf16 v[164:167], v[228:231], v[8:11], v[164:167]\n"
    "s_waitcnt lgkmcnt(4)\n"
    "v_mfma_f32_16x16x32_bf16 v[168:171], v[232:235], v[0:3], v[168:171]\n"
    "ds_read_b64_tr_b16 v[228:229], v245 offset:512\n"
    "ds_read_b64_tr_b16 v[230:231], v245 offset:736\n"
    "v_mfma_f32_16x16x32_bf16 v[172:175], v[232:235], v[4:7], v[172:175]\n"
    "v_mfma_f32_16x16x32_bf16 v[176:179], v[232:235], v[8:11], v[176:179]\n"
    "s_waitcnt lgkmcnt(4)\n"
    "v_mfma_f32_16x16x32_bf16 v[96:99], v[236:239], v[12:15], v[96:99]\n"
    "s_add_u32 s81, s73, 8192\n"
    "ds_read_b64_tr_b16 v[232:233], v245 offset:544\n"
    "ds_read_b64_tr_b16 v[234:235], v245 offset:768\n"
    "v_mfma_f32_16x16x32_bf16 v[100:103], v[236:239], v[16:19], v[100:103]\n"
    "buffer_load_dwordx4 v190, s[36:39], s72 offen offset:1792 lds\n"
    "v_mfma_f32_16x16x32_bf16 v[104:107], v[236:239], v[20:23], v[104:107]\n"
    "s_waitcnt lgkmcnt(4)\n"
    "v_mfma_f32_16x16x32_bf16 v[108:111], v[224:227], v[12:15], v[108:111]\n"
    "s_add_u32 m0, s49, 480\n"
    "ds_read_b64_tr_b16 v[236:237], v245 offset:576\n"
    "ds_read_b64_tr_b16 v[238:239], v245 offset:800\n"
    "v_mfma_f32_16x16x32_bf16 v[112:115], v[224:227], v[16:19], v[112:115]\n"
    "v_add_u32_e64 v246, s52, v255\n"
    "v_mfma_f32_16x16x32_bf16 v[116:119], v[224:227], v[20:23], v[116:119]\n"
    "s_waitcnt lgkmcnt(4)\n"
    "v_mfma_f32_16x16x32_bf16 v[120:123], v[228:231], v[12:15], v[120:123]\n"
    "s_add_u32 s52, s52, 16896\n"
    "ds_read_b64_tr_b16 v[224:225], v245 offset:608\n"
    "ds_read_b64_tr_b16 v[226:227], v245 offset:832\n"
    "v_mfma_f32_16x16x32_bf16 v[124:127], v[228:231], v[16:19], v[124:127]\n"
    "s_cmp_eq_u32 s52, s53\n"
    "buffer_store_dwordx4 a[32:35], v240, s[40:43], s81 offen offset:0 sc1\n"
    "v_mfma_f32_16x16x32_bf16 v[128:131], v[228:231], v[20:23], v[128:131]\n"
    "s_waitcnt lgkmcnt(4)\n"
    "v_mfma_f32_16x16x32_bf16 v[132:135], v[232:235], v[12:15], v[132:135]\n"
    "s_cselect_b32 s52, s79, s52\n"
    "ds_read_b64_tr_b16 v[228:229], v245 offset:640\n"
    "ds_read_b64_tr_b16 v[230:231], v245 offset:864\n"
    "v_mfma_f32_16x16x32_bf16 v[136:139], v[232:235], v[16:19], v[136:139]\n"
    "buffer_load_dwordx4 v190, s[36:39], s72 offen offset:2688 lds\n"
    "v_mfma_f32_16x16x32_bf16 v[140:143], v[232:235], v[20:23], v[140:143]\n"
    "s_waitcnt lgkmcnt(4)\n"
    "v_mfma_f32_16x16x32_bf16 v[144:147], v[236:239], v[12:15], v[144:147]\n"
    "s_add_u32 s72, s72, 57344\n"
    "ds_read_b64_tr_b16 v[232:233], v246 offset:0\n"
    "ds_read_b64_tr_b16 v[234:235], v246 offset:224\n"
    "v_mfma_f32_16x16x32_bf16 v[148:151], v[236:239], v[16:19], v[148:151]\n"
    "s_add_u32 s49, s49, 16896\n"
    "v_mfma_f32_16x16x32_bf16 v[152:155], v[236:239], v[20:23], v[152:155]\n"
    "s_waitcnt lgkmcnt(4)\n"
    "v_mfma_f32_16x16x32_bf16 v[156:159], v[224:227], v[12:15], v[156:159]\n"
    "s_cmp_eq_u32 s49, s50\n"
    "ds_read_b64_tr_b16 v[236:237], v246 offset:32\n"
    "ds_read_b64_tr_b16 v[238:239], v246 offset:256\n"
    "v_mfma_f32_16x16x32_bf16 v[160:163], v[224:227], v[16:19], v[160:163]\n"
    "s_cselect_b32 s49, s51, s49\n"
    "v_mfma_f32_16x16x32_bf16 v[164:167], v[224:227], v[20:23], v[164:167]\n"
    "s_waitcnt lgkmcnt(4)\n"
    "v_mfma_f32_16x16x32_bf16 v[168:171], v[228:231], v[12:15], v[168:171]\n"
    "s_mov_b32 m0, s49\n"
    "ds_read_b64_tr_b16 v[224:225], v246 offset:64\n"
    "ds_read_b64_tr_b16 v[226:227], v246 offset:288\n"
    "v_mfma_f32_16x16x32_bf16 v[172:175], v[228:231], v[16:19], v[172:175]\n"
    "v_mfma_f32_16x16x32_bf16 v[176:179], v[228:231], v[20:23], v[176:179]\n"
    "s_waitcnt lgkmcnt(4)\n"
    "v_mfma_f32_16x16x32_bf16 v[96:99], v[232:235], v[24:27], v[96:99]\n"
    "ds_read_b64_tr_b16 v[228:229], v246 offset:96\n"
    "ds_read_b64_tr_b16 v[230:231], v246 offset:320\n"
    "v_mfma_f32_16x16x32_bf16 v[100:103], v[232:235], v[28:31], v[100:103]\n"
    "buffer_load_dwordx4 v190, s[36:39], s72 offen offset:0 lds\n"
    "v_mfma_f32_16x16x32_bf16 v[104:107], v[232:235], v[32:35], v[104:107]\n"
    "s_waitcnt lgkmcnt(4)\n"
    "v_mfma_f32_16x16x32_bf16 v[108:111], v[236:239], v[24:27], v[108:111]\n"
    "s_add_u32 m0, s49, 160\n"
    "ds_read_b64_tr_b16 v[232:233], v246 offset:128\n"
    "ds_read_b64_tr_b16 v[234:235], v246 offset:352\n"
    "v_mfma_f32_16x16x32_bf16 v[112:115], v[236:239], v[28:31], v[112:115]\n"
    "buffer_store_dwordx4 a[36:39], v240, s[40:43], s81 offen offset:1024 sc1\n"
    "v_mfma_f32_16x16x32_bf16 v[116:119], v[236:239], v[32:35], v[116:119]\n"
    "s_waitcnt lgkmcnt(4)\n"
    "v_mfma_f32_16x16x32_bf16 v[120:123], v[224:227], v[24:27], v[120:123]\n"
    "ds_read_b64_tr_b16 v[236:237], v246 offset:160\n"
    "ds_read_b64_tr_b16 v[238:239], v246 offset:384\n"
    "v_mfma_f32_16x16x32_bf16 v[124:127], v[224:227], v[28:31], v[124:127]\n"
    "v_mfma_f32_16x16x32_bf16 v[128:131], v[224:227], v[32:35], v[128:131]\n"
    "s_waitcnt lgkmcnt(4)\n"
    "v_mfma_f32_16x16x32_bf16 v[132:135], v[228:231], v[24:27], v[132:135]\n"
    "ds_read_b64_tr_b16 v[224:225], v246 offset:192\n"
    "ds_read_b64_tr_b16 v[226:227], v246 offset:416\n"
    "v_mfma_f32_16x16x32_bf16 v[136:139], v[228:231], v[28:31], v[136:139]\n"
    "buffer_load_dwordx4 v190, s[36:39], s72 offen offset:896 lds\n"
    "v_mfma_f32_16x16x32_bf16 v[140:143], v[228:231], v[32:35], v[140:143]\n"
    "s_waitcnt lgkmcnt(4)\n"
    "v_mfma_f32_16x16x32_bf16 v[144:147], v[232:235], v[24:27], v[144:147]\n"
    "s_add_u32 m0, s49, 320\n"
    "ds_read_b64_tr_b16 v[228:229], v246 offset:448\n"
    "ds_read_b64_tr_b16 v[230:231], v246 offset:672\n"
    "v_mfma_f32_16x16x32_bf16 v[148:151], v[232:235], v[28:31], v[148:151]\n"
    "v_mfma_f32_16x16x32_bf16 v[152:155], v[232:235], v[32:35], v[152:155]\n"
    "s_waitcnt lgkmcnt(4)\n"
    "v_mfma_f32_16x16x32_bf16 v[156:159], v[236:239], v[24:27], v[156:159]\n"
    "ds_read_b64_tr_b16 v[232:233], v246 offset:480\n"
    "ds_read_b64_tr_b16 v[234:235], v246 offset:704\n"
    "v_mfma_f32_16x16x32_bf16 v[160:163], v[236:239], v[28:31], v[160:163]\n"
    "v_add_u32_e64 v245, s52, v255\n"
    "v_mfma_f32_16x16x32_bf16 v[164:167], v[236:239], v[32:35], v[164:167]\n"
    "s_waitcnt lgkmcnt(4)\n"
    "v_mfma_f32_16x16x32_bf16 v[168:171], v[224:227], v[24:27], v[168:171]\n"
    "s_add_u32 s52, s52, 16896\n"
    "ds_read_b64_tr_b16 v[236:237], v246 offset:512\n"
    "ds_read_b64_tr_b16 v[238:239], v246 offset:736\n"
    "v_mfma_f32_16x16x32_bf16 v[172:175], v[224:227], v[28:31], v[172:175]\n"
    "buffer_load_dwordx4 v190, s[36:39], s72 offen offset:1792 lds\n"
    "v_mfma_f32_16x16x32_bf16 v[176:179], v[224:227], v[32:35], v[176:179]\n"
    "s_waitcnt lgkmcnt(4)\n"
    "v_mfma_f32_16x16x32_bf16 v[96:99], v[228:231], v[36:39], v[96:99]\n"
    "s_add_u32 m0, s49, 480\n"
    "ds_read_b64_tr_b16 v[224:225], v246 offset:544\n"
    "ds_read_b64_tr_b16 v[226:227], v246 offset:768\n"
    "v_mfma_f32_16x16x32_bf16 v[100:103], v[228:231], v[40:43], v[100:103]\n"
    "s_cmp_eq_u32 s52, s53\n"
    "buffer_store_dwordx2 a[40:41], v241, s[40:43], s81 offen offset:2048 sc1\n"
    "v_mfma_f32_16x16x32_bf16 v[104:107], v[228:231], v[44:47], v[104:107]\n"
    "s_waitcnt lgkmcnt(4)\n"
    "v_mfma_f32_16x16x32_bf16 v[108:111], v[232:235], v[36:39], v[108:111]\n"
    "s_cselect_b32 s52, s79, s52\n"
    "ds_read_b64_tr_b16 v[228:229], v246 offset:576\n"
    "ds_read_b64_tr_b16 v[230:231], v246 offset:800\n"
    "v_mfma_f32_16x16x32_bf16 v[112:115], v[232:235], v[40:43], v[112:115]\n"
    "s_add_u32 s73, s73, 10752\n"
    "v_mfma_f32_16x16x32_bf16 v[116:119], v[232:235], v[44:47], v[116:119]\n"
    "s_waitcnt lgkmcnt(4)\n"
    "v_mfma_f32_16x16x32_bf16 v[120:123], v[236:239], v[36:39], v[120:123]\n"
    "s_mov_b32 s42, 172032\n"
    "ds_read_b64_tr_b16 v[232:233], v246 offset:608\n"
    "ds_read_b64_tr_b16 v[234:235], v246 offset:832\n"
    "v_mfma_f32_16x16x32_bf16 v[124:127], v[236:239], v[40:43], v[124:127]\n"
    "buffer_load_dwordx4 v190, s[36:39], s72 offen offset:2688 lds\n"
    "v_mfma_f32_16x16x32_bf16 v[128:131], v[236:239], v[44:47], v[128:131]\n"
    "s_waitcnt lgkmcnt(4)\n"
    "v_mfma_f32_16x16x32_bf16 v[132:135], v[224:227], v[36:39], v[132:135]\n"
    "s_add_u32 s72, s72, 57344\n"
    "ds_read_b64_tr_b16 v[236:237], v246 offset:640\n"
    "ds_read_b64_tr_b16 v[238:239], v246 offset:864\n"
    "v_mfma_f32_16x16x32_bf16 v[136:139], v[224:227], v[40:43], v[136:139]\n"
    "s_waitcnt vmcnt(24)\n"
    "v_add_u32_e64 v246, s52, v255\n"
    "v_mfma_f32_16x16x32_bf16 v[140:143], v[224:227], v[44:47], v[140:143]\n"
    "s_waitcnt lgkmcnt(4)\n"
    "v_mfma_f32_16x16x32_bf16 v[144:147], v[228:231], v[36:39], v[144:147]\n"
    "s_barrier\n"
    "ds_read_b64_tr_b16 v[224:225], v245 offset:0\n"
    "ds_read_b64_tr_b16 v[226:227], v245 offset:224\n"
    "v_mfma_f32_16x16x32_bf16 v[148:151], v[228:231], v[40:43], v[148:151]\n"
    "s_add_u32 s49, s49, 16896\n"
    "v_mfma_f32_16x16x32_bf16 v[152:155], v[228:231], v[44:47], v[152:155]\n"
    "s_waitcnt lgkmcnt(4)\n"
    "v_mfma_f32_16x16x32_bf16 v[156:159], v[232:235], v[36:39], v[156:159]\n"
    "s_cmp_eq_u32 s49, s50\n"
    "ds_read_b64_tr_b16 v[228:229], v245 offset:448\n"
    "ds_read_b64_tr_b16 v[230:231], v245 offset:672\n"
    "v_mfma_f32_16x16x32_bf16 v[160:163], v[232:235], v[40:43], v[160:163]\n"
    "s_cselect_b32 s49, s51, s49\n"
    "v_mfma_f32_16x16x32_bf16 v[164:167], v[232:235], v[44:47], v[164:167]\n"
    "s_waitcnt lgkmcnt(4)\n"
    "v_mfma_f32_16x16x32_bf16 v[168:171], v[236:239], v[36:39], v[168:171]\n"
    "s_mov_b32 m0, s49\n"
    "ds_read_b64_tr_b16 v[232:233], v246 offset:0\n"
    "ds_read_b64_tr_b16 v[234:235], v246 offset:224\n"
    "v_mfma_f32_16x16x32_bf16 v[172:175], v[236:239], v[40:43], v[172:175]\n"
    "s_add_u32 s52, s52, 16896\n"
    "v_mfma_f32_16x16x32_bf16 v[176:179], v[236:239], v[44:47], v[176:179]\n"
    "s_waitcnt lgkmcnt(4)\n"
    "v_mfma_f32_16x16x32_bf16 v[96:99], v[224:227], v[48:51], v[96:99]\n"
    "s_cmp_eq_u32 s52, s53\n"
    "ds_read_b64_tr_b16 v[236:237], v246 offset:448\n"
    "ds_read_b64_tr_b16 v[238:239], v246 offset:672\n"
    "v_mfma_f32_16x16x32_bf16 v[100:103], v[224:227], v[52:55], v[100:103]\n"
    "s_cselect_b32 s52, s79, s52\n"
    "buffer_load_dwordx4 v190, s[36:39], s72 offen offset:0 lds\n"
    "v_mfma_f32_16x16x32_bf16 v[104:107], v[224:227], v[56:59], v[104:107]\n"
    "s_waitcnt lgkmcnt(4)\n"
    "v_mfma_f32_16x16x32_bf16 v[96:99], v[228:231], v[60:63], v[96:99]\n"
    "s_add_u32 m0, s49, 160\n"
    "ds_read_b64_tr_b16 v[224:225], v245 offset:32\n"
    "ds_read_b64_tr_b16 v[226:227], v245 offset:256\n"
    "v_mfma_f32_16x16x32_bf16 v[100:103], v[228:231], v[64:67], v[100:103]\n"
    "v_mfma_f32_16x16x32_bf16 v[104:107], v[228:231], v[68:71], v[104:107]\n"
    "s_waitcnt lgkmcnt(4)\n"
    "v_mfma_f32_16x16x32_bf16 v[96:99], v[232:235], v[72:75], v[96:99]\n"
    "ds_read_b64_tr_b16 v[228:229], v245 offset:480\n"
    "ds_read_b64_tr_b16 v[230:231], v245 offset:704\n"
    "v_mfma_f32_16x16x32_bf16 v[100:103], v[232:235], v[76:79], v[100:103]\n"
    "buffer_load_dwordx4 v190, s[36:39], s72 offen offset:896 lds\n"
    "v_mfma_f32_16x16x32_bf16 v[104:107], v[232:235], v[80:83], v[104:107]\n"
    "s_waitcnt lgkmcnt(4)\n"
    "v_mfma_f32_16x16x32_bf16 v[96:99], v[236:239], v[84:87], v[96:99]\n"
    "s_add_u32 m0, s49, 320\n"
    "ds_read_b64_tr_b16 v[232:233], v246 offset:32\n"
    "ds_read_b64_tr_b16 v[234:235], v246 offset:256\n"
    "v_mfma_f32_16x16x32_bf16 v[100:103], v[236:239], v[88:91], v[100:103]\n"
    "v_mfma_f32_16x16x32_bf16 v[104:107], v[236:239], v[92:95], v[104:107]\n"
    "s_waitcnt lgkmcnt(4)\n"
    "v_mfma_f32_16x16x32_bf16 v[108:111], v[224:227], v[48:51], v[108:111]\n"
    "ds_read_b64_tr_b16 v[236:237], v246 offset:480\n"
    "ds_read_b64_tr_b16 v[238:239], v246 offset:704\n"
    "v_mfma_f32_16x16x32_bf16 v[112:115], v[224:227], v[52:55], v[112:115]\n"
    "buffer_load_dwordx4 v190, s[36:39], s72 offen offset:1792 lds\n"
    "v_mfma_f32_16x16x32_bf16 v[116:119], v[224:227], v[56:59], v[116:119]\n"
    "s_waitcnt lgkmcnt(4)\n"
    "v_mfma_f32_16x16x32_bf16 v[108:111], v[228:231], v[60:63], v[108:111]\n"
    "s_add_u32 m0, s49, 480\n"
    "ds_read_b64_tr_b16 v[224:225], v245 offset:64\n"
    "ds_read_b64_tr_b16 v[226:227], v245 offset:288\n"
    "v_mfma_f32_16x16x32_bf16 v[112:115], v[228:231], v[64:67], v[112:115]\n"
    "v_cvt_pk_bf16_f32 v250, v96, v97\n"
    "v_cvt_pk_bf16_f32 v251, v98, v99\n"
    "v_mfma_f32_16x16x32_bf16 v[116:119], v[228:231], v[68:71], v[116:119]\n"
    "s_waitcnt lgkmcnt(4)\n"
    "v_accvgpr_write_b32 a0, v250\n"
    "v_accvgpr_write_b32 a1, v251\n"
    "v_mfma_f32_16x16x32_bf16 v[108:111], v[232:235], v[72:75], v[108:111]\n"
    "ds_read_b64_tr_b16 v[228:229], v245 offset:512\n"
    "ds_read_b64_tr_b16 v[230:231], v245 offset:736\n"
    "v_mfma_f32_16x16x32_bf16 v[112:115], v[232:235], v[76:79], v[112:115]\n"
    "buffer_load_dwordx4 v190, s[36:39], s72 offen offset:2688 lds\n"
    "v_mfma_f32_16x16x32_bf16 v[116:119], v[232:235], v[80:83], v[116:119]\n"
    "s_waitcnt lgkmcnt(4)\n"
    "v_mfma_f32_16x16x32_bf16 v[108:111], v[236:239], v[84:87], v[108:111]\n"
    "s_add_u32 s72, s72, 57344\n"
    "ds_read_b64_tr_b16 v[232:233], v246 offset:64\n"
    "ds_read_b64_tr_b16 v[234:235], v246 offset:288\n"
    "v_mfma_f32_16x16x32_bf16 v[112:115], v[236:239], v[88:91], v[112:115]\n"
    "s_add_u32 s49, s49, 16896\n"
    "v_cvt_pk_bf16_f32 v252, v100, v101\n"
    "v_cvt_pk_bf16_f32 v253, v102, v103\n"
    "v_mfma_f32_16x16x32_bf16 v[116:119], v[236:239], v[92:95], v[116:119]\n"
    "s_waitcnt lgkmcnt(4)\n"
    "v_accvgpr_write_b32 a2, v252\n"
    "v_accvgpr_write_b32 a3, v253\n"
    "v_mfma_f32_16x16x32_bf16 v[120:123], v[224:227], v[48:51], v[120:123]\n"
    "s_cmp_eq_u32 s49, s50\n"
    "ds_read_b64_tr_b16 v[236:237], v246 offset:512\n"
    "ds_read_b64_tr_b16 v[238:239], v246 offset:736\n"
    "v_mfma_f32_16x16x32_bf16 v[124:127], v[224:227], v[52:55], v[124:127]\n"
    "s_cselect_b32 s49, s51, s49\n"
    "v_cvt_pk_bf16_f32 v250, v104, v105\n"
    "v_cvt_pk_bf16_f32 v251, v106, v107\n"
    "v_mfma_f32_16x16x32_bf16 v[128:131], v[224:227], v[56:59], v[128:131]\n"
    "s_waitcnt lgkmcnt(4)\n"
    "v_accvgpr_write_b32 a4, v250\n"
    "v_accvgpr_write_b32 a5, v251\n"
    "v_mfma_f32_16x16x32_bf16 v[120:123], v[228:231], v[60:63], v[120:123]\n"
    "s_mov_b32 m0, s49\n"
    "ds_read_b64_tr_b16 v[224:225], v245 offset:96\n"
    "ds_read_b64_tr_b16 v[226:227], v245 offset:320\n"
    "v_mfma_f32_16x16x32_bf16 v[124:127], v[228:231], v[64:67], v[124:127]\n"
    "buffer_load_dwordx4 v190, s[36:39], s72 offen offset:0 lds\n"
    "v_mfma_f32_16x16x32_bf16 v[128:131], v[228:231], v[68:71], v[128:131]\n"
    "s_waitcnt lgkmcnt(4)\n"
    "v_mfma_f32_16x16x32_bf16 v[120:123], v[232:235], v[72:75], v[120:123]\n"
    "s_add_u32 m0, s49, 160\n"
    "ds_read_b64_tr_b16 v[228:229], v245 offset:544\n"
    "ds_read_b64_tr_b16 v[230:231], v245 offset:768\n"
    "v_mfma_f32_16x16x32_bf16 v[124:127], v[232:235], v[76:79], v[124:127]\n"
    "v_cvt_pk_bf16_f32 v252, v108, v109\n"
    "v_cvt_pk_bf16_f32 v253, v110, v111\n"
    "v_mfma_f32_16x16x32_bf16 v[128:131], v[232:235], v[80:83], v[128:131]\n"
    "s_waitcnt lgkmcnt(4)\n"
    "v_accvgpr_write_b32 a6, v252\n"
    "v_accvgpr_write_b32 a7, v253\n"
    "v_mfma_f32_16x16x32_bf16 v[120:123], v[236:239], v[84:87], v[120:123]\n"
    "ds_read_b64_tr_b16 v[232:233], v246 offset:96\n"
    "ds_read_b64_tr_b16 v[234:235], v246 offset:320\n"
    "v_mfma_f32_16x16x32_bf16 v[124:127], v[236:239], v[88:91], v[124:127]\n"
    "buffer_load_dwordx4 v190, s[36:39], s72 offen offset:896 lds\n"
    "v_mfma_f32_16x16x32_bf16 v[128:131], v[236:239], v[92:95], v[128:131]\n"
    "s_waitcnt lgkmcnt(4)\n"
    "v_mfma_f32_16x16x32_bf16 v[132:135], v[224:227], v[48:51], v[132:135]\n"
    "s_add_u32 m0, s49, 320\n"
    "ds_read_b64_tr_b16 v[236:237], v246 offset:544\n"
    "ds_read_b64_tr_b16 v[238:239], v246 offset:768\n"
    "v_mfma_f32_16x16x32_bf16 v[136:139], v[224:227], v[52:55], v[136:139]\n"
    "v_cvt_pk_bf16_f32 v250, v112, v113\n"
    "v_cvt_pk_bf16_f32 v251, v114, v115\n"
    "v_mfma_f32_16x16x32_bf16 v[140:143], v[224:227], v[56:59], v[140:143]\n"
    "s_waitcnt lgkmcnt(4)\n"
    "v_accvgpr_write_b32 a8, v250\n"
    "v_accvgpr_write_b32 a9, v251\n"
    "v_mfma_f32_16x16x32_bf16 v[132:135], v[228:231], v[60:63], v[132:135]\n"
    "ds_read_b64_tr_b16 v[224:225], v245 offset:128\n"
    "ds_read_b64_tr_b16 v[226:227], v245 offset:352\n"
    "v_mfma_f32_16x16x32_bf16 v[136:139], v[228:231], v[64:67], v[136:139]\n"
    "buffer_load_dwordx4 v190, s[36:39], s72 offen offset:1792 lds\n"
    "v_mfma_f32_16x16x32_bf16 v[140:143], v[228:231], v[68:71], v[140:143]\n"
    "s_waitcnt lgkmcnt(4)\n"
    "v_mfma_f32_16x16x32_bf16 v[132:135], v[232:235], v[72:75], v[132:135]\n"
    "s_add_u32 m0, s49, 480\n"
    "ds_read_b64_tr_b16 v[228:229], v245 offset:576\n"
    "ds_read_b64_tr_b16 v[230:231], v245 offset:800\n"
    "v_mfma_f32_16x16x32_bf16 v[136:139], v[232:235], v[76:79], v[136:139]\n"
    "v_cvt_pk_bf16_f32 v252, v116, v117\n"
    "v_cvt_pk_bf16_f32 v253, v118, v119\n"
    "v_mfma_f32_16x16x32_bf16 v[140:143], v[232:235], v[80:83], v[140:143]\n"
    "s_waitcnt lgkmcnt(4)\n"
    "v_accvgpr_write_b32 a10, v252\n"
    "v_accvgpr_write_b32 a11, v253\n"
    "v_mfma_f32_16x16x32_bf16 v[132:135], v[236:239], v[84:87], v[132:135]\n"
    "ds_read_b64_tr_b16 v[232:233], v246 offset:128\n"
    "ds_read_b64_tr_b16 v[234:235], v246 offset:352\n"
    "v_mfma_f32_16x16x32_bf16 v[136:139], v[236:239], v[88:91], v[136:139]\n"
    "buffer_load_dwordx4 v190, s[36:39], s72 offen offset:2688 lds\n"
    "v_mfma_f32_16x16x32_bf16 v[140:143], v[236:239], v[92:95], v[140:143]\n"
    "s_waitcnt lgkmcnt(4)\n"
    "v_mfma_f32_16x16x32_bf16 v[144:147], v[224:227], v[48:51], v[144:147]\n"
    "s_add_u32 s72, s72, 57344\n"
    "ds_read_b64_tr_b16 v[236:237], v246 offset:576\n"
    "ds_read_b64_tr_b16 v[238:239], v246 offset:800\n"
    "v_mfma_f32_16x16x32_bf16 v[148:151], v[224:227], v[52:55], v[148:151]\n"
    "s_add_u32 s49, s49, 16896\n"
    "v_cvt_pk_bf16_f32 v250, v120, v121\n"
    "v_cvt_pk_bf16_f32 v251, v122, v123\n"
    "v_mfma_f32_16x16x32_bf16 v[152:155], v[224:227], v[56:59], v[152:155]\n"
    "s_waitcnt lgkmcnt(4)\n"
    "v_accvgpr_write_b32 a12, v250\n"
    "v_accvgpr_write_b32 a13, v251\n"
    "v_mfma_f32_16x16x32_bf16 v[144:147], v[228:231], v[60:63], v[144:147]\n"
    "s_cmp_eq_u32 s49, s50\n"
    "ds_read_b64_tr_b16 v[224:225], v245 offset:160\n"
    "ds_read_b64_tr_b16 v[226:227], v245 offset:384\n"
    "v_mfma_f32_16x16x32_bf16 v[148:151], v[228:231], v[64:67], v[148:151]\n"
    "s_cselect_b32 s49, s51, s49\n"
    "v_cvt_pk_bf16_f32 v252, v124, v125\n"
    "v_cvt_pk_bf16_f32 v253, v126, v127\n"
    "v_mfma_f32_16x16x32_bf16 v[152:155], v[228:231], v[68:71], v[152:155]\n"
    "s_waitcnt lgkmcnt(4)\n"
    "v_accvgpr_write_b32 a14, v252\n"
    "v_accvgpr_write_b32 a15, v253\n"
    "v_mfma_f32_16x16x32_bf16 v[144:147], v[232:235], v[72:75], v[144:147]\n"
    "ds_read_b64_tr_b16 v[228:229], v245 offset:608\n"
    "ds_read_b64_tr_b16 v[230:231], v245 offset:832\n"
    "v_mfma_f32_16x16x32_bf16 v[148:151], v[232:235], v[76:79], v[148:151]\n"
    "v_cvt_pk_bf16_f32 v250, v128, v129\n"
    "v_cvt_pk_bf16_f32 v251, v130, v131\n"
    "v_mfma_f32_16x16x32_bf16 v[152:155], v[232:235], v[80:83], v[152:155]\n"
    "s_waitcnt lgkmcnt(4)\n"
    "v_accvgpr_write_b32 a16, v250\n"
    "v_accvgpr_write_b32 a17, v251\n"
    "v_mfma_f32_16x16x32_bf16 v[144:147], v[236:239], v[84:87], v[144:147]\n"
    "ds_read_b64_tr_b16 v[232:233], v246 offset:160\n"
    "ds_read_b64_tr_b16 v[234:235], v246 offset:384\n"
    "v_mfma_f32_16x16x32_bf16 v[148:151], v[236:239], v[88:91], v[148:151]\n"
    "v_cvt_pk_bf16_f32 v252, v132, v133\n"
    "v_cvt_pk_bf16_f32 v253, v134, v135\n"
    "v_mfma_f32_16x16x32_bf16 v[152:155], v[236:239], v[92:95], v[152:155]\n"
    "s_waitcnt lgkmcnt(4)\n"
    "v_accvgpr_write_b32 a18, v252\n"
    "v_accvgpr_write_b32 a19, v253\n"
    "v_mfma_f32_16x16x32_bf16 v[156:159], v[224:227], v[48:51], v[156:159]\n"
    "ds_read_b64_tr_b16 v[236:237], v246 offset:608\n"
    "ds_read_b64_tr_b16 v[238:239], v246 offset:832\n"
    "v_mfma_f32_16x16x32_bf16 v[160:163], v[224:227], v[52:55], v[160:163]\n"
    "v_cvt_pk_bf16_f32 v250, v136, v137\n"
    "v_cvt_pk_bf16_f32 v251, v138, v139\n"
    "v_mfma_f32_16x16x32_bf16 v[164:167], v[224:227], v[56:59], v[164:167]\n"
    "s_waitcnt lgkmcnt(4)\n"
    "v_accvgpr_write_b32 a20, v250\n"
    "v_accvgpr_write_b32 a21, v251\n"
    "v_mfma_f32_16x16x32_bf16 v[156:159], v[228:231], v[60:63], v[156:159]\n"
    "ds_read_b64_tr_b16 v[224:225], v245 offset:192\n"
    "ds_read_b64_tr_b16 v[226:227], v245 offset:416\n"
    "v_mfma_f32_16x16x32_bf16 v[160:163], v[228:231], v[64:67], v[160:163]\n"
    "v_cvt_pk_bf16_f32 v252, v140, v141\n"
    "v_cvt_pk_bf16_f32 v253, v142, v143\n"
    "v_mfma_f32_16x16x32_bf16 v[164:167], v[228:231], v[68:71], v[164:167]\n"
    "s_waitcnt lgkmcnt(4)\n"
    "v_accvgpr_write_b32 a22, v252\n"
    "v_accvgpr_write_b32 a23, v253\n"
    "v_mfma_f32_16x16x32_bf16 v[156:159], v[232:235], v[72:75], v[156:159]\n"
    "ds_read_b64_tr_b16 v[228:229], v245 offset:640\n"
    "ds_read_b64_tr_b16 v[230:231], v245 offset:864\n"
    "v_mfma_f32_16x16x32_bf16 v[160:163], v[232:235], v[76:79], v[160:163]\n"
    "v_add_u32_e64 v245, s52, v255\n"
    "v_cvt_pk_bf16_f32 v250, v144, v145\n"
    "v_mfma_f32_16x16x32_bf16 v[164:167], v[232:235], v[80:83], v[164:167]\n"
    "s_waitcnt lgkmcnt(4)\n"
    "v_cvt_pk_bf16_f32 v251, v146, v147\n"
    "v_accvgpr_write_b32 a24, v250\n"
    "v_mfma_f32_16x16x32_bf16 v[156:159], v[236:239], v[84:87], v[156:159]\n"
    "s_add_u32 s52, s52, 16896\n"
    "ds_read_b64_tr_b16 v[232:233], v246 offset:192\n"
    "ds_read_b64_tr_b16 v[234:235], v246 offset:416\n"
    "v_mfma_f32_16x16x32_bf16 v[160:163], v[236:239], v[88:91], v[160:163]\n"
    "s_cmp_eq_u32 s52, s53\n"
    "v_accvgpr_write_b32 a25, v251\n"
    "v_cvt_pk_bf16_f32 v252, v148, v149\n"
    "v_mfma_f32_16x16x32_bf16 v[164:167], v[236:239], v[92:95], v[164:167]\n"
    "s_waitcnt lgkmcnt(4)\n"
    "v_cvt_pk_bf16_f32 v253, v150, v151\n"
    "v_accvgpr_write_b32 a26, v252\n"
    "v_mfma_f32_16x16x32_bf16 v[168:171], v[224:227], v[48:51], v[168:171]\n"
    "s_cselect_b32 s52, s79, s52\n"
    "ds_read_b64_tr_b16 v[236:237], v246 offset:640\n"
    "ds_read_b64_tr_b16 v[238:239], v246 offset:864\n"
    "v_mfma_f32_16x16x32_bf16 v[172:175], v[224:227], v[52:55], v[172:175]\n"
    "s_waitcnt vmcnt(20)\n"
    "v_accvgpr_write_b32 a27, v253\n"
    "v_cvt_pk_bf16_f32 v250, v152, v153\n"
    "v_mfma_f32_16x16x32_bf16 v[176:179], v[224:227], v[56:59], v[176:179]\n"
    "s_waitcnt lgkmcnt(4)\n"
    "v_cvt_pk_bf16_f32 v251, v154, v155\n"
    "v_accvgpr_write_b32 a28, v250\n"
    "v_mfma_f32_16x16x32_bf16 v[168:171], v[228:231], v[60:63], v[168:171]\n"
    "s_barrier\n"
    "ds_read_b64_tr_b16 v[224:225], v245 offset:0\n"
    "ds_read_b64_tr_b16 v[226:227], v245 offset:224\n"
    "v_mfma_f32_16x16x32_bf16 v[172:175], v[228:231], v[64:67], v[172:175]\n"
    "v_accvgpr_write_b32 a29, v251\n"
    "v_cvt_pk_bf16_f32 v252, v156, v157\n"
    "v_mfma_f32_16x16x32_bf16 v[176:179], v[228:231], v[68:71], v[176:179]\n"
    "s_waitcnt lgkmcnt(4)\n"
    "v_cvt_pk_bf16_f32 v253, v158, v159\n"
    "v_accvgpr_write_b32 a30, v252\n"
    "v_mfma_f32_16x16x32_bf16 v[168:171], v[232:235], v[72:75], v[168:171]\n"
    "ds_read_b64_tr_b16 v[228:229], v245 offset:32\n"
    "ds_read_b64_tr_b16 v[230:231], v245 offset:256\n"
    "v_mfma_f32_16x16x32_bf16 v[172:175], v[232:235], v[76:79], v[172:175]\n"
    "v_accvgpr_write_b32 a31, v253\n"
    "v_cvt_pk_bf16_f32 v250, v160, v161\n"
    "v_mfma_f32_16x16x32_bf16 v[176:179], v[232:235], v[80:83], v[176:179]\n"
    "s_waitcnt lgkmcnt(4)\n"
    "v_cvt_pk_bf16_f32 v251, v162, v163\n"
    "v_accvgpr_write_b32 a32, v250\n"
    "v_mfma_f32_16x16x32_bf16 v[168:171], v[236:239], v[84:87], v[168:171]\n"
    "ds_read_b64_tr_b16 v[232:233], v245 offset:64\n"
    "ds_read_b64_tr_b16 v[234:235], v245 offset:288\n"
    "v_mfma_f32_16x16x32_bf16 v[172:175], v[236:239], v[88:91], v[172:175]\n"
    "v_accvgpr_write_b32 a33, v251\n"
    "v_mfma_f32_16x16x32_bf16 v[176:179], v[236:239], v[92:95], v[176:179]\n"
    "s_waitcnt lgkmcnt(4)\n"
    "s_sub_u32 s70, s70, 1\n"
    "s_cmp_lg_u32 s70, 0\n"
    "s_cbranch_scc1 L_outp_%=\n"
    "buffer_load_dword v187, v183, s[44:47], s61 offen sc1\n"
    "v_mbcnt_lo_u32_b32 v188, -1, 0\n"
    "v_mbcnt_hi_u32_b32 v188, -1, v188\n"
    "v_lshlrev_b32 v188, 2, v188\n"
    "v_add_u32 v188, s79, v188\n"
    "v_add_u32 v188, 152064, v188\n"
    "ds_read_b32 v184, v188\n"
    "ds_read_b32 v185, v188 offset:256\n"
    "ds_read_b32 v186, v188 offset:512\n"
    "buffer_store_dwordx4 a[0:3], v240, s[40:43], s73 offen offset:0 sc1\n"
    "buffer_store_dwordx4 a[4:7], v240, s[40:43], s73 offen offset:1024 sc1\n"
    "buffer_store_dwordx4 a[8:11], v240, s[40:43], s73 offen offset:2048 sc1\n"
    "buffer_store_dwordx4 a[12:15], v240, s[40:43], s73 offen offset:3072 sc1\n"
    "s_add_u32 s81, s73, 4096\n"
    "buffer_store_dwordx4 a[16:19], v240, s[40:43], s81 offen offset:0 sc1\n"
    "buffer_store_dwordx4 a[20:23], v240, s[40:43], s81 offen offset:1024 sc1\n"
    "buffer_store_dwordx4 a[24:27], v240, s[40:43], s81 offen offset:2048 sc1\n"
    "buffer_store_dwordx4 a[28:31], v240, s[40:43], s81 offen offset:3072 sc1\n"
    "v_cvt_pk_bf16_f32 v252, v164, v165\n"
    "v_cvt_pk_bf16_f32 v253, v166, v167\n"
    "v_accvgpr_write_b32 a34, v252\n"
    "v_accvgpr_write_b32 a35, v253\n"
    "v_cvt_pk_bf16_f32 v250, v168, v169\n"
    "v_cvt_pk_bf16_f32 v251, v170, v171\n"
    "v_accvgpr_write_b32 a36, v250\n"
    "v_accvgpr_write_b32 a37, v251\n"
    "v_cvt_pk_bf16_f32 v252, v172, v173\n"
    "v_cvt_pk_bf16_f32 v253, v174, v175\n"
    "v_accvgpr_write_b32 a38, v252\n"
    "v_accvgpr_write_b32 a39, v253\n"
    "v_cvt_pk_bf16_f32 v250, v176, v177\n"
    "v_cvt_pk_bf16_f32 v251, v178, v179\n"
    "v_accvgpr_write_b32 a40, v250\n"
    "v_accvgpr_write_b32 a41, v251\n"
    "s_add_u32 s81, s73, 8192\n"
    "buffer_store_dwordx4 a[32:35], v240, s[40:43], s81 offen offset:0 sc1\n"
    "buffer_store_dwordx4 a[36:39], v240, s[40:43], s81 offen offset:1024 sc1\n"
    "buffer_store_dwordx2 a[40:41], v241, s[40:43], s81 offen offset:2048 sc1\n"
    "s_lshr_b32 s72, s100, 10\n"
    "s_and_b32 s72, s72, 1\n"
    "s_sub_u32 s73, 1, s72\n"
    "s_waitcnt vmcnt(0) lgkmcnt(0)\n"
    "s_barrier\n"
    "v_add_u32 v188, 1, v181\n"
    "buffer_store_dword v188, v182, s[44:47], s63 offen sc1\n"
    "s_nop 1\n"
    "v_sub_u32 v188, v187, v184\n"
    "v_subrev_u32 v188, s72, v188\n"
    "v_cmp_ge_u32 vcc, s73, v188\n"
    "s_nop 4\n"
    "s_and_b32 s74, vcc_lo, 255\n"
    "s_cmp_eq_u32 s74, 255\n"
    "s_cbranch_scc1 L_got_%=\n"
    "s_mov_b32 s65, 50000\n"
    "L_sb2_%=:\n"
    "buffer_load_dword v187, v183, s[44:47], s61 offen sc1\n"
    "buffer_load_dword v188, v183, s[44:47], s60 offen sc1\n"
    "s_waitcnt vmcnt(0)\n"
    "v_cmp_ne_u32 vcc, v188, v181\n"
    "s_nop 4\n"
    "s_cbranch_vccz L_sb2n_%=\n"
    "v_mov_b32_e64 v181, v188\n"
    "v_add_u32 v188, 1, v181\n"
    "buffer_store_dword v188, v182, s[44:47], s63 offen sc1\n"
    "s_nop 1\n"
    "L_sb2n_%=:\n"
    "v_sub_u32 v188, v187, v184\n"
    "v_subrev_u32 v188, s72, v188\n"
    "v_cmp_ge_u32 vcc, s73, v188\n"
    "s_nop 4\n"
    "s_and_b32 s74, vcc_lo, 255\n"
    "s_cmp_eq_u32 s74, 255\n"
    "s_cbranch_scc1 L_got_%=\n"
    "s_sleep 1\n"
    "s_sub_u32 s65, s65, 1\n"
    "s_cmp_lg_u32 s65, 0\n"
    "s_cbranch_scc1 L_sb2_%=\n"
    "L_got_%=:\n"
    "v_add_u32 v186, 1, v186\n"
    "buffer_store_dword v186, v182, s[44:47], s64 offen sc1\n"
    "s_nop 1\n"
    "L_loads_%=:\n"
    "s_lshr_b32 s66, s100, 10\n"
    "s_mul_i32 s66, s66, 172032\n"
    "s_sub_u32 s24, s40, s66\n"
    "s_subb_u32 s25, s41, 0\n"
    "s_mov_b32 s26, 1376256\n"
    "s_mov_b32 s27, 131072\n"
    "s_and_b32 s68, s48, 1\n"
    "s_lshr_b32 s69, s48, 1\n"
    "s_lshr_b32 s66, s100, 9\n"
    "s_add_u32 s66, s66, s69\n"
    "s_mul_i32 s66, s66, 10752\n"
    "s_mul_i32 s67, s68, 5120\n"
    "s_add_u32 s66, s66, s67\n"
    "v_mbcnt_lo_u32_b32 v202, -1, 0\n"
    "v_mbcnt_hi_u32_b32 v202, -1, v202\n"
    "v_lshlrev_b32 v200, 4, v202\n"
    "v_lshlrev_b32 v201, 3, v202\n"
    "s_waitcnt lgkmcnt(0)\n"
    "s_add_u32 s74, s66, 0\n"
    "buffer_load_dwordx4 v[0:3], v200, s[24:27], s74 offen offset:0 sc1\n"
    "s_add_u32 s75, s66, 172032\n"
    "buffer_load_dwordx4 v[4:7], v200, s[24:27], s75 offen offset:0 sc1\n"
    "s_add_u32 s74, s66, 344064\n"
    "buffer_load_dwordx4 v[8:11], v200, s[24:27], s74 offen offset:0 sc1\n"
    "s_add_u32 s75, s66, 516096\n"
    "buffer_load_dwordx4 v[12:15], v200, s[24:27], s75 offen offset:0 sc1\n"
    "s_add_u32 s74, s66, 688128\n"
    "buffer_load_dwordx4 v[16:19], v200, s[24:27], s74 offen offset:0 sc1\n"
    "s_add_u32 s75, s66, 860160\n"
    "buffer_load_dwordx4 v[20:23], v200, s[24:27], s75 offen offset:0 sc1\n"
    "s_add_u32 s74, s66, 1032192\n"
    "buffer_load_dwordx4 v[24:27], v200, s[24:27], s74 offen offset:0 sc1\n"
    "s_add_u32 s75, s66, 1204224\n"
    "buffer_load_dwordx4 v[28:31], v200, s[24:27], s75 offen offset:0 sc1\n"
    "s_add_u32 s74, s66, 0\n"
    "buffer_load_dwordx4 v[32:35], v200, s[24:27], s74 offen offset:1024 sc1\n"
    "s_add_u32 s75, s66, 172032\n"
    "buffer_load_dwordx4 v[36:39], v200, s[24:27], s75 offen offset:1024 sc1\n"
    "s_add_u32 s74, s66, 344064\n"
    "buffer_load_dwordx4 v[40:43], v200, s[24:27], s74 offen offset:1024 sc1\n"
    "s_add_u32 s75, s66, 516096\n"
    "buffer_load_dwordx4 v[44:47], v200, s[24:27], s75 offen offset:1024 sc1\n"
    "s_add_u32 s74, s66, 688128\n"
    "buffer_load_dwordx4 v[48:51], v200, s[24:27], s74 offen offset:1024 sc1\n"
    "s_add_u32 s75, s66, 860160\n"
    "buffer_load_dwordx4 v[52:55], v200, s[24:27], s75 offen offset:1024 sc1\n"
    "s_add_u32 s74, s66, 1032192\n"
    "buffer_load_dwordx4 v[56:59], v200, s[24:27], s74 offen offset:1024 sc1\n"
    "s_add_u32 s75, s66, 1204224\n"
    "buffer_load_dwordx4 v[60:63], v200, s[24:27], s75 offen offset:1024 sc1\n"
    "s_add_u32 s74, s66, 0\n"
    "buffer_load_dwordx4 v[64:67], v200, s[24:27], s74 offen offset:2048 sc1\n"
    "s_add_u32 s75, s66, 172032\n"
    "buffer_load_dwordx4 v[68:71], v200, s[24:27], s75 offen offset:2048 sc1\n"
    "s_add_u32 s74, s66, 344064\n"
    "buffer_load_dwordx4 v[72:75], v200, s[24:27], s74 offen offset:2048 sc1\n"
    "s_add_u32 s75, s66, 516096\n"
    "buffer_load_dwordx4 v[76:79], v200, s[24:27], s75 offen offset:2048 sc1\n"
    "s_add_u32 s74, s66, 688128\n"
    "buffer_load_dwordx4 v[80:83], v200, s[24:27], s74 offen offset:2048 sc1\n"
    "s_add_u32 s75, s66, 860160\n"
    "buffer_load_dwordx4 v[84:87], v200, s[24:27], s75 offen offset:2048 sc1\n"
    "s_add_u32 s74, s66, 1032192\n"
    "buffer_load_dwordx4 v[88:91], v200, s[24:27], s74 offen offset:2048 sc1\n"
    "s_add_u32 s75, s66, 1204224\n"
    "buffer_load_dwordx4 v[92:95], v200, s[24:27], s75 offen offset:2048 sc1\n"
    "s_add_u32 s74, s66, 0\n"
    "buffer_load_dwordx4 v[96:99], v200, s[24:27], s74 offen offset:3072 sc1\n"
    "s_add_u32 s75, s66, 172032\n"
    "buffer_load_dwordx4 v[100:103], v200, s[24:27], s75 offen offset:3072 sc1\n"
    "s_add_u32 s74, s66, 344064\n"
    "buffer_load_dwordx4 v[104:107], v200, s[24:27], s74 offen offset:3072 sc1\n"
    "s_add_u32 s75, s66, 516096\n"
    "buffer_load_dwordx4 v[108:111], v200, s[24:27], s75 offen offset:3072 sc1\n"
    "s_add_u32 s74, s66, 688128\n"
    "buffer_load_dwordx4 v[112:115], v200, s[24:27], s74 offen offset:3072 sc1\n"
    "s_add_u32 s75, s66, 860160\n"
    "buffer_load_dwordx4 v[116:119], v200, s[24:27], s75 offen offset:3072 sc1\n"
    "s_add_u32 s74, s66, 1032192\n"
    "buffer_load_dwordx4 v[120:123], v200, s[24:27], s74 offen offset:3072 sc1\n"
    "s_add_u32 s75, s66, 1204224\n"
    "buffer_load_dwordx4 v[124:127], v200, s[24:27], s75 offen offset:3072 sc1\n"
    "s_add_u32 s74, s66, 4096\n"
    "buffer_load_dwordx4 v[208:211], v200, s[24:27], s74 offen offset:0 sc1\n"
    "s_add_u32 s75, s66, 176128\n"
    "buffer_load_dwordx4 v[212:215], v200, s[24:27], s75 offen offset:0 sc1\n"
    "s_add_u32 s74, s66, 348160\n"
    "buffer_load_dwordx4 v[216:219], v200, s[24:27], s74 offen offset:0 sc1\n"
    "s_add_u32 s75, s66, 520192\n"
    "buffer_load_dwordx4 v[220:223], v200, s[24:27], s75 offen offset:0 sc1\n"
    "s_add_u32 s74, s66, 692224\n"
    "buffer_load_dwordx4 v[224:227], v200, s[24:27], s74 offen offset:0 sc1\n"
    "s_add_u32 s75, s66, 864256\n"
    "buffer_load_dwordx4 v[228:231], v200, s[24:27], s75 offen offset:0 sc1\n"
    "s_add_u32 s74, s66, 1036288\n"
    "buffer_load_dwordx4 v[232:235], v200, s[24:27], s74 offen offset:0 sc1\n"
    "s_add_u32 s75, s66, 1208320\n"
    "buffer_load_dwordx4 v[236:239], v200, s[24:27], s75 offen offset:0 sc1\n"
    "s_add_u32 s74, s66, 5120\n"
    "buffer_load_dwordx2 v[160:161], v201, s[24:27], s74 offen sc1\n"
    "s_add_u32 s75, s66, 177152\n"
    "buffer_load_dwordx2 v[162:163], v201, s[24:27], s75 offen sc1\n"
    "s_add_u32 s74, s66, 349184\n"
    "buffer_load_dwordx2 v[164:165], v201, s[24:27], s74 offen sc1\n"
    "s_add_u32 s75, s66, 521216\n"
    "buffer_load_dwordx2 v[166:167], v201, s[24:27], s75 offen sc1\n"
    "s_add_u32 s74, s66, 693248\n"
    "buffer_load_dwordx2 v[168:169], v201, s[24:27], s74 offen sc1\n"
    "s_add_u32 s75, s66, 865280\n"
    "buffer_load_dwordx2 v[170:171], v201, s[24:27], s75 offen sc1\n"
    "s_add_u32 s74, s66, 1037312\n"
    "buffer_load_dwordx2 v[172:173], v201, s[24:27], s74 offen sc1\n"
    "s_add_u32 s75, s66, 1209344\n"
    "buffer_load_dwordx2 v[174:175], v201, s[24:27], s75 offen sc1\n"
    "s_mov_b32 s20, s3\n"
    "s_and_b32 s21, s11, 65535\n"
    "s_mov_b32 s22, 172032\n"
    "s_mov_b32 s23, 131072\n"
    "s_lshr_b32 s82, s100, 11\n"
    "s_mul_i32 s82, s82, 43008\n"
    "s_lshr_b32 s67, s100, 10\n"
    "s_and_b32 s67, s67, 1\n"
    "s_mul_i32 s67, s67, 448\n"
    "s_add_u32 s82, s82, s67\n"
    "s_mul_i32 s67, s48, 336\n"
    "v_add_u32 v140, s67, v202\n"
    "v_add_u32 v141, 0, v140\n"
    "v_mul_u32_u24 v142, 2341, v141\n"
    "v_lshrrev_b32 v142, 16, v142\n"
    "v_mul_u32_u24 v143, 28, v142\n"
    "v_sub_u32 v143, v141, v143\n"
    "v_lshlrev_b32 v143, 4, v143\n"
    "v_mul_u32_u24 v128, 464, v142\n"
    "v_add_u32 v128, v128, v143\n"
    "v_add_u32 v128, s79, v128\n"
    "v_mul_u32_u24 v129, 896, v142\n"
    "v_add_u32 v129, v129, v143\n"
    "v_add_u32 v141, 64, v140\n"
    "v_mul_u32_u24 v142, 2341, v141\n"
    "v_lshrrev_b32 v142, 16, v142\n"
    "v_mul_u32_u24 v143, 28, v142\n"
    "v_sub_u32 v143, v141, v143\n"
    "v_lshlrev_b32 v143, 4, v143\n"
    "v_mul_u32_u24 v130, 464, v142\n"
    "v_add_u32 v130, v130, v143\n"
    "v_add_u32 v130, s79, v130\n"
    "v_mul_u32_u24 v131, 896, v142\n"
    "v_add_u32 v131, v131, v143\n"
    "v_add_u32 v141, 128, v140\n"
    "v_mul_u32_u24 v142, 2341, v141\n"
    "v_lshrrev_b32 v142, 16, v142\n"
    "v_mul_u32_u24 v143, 28, v142\n"
    "v_sub_u32 v143, v141, v143\n"
    "v_lshlrev_b32 v143, 4, v143\n"
    "v_mul_u32_u24 v132, 464, v142\n"
    "v_add_u32 v132, v132, v143\n"
    "v_add_u32 v132, s79, v132\n"
    "v_mul_u32_u24 v133, 896, v142\n"
    "v_add_u32 v133, v133, v143\n"
    "v_add_u32 v141, 192, v140\n"
    "v_mul_u32_u24 v142, 2341, v141\n"
    "v_lshrrev_b32 v142, 16, v142\n"
    "v_mul_u32_u24 v143, 28, v142\n"
    "v_sub_u32 v143, v141, v143\n"
    "v_lshlrev_b32 v143, 4, v143\n"
    "v_mul_u32_u24 v134, 464, v142\n"
    "v_add_u32 v134, v134, v143\n"
    "v_add_u32 v134, s79, v134\n"
    "v_mul_u32_u24 v135, 896, v142\n"
    "v_add_u32 v135, v135, v143\n"
    "v_add_u32 v141, 256, v140\n"
    "v_mul_u32_u24 v142, 2341, v141\n"
    "v_lshrrev_b32 v142, 16, v142\n"
    "v_mul_u32_u24 v143, 28, v142\n"
    "v_sub_u32 v143, v141, v143\n"
    "v_lshlrev_b32 v143, 4, v143\n"
    "v_mul_u32_u24 v136, 464, v142\n"
    "v_add_u32 v136, v136, v143\n"
    "v_add_u32 v136, s79, v136\n"
    "v_mul_u32_u24 v137, 896, v142\n"
    "v_add_u32 v137, v137, v143\n"
    "v_add_u32 v141, 320, v140\n"
    "v_mul_u32_u24 v142, 2341, v141\n"
    "v_lshrrev_b32 v142, 16, v142\n"
    "v_mul_u32_u24 v143, 28, v142\n"
    "v_sub_u32 v143, v141, v143\n"
    "v_lshlrev_b32 v143, 4, v143\n"
    "v_mul_u32_u24 v138, 464, v142\n"
    "v_add_u32 v138, v138, v143\n"
    "v_add_u32 v138, s79, v138\n"
    "v_mul_u32_u24 v139, 896, v142\n"
    "v_add_u32 v139, v139, v143\n"
    "v_cmp_gt_u32 vcc, 16, v202\n"
    "v_mov_b32 v141, 1048576\n"
    "s_nop 0\n"
    "v_cndmask_b32 v139, v141, v139, vcc\n"
    "v_mov_b32 v141, 0\n"
    "v_cndmask_b32 v138, v141, v138, vcc\n"
    "L_sums_%=:\n"
    "v_and_b32 v203, 15, v202\n"
    "v_mul_u32_u24 v203, 464, v203\n"
    "v_lshrrev_b32 v204, 4, v202\n"
    "v_lshlrev_b32 v204, 3, v204\n"
    "v_add_u32 v203, v203, v204\n"
    "s_mul_i32 s67, s69, 224\n"
    "s_add_u32 s67, s67, s79\n"
    "v_add_u32 v203, s67, v203\n"
    "s_mov_b32 s70, 16256\n"
    "s_mov_b32 s71, 1065353216\n"
    "s_mul_i32 s49, s68, 7520\n"
    "s_add_u32 s49, s49, 0\n"
    "s_mul_i32 s50, s68, 7520\n"
    "s_add_u32 s50, s50, 7424\n"
    "s_mul_i32 s51, s68, 4294952576\n"
    "s_add_u32 s51, s51, 14848\n"
    "s_mul_i32 s52, s68, 7520\n"
    "s_add_u32 s52, s52, 32\n"
    "s_mul_i32 s53, s68, 7520\n"
    "s_add_u32 s53, s53, 7456\n"
    "s_mul_i32 s54, s68, 4294952576\n"
    "s_add_u32 s54, s54, 14880\n"
    "s_mul_i32 s55, s68, 7520\n"
    "s_add_u32 s55, s55, 64\n"
    "s_mul_i32 s56, s68, 7520\n"
    "s_add_u32 s56, s56, 7488\n"
    "s_mul_i32 s57, s68, 4294952576\n"
    "s_add_u32 s57, s57, 14912\n"
    "s_mul_i32 s58, s68, 7520\n"
    "s_add_u32 s58, s58, 96\n"
    "s_mul_i32 s59, s68, 4294960064\n"
    "s_add_u32 s59, s59, 22272\n"
    "s_waitcnt vmcnt(40)\n"
    ".p2align 3\n"
    "v_dot2_f32_bf16 v240, v0, s70, 0\n"
    "v_dot2_f32_bf16 v241, v0, s71, 0\n"
    "v_dot2_f32_bf16 v242, v1, s70, 0\n"
    "v_dot2_f32_bf16 v243, v1, s71, 0\n"
    "v_dot2_f32_bf16 v244, v2, s70, 0\n"
    "v_dot2_f32_bf16 v245, v2, s71, 0\n"
    "v_dot2_f32_bf16 v246, v3, s70, 0\n"
    "v_dot2_f32_bf16 v247, v3, s71, 0\n"
    "v_dot2_f32_bf16 v240, v4, s70, v240\n"
    "v_dot2_f32_bf16 v241, v4, s71, v241\n"
    "v_dot2_f32_bf16 v242, v5, s70, v242\n"
    "v_dot2_f32_bf16 v243, v5, s71, v243\n"
    "v_dot2_f32_bf16 v244, v6, s70, v244\n"
    "v_dot2_f32_bf16 v245, v6, s71, v245\n"
    "v_dot2_f32_bf16 v246, v7, s70, v246\n"
    "v_dot2_f32_bf16 v247, v7, s71, v247\n"
    "v_dot2_f32_bf16 v240, v8, s70, v240\n"
    "v_dot2_f32_bf16 v241, v8, s71, v241\n"
    "v_dot2_f32_bf16 v242, v9, s70, v242\n"
    "v_dot2_f32_bf16 v243, v9, s71, v243\n"
    "v_dot2_f32_bf16 v244, v10, s70, v244\n"
    "v_dot2_f32_bf16 v245, v10, s71, v245\n"
    "v_dot2_f32_bf16 v246, v11, s70, v246\n"
    "v_dot2_f32_bf16 v247, v11, s71, v247\n"
    "v_dot2_f32_bf16 v240, v12, s70, v240\n"
    "v_dot2_f32_bf16 v241, v12, s71, v241\n"
    "v_dot2_f32_bf16 v242, v13, s70, v242\n"
    "v_dot2_f32_bf16 v243, v13, s71, v243\n"
    "v_dot2_f32_bf16 v244, v14, s70, v244\n"
    "v_dot2_f32_bf16 v245, v14, s71, v245\n"
    "v_dot2_f32_bf16 v246, v15, s70, v246\n"
    "v_dot2_f32_bf16 v247, v15, s71, v247\n"
    "v_dot2_f32_bf16 v240, v16, s70, v240\n"
    "v_dot2_f32_bf16 v241, v16, s71, v241\n"
    "v_dot2_f32_bf16 v242, v17, s70, v242\n"
    "v_dot2_f32_bf16 v243, v17, s71, v243\n"
    "v_dot2_f32_bf16 v244, v18, s70, v244\n"
    "v_dot2_f32_bf16 v245, v18, s71, v245\n"
    "v_dot2_f32_bf16 v246, v19, s70, v246\n"
    "v_dot2_f32_bf16 v247, v19, s71, v247\n"
    "v_dot2_f32_bf16 v240, v20, s70, v240\n"
    "v_dot2_f32_bf16 v241, v20, s71, v241\n"
    "v_dot2_f32_bf16 v242, v21, s70, v242\n"
    "v_dot2_f32_bf16 v243, v21, s71, v243\n"
    "v_dot2_f32_bf16 v244, v22, s70, v244\n"
    "v_dot2_f32_bf16 v245, v22, s71, v245\n"
    "v_dot2_f32_bf16 v246, v23, s70, v246\n"
    "v_dot2_f32_bf16 v247, v23, s71, v247\n"
    "v_dot2_f32_bf16 v240, v24, s70, v240\n"
    "v_dot2_f32_bf16 v241, v24, s71, v241\n"
    "v_dot2_f32_bf16 v242, v25, s70, v242\n"
    "v_dot2_f32_bf16 v243, v25, s71, v243\n"
    "v_dot2_f32_bf16 v244, v26, s70, v244\n"
    "v_dot2_f32_bf16 v245, v26, s71, v245\n"
    "v_dot2_f32_bf16 v246, v27, s70, v246\n"
    "v_dot2_f32_bf16 v247, v27, s71, v247\n"
    "v_dot2_f32_bf16 v240, v28, s70, v240\n"
    "v_dot2_f32_bf16 v241, v28, s71, v241\n"
    "v_dot2_f32_bf16 v242, v29, s70, v242\n"
    "v_dot2_f32_bf16 v243, v29, s71, v243\n"
    "v_dot2_f32_bf16 v244, v30, s70, v244\n"
    "v_dot2_f32_bf16 v245, v30, s71, v245\n"
    "v_dot2_f32_bf16 v246, v31, s70, v246\n"
    "v_dot2_f32_bf16 v247, v31, s71, v247\n"
    "v_cvt_pk_bf16_f32 v248, v240, v241\n"
    "v_cvt_pk_bf16_f32 v249, v242, v243\n"
    "v_cvt_pk_bf16_f32 v250, v244, v245\n"
    "v_cvt_pk_bf16_f32 v251, v246, v247\n"
    "v_add_u32 v204, s49, v203\n"
    "ds_write_b64 v204, v[248:249]\n"
    "v_add_u32 v204, s50, v203\n"
    "ds_write_b64 v204, v[250:251]\n"
    "s_waitcnt vmcnt(32)\n"
    ".p2align 3\n"
    "v_dot2_f32_bf16 v240, v32, s70, 0\n"
    "v_dot2_f32_bf16 v241, v32, s71, 0\n"
    "v_dot2_f32_bf16 v242, v33, s70, 0\n"
    "v_dot2_f32_bf16 v243, v33, s71, 0\n"
    "v_dot2_f32_bf16 v244, v34, s70, 0\n"
    "v_dot2_f32_bf16 v245, v34, s71, 0\n"
    "v_dot2_f32_bf16 v246, v35, s70, 0\n"
    "v_dot2_f32_bf16 v247, v35, s71, 0\n"
    "v_dot2_f32_bf16 v240, v36, s70, v240\n"
    "v_dot2_f32_bf16 v241, v36, s71, v241\n"
    "v_dot2_f32_bf16 v242, v37, s70, v242\n"
    "v_dot2_f32_bf16 v243, v37, s71, v243\n"
    "v_dot2_f32_bf16 v244, v38, s70, v244\n"
    "v_dot2_f32_bf16 v245, v38, s71, v245\n"
    "v_dot2_f32_bf16 v246, v39, s70, v246\n"
    "v_dot2_f32_bf16 v247, v39, s71, v247\n"
    "v_dot2_f32_bf16 v240, v40, s70, v240\n"
    "v_dot2_f32_bf16 v241, v40, s71, v241\n"
    "v_dot2_f32_bf16 v242, v41, s70, v242\n"
    "v_dot2_f32_bf16 v243, v41, s71, v243\n"
    "v_dot2_f32_bf16 v244, v42, s70, v244\n"
    "v_dot2_f32_bf16 v245, v42, s71, v245\n"
    "v_dot2_f32_bf16 v246, v43, s70, v246\n"
    "v_dot2_f32_bf16 v247, v43, s71, v247\n"
    "v_dot2_f32_bf16 v240, v44, s70, v240\n"
    "v_dot2_f32_bf16 v241, v44, s71, v241\n"
    "v_dot2_f32_bf16 v242, v45, s70, v242\n"
    "v_dot2_f32_bf16 v243, v45, s71, v243\n"
    "v_dot2_f32_bf16 v244, v46, s70, v244\n"
    "v_dot2_f32_bf16 v245, v46, s71, v245\n"
    "v_dot2_f32_bf16 v246, v47, s70, v246\n"
    "v_dot2_f32_bf16 v247, v47, s71, v247\n"
    "v_dot2_f32_bf16 v240, v48, s70, v240\n"
    "v_dot2_f32_bf16 v241, v48, s71, v241\n"
    "v_dot2_f32_bf16 v242, v49, s70, v242\n"
    "v_dot2_f32_bf16 v243, v49, s71, v243\n"
    "v_dot2_f32_bf16 v244, v50, s70, v244\n"
    "v_dot2_f32_bf16 v245, v50, s71, v245\n"
    "v_dot2_f32_bf16 v246, v51, s70, v246\n"
    "v_dot2_f32_bf16 v247, v51, s71, v247\n"
    "v_dot2_f32_bf16 v240, v52, s70, v240\n"
    "v_dot2_f32_bf16 v241, v52, s71, v241\n"
    "v_dot2_f32_bf16 v242, v53, s70, v242\n"
    "v_dot2_f32_bf16 v243, v53, s71, v243\n"
    "v_dot2_f32_bf16 v244, v54, s70, v244\n"
    "v_dot2_f32_bf16 v245, v54, s71, v245\n"
    "v_dot2_f32_bf16 v246, v55, s70, v246\n"
    "v_dot2_f32_bf16 v247, v55, s71, v247\n"
    "v_dot2_f32_bf16 v240, v56, s70, v240\n"
    "v_dot2_f32_bf16 v241, v56, s71, v241\n"
    "v_dot2_f32_bf16 v242, v57, s70, v242\n"
    "v_dot2_f32_bf16 v243, v57, s71, v243\n"
    "v_dot2_f32_bf16 v244, v58, s70, v244\n"
    "v_dot2_f32_bf16 v245, v58, s71, v245\n"
    "v_dot2_f32_bf16 v246, v59, s70, v246\n"
    "v_dot2_f32_bf16 v247, v59, s71, v247\n"
    "v_dot2_f32_bf16 v240, v60, s70, v240\n"
    "v_dot2_f32_bf16 v241, v60, s71, v241\n"
    "v_dot2_f32_bf16 v242, v61, s70, v242\n"
    "v_dot2_f32_bf16 v243, v61, s71, v243\n"
    "v_dot2_f32_bf16 v244, v62, s70, v244\n"
    "v_dot2_f32_bf16 v245, v62, s71, v245\n"
    "v_dot2_f32_bf16 v246, v63, s70, v246\n"
    "v_dot2_f32_bf16 v247, v63, s71, v247\n"
    "v_cvt_pk_bf16_f32 v248, v240, v241\n"
    "v_cvt_pk_bf16_f32 v249, v242, v243\n"
    "v_cvt_pk_bf16_f32 v250, v244, v245\n"
    "v_cvt_pk_bf16_f32 v251, v246, v247\n"
    "v_add_u32 v204, s51, v203\n"
    "ds_write_b64 v204, v[248:249]\n"
    "v_add_u32 v204, s52, v203\n"
    "ds_write_b64 v204, v[250:251]\n"
    "s_waitcnt vmcnt(24)\n"
    ".p2align 3\n"
    "v_dot2_f32_bf16 v240, v64, s70, 0\n"
    "v_dot2_f32_bf16 v241, v64, s71, 0\n"
    "v_dot2_f32_bf16 v242, v65, s70, 0\n"
    "v_dot2_f32_bf16 v243, v65, s71, 0\n"
    "v_dot2_f32_bf16 v244, v66, s70, 0\n"
    "v_dot2_f32_bf16 v245, v66, s71, 0\n"
    "v_dot2_f32_bf16 v246, v67, s70, 0\n"
    "v_dot2_f32_bf16 v247, v67, s71, 0\n"
    "v_dot2_f32_bf16 v240, v68, s70, v240\n"
    "v_dot2_f32_bf16 v241, v68, s71, v241\n"
    "v_dot2_f32_bf16 v242, v69, s70, v242\n"
    "v_dot2_f32_bf16 v243, v69, s71, v243\n"
    "v_dot2_f32_bf16 v244, v70, s70, v244\n"
    "v_dot2_f32_bf16 v245, v70, s71, v245\n"
    "v_dot2_f32_bf16 v246, v71, s70, v246\n"
    "v_dot2_f32_bf16 v247, v71, s71, v247\n"
    "v_dot2_f32_bf16 v240, v72, s70, v240\n"
    "v_dot2_f32_bf16 v241, v72, s71, v241\n"
    "v_dot2_f32_bf16 v242, v73, s70, v242\n"
    "v_dot2_f32_bf16 v243, v73, s71, v243\n"
    "v_dot2_f32_bf16 v244, v74, s70, v244\n"
    "v_dot2_f32_bf16 v245, v74, s71, v245\n"
    "v_dot2_f32_bf16 v246, v75, s70, v246\n"
    "v_dot2_f32_bf16 v247, v75, s71, v247\n"
    "v_dot2_f32_bf16 v240, v76, s70, v240\n"
    "v_dot2_f32_bf16 v241, v76, s71, v241\n"
    "v_dot2_f32_bf16 v242, v77, s70, v242\n"
    "v_dot2_f32_bf16 v243, v77, s71, v243\n"
    "v_dot2_f32_bf16 v244, v78, s70, v244\n"
    "v_dot2_f32_bf16 v245, v78, s71, v245\n"
    "v_dot2_f32_bf16 v246, v79, s70, v246\n"
    "v_dot2_f32_bf16 v247, v79, s71, v247\n"
    "v_dot2_f32_bf16 v240, v80, s70, v240\n"
    "v_dot2_f32_bf16 v241, v80, s71, v241\n"
    "v_dot2_f32_bf16 v242, v81, s70, v242\n"
    "v_dot2_f32_bf16 v243, v81, s71, v243\n"
    "v_dot2_f32_bf16 v244, v82, s70, v244\n"
    "v_dot2_f32_bf16 v245, v82, s71, v245\n"
    "v_dot2_f32_bf16 v246, v83, s70, v246\n"
    "v_dot2_f32_bf16 v247, v83, s71, v247\n"
    "v_dot2_f32_bf16 v240, v84, s70, v240\n"
    "v_dot2_f32_bf16 v241, v84, s71, v241\n"
    "v_dot2_f32_bf16 v242, v85, s70, v242\n"
    "v_dot2_f32_bf16 v243, v85, s71, v243\n"
    "v_dot2_f32_bf16 v244, v86, s70, v244\n"
    "v_dot2_f32_bf16 v245, v86, s71, v245\n"
    "v_dot2_f32_bf16 v246, v87, s70, v246\n"
    "v_dot2_f32_bf16 v247, v87, s71, v247\n"
    "v_dot2_f32_bf16 v240, v88, s70, v240\n"
    "v_dot2_f32_bf16 v241, v88, s71, v241\n"
    "v_dot2_f32_bf16 v242, v89, s70, v242\n"
    "v_dot2_f32_bf16 v243, v89, s71, v243\n"
    "v_dot2_f32_bf16 v244, v90, s70, v244\n"
    "v_dot2_f32_bf16 v245, v90, s71, v245\n"
    "v_dot2_f32_bf16 v246, v91, s70, v246\n"
    "v_dot2_f32_bf16 v247, v91, s71, v247\n"
    "v_dot2_f32_bf16 v240, v92, s70, v240\n"
    "v_dot2_f32_bf16 v241, v92, s71, v241\n"
    "v_dot2_f32_bf16 v242, v93, s70, v242\n"
    "v_dot2_f32_bf16 v243, v93, s71, v243\n"
    "v_dot2_f32_bf16 v244, v94, s70, v244\n"
    "v_dot2_f32_bf16 v245, v94, s71, v245\n"
    "v_dot2_f32_bf16 v246, v95, s70, v246\n"
    "v_dot2_f32_bf16 v247, v95, s71, v247\n"
    "v_cvt_pk_bf16_f32 v248, v240, v241\n"
    "v_cvt_pk_bf16_f32 v249, v242, v243\n"
    "v_cvt_pk_bf16_f32 v250, v244, v245\n"
    "v_cvt_pk_bf16_f32 v251, v246, v247\n"
    "v_add_u32 v204, s53, v203\n"
    "ds_write_b64 v204, v[248:249]\n"
    "v_add_u32 v204, s54, v203\n"
    "ds_write_b64 v204, v[250:251]\n"
    "s_waitcnt vmcnt(16)\n"
    ".p2align 3\n"
    "v_dot2_f32_bf16 v240, v96, s70, 0\n"
    "v_dot2_f32_bf16 v241, v96, s71, 0\n"
    "v_dot2_f32_bf16 v242, v97, s70, 0\n"
    "v_dot2_f32_bf16 v243, v97, s71, 0\n"
    "v_dot2_f32_bf16 v244, v98, s70, 0\n"
    "v_dot2_f32_bf16 v245, v98, s71, 0\n"
    "v_dot2_f32_bf16 v246, v99, s70, 0\n"
    "v_dot2_f32_bf16 v247, v99, s71, 0\n"
    "v_dot2_f32_bf16 v240, v100, s70, v240\n"
    "v_dot2_f32_bf16 v241, v100, s71, v241\n"
    "v_dot2_f32_bf16 v242, v101, s70, v242\n"
    "v_dot2_f32_bf16 v243, v101, s71, v243\n"
    "v_dot2_f32_bf16 v244, v102, s70, v244\n"
    "v_dot2_f32_bf16 v245, v102, s71, v245\n"
    "v_dot2_f32_bf16 v246, v103, s70, v246\n"
    "v_dot2_f32_bf16 v247, v103, s71, v247\n"
    "v_dot2_f32_bf16 v240, v104, s70, v240\n"
    "v_dot2_f32_bf16 v241, v104, s71, v241\n"
    "v_dot2_f32_bf16 v242, v105, s70, v242\n"
    "v_dot2_f32_bf16 v243, v105, s71, v243\n"
    "v_dot2_f32_bf16 v244, v106, s70, v244\n"
    "v_dot2_f32_bf16 v245, v106, s71, v245\n"
    "v_dot2_f32_bf16 v246, v107, s70, v246\n"
    "v_dot2_f32_bf16 v247, v107, s71, v247\n"
    "v_dot2_f32_bf16 v240, v108, s70, v240\n"
    "v_dot2_f32_bf16 v241, v108, s71, v241\n"
    "v_dot2_f32_bf16 v242, v109, s70, v242\n"
    "v_dot2_f32_bf16 v243, v109, s71, v243\n"
    "v_dot2_f32_bf16 v244, v110, s70, v244\n"
    "v_dot2_f32_bf16 v245, v110, s71, v245\n"
    "v_dot2_f32_bf16 v246, v111, s70, v246\n"
    "v_dot2_f32_bf16 v247, v111, s71, v247\n"
    "v_dot2_f32_bf16 v240, v112, s70, v240\n"
    "v_dot2_f32_bf16 v241, v112, s71, v241\n"
    "v_dot2_f32_bf16 v242, v113, s70, v242\n"
    "v_dot2_f32_bf16 v243, v113, s71, v243\n"
    "v_dot2_f32_bf16 v244, v114, s70, v244\n"
    "v_dot2_f32_bf16 v245, v114, s71, v245\n"
    "v_dot2_f32_bf16 v246, v115, s70, v246\n"
    "v_dot2_f32_bf16 v247, v115, s71, v247\n"
    "v_dot2_f32_bf16 v240, v116, s70, v240\n"
    "v_dot2_f32_bf16 v241, v116, s71, v241\n"
    "v_dot2_f32_bf16 v242, v117, s70, v242\n"
    "v_dot2_f32_bf16 v243, v117, s71, v243\n"
    "v_dot2_f32_bf16 v244, v118, s70, v244\n"
    "v_dot2_f32_bf16 v245, v118, s71, v245\n"
    "v_dot2_f32_bf16 v246, v119, s70, v246\n"
    "v_dot2_f32_bf16 v247, v119, s71, v247\n"
    "v_dot2_f32_bf16 v240, v120, s70, v240\n"
    "v_dot2_f32_bf16 v241, v120, s71, v241\n"
    "v_dot2_f32_bf16 v242, v121, s70, v242\n"
    "v_dot2_f32_bf16 v243, v121, s71, v243\n"
    "v_dot2_f32_bf16 v244, v122, s70, v244\n"
    "v_dot2_f32_bf16 v245, v122, s71, v245\n"
    "v_dot2_f32_bf16 v246, v123, s70, v246\n"
    "v_dot2_f32_bf16 v247, v123, s71, v247\n"
    "v_dot2_f32_bf16 v240, v124, s70, v240\n"
    "v_dot2_f32_bf16 v241, v124, s71, v241\n"
    "v_dot2_f32_bf16 v242, v125, s70, v242\n"
    "v_dot2_f32_bf16 v243, v125, s71, v243\n"
    "v_dot2_f32_bf16 v244, v126, s70, v244\n"
    "v_dot2_f32_bf16 v245, v126, s71, v245\n"
    "v_dot2_f32_bf16 v246, v127, s70, v246\n"
    "v_dot2_f32_bf16 v247, v127, s71, v247\n"
    "v_cvt_pk_bf16_f32 v248, v240, v241\n"
    "v_cvt_pk_bf16_f32 v249, v242, v243\n"
    "v_cvt_pk_bf16_f32 v250, v244, v245\n"
    "v_cvt_pk_bf16_f32 v251, v246, v247\n"
    "v_add_u32 v204, s55, v203\n"
    "ds_write_b64 v204, v[248:249]\n"
    "v_add_u32 v204, s56, v203\n"
    "ds_write_b64 v204, v[250:251]\n"
    "s_waitcnt vmcnt(8)\n"
    ".p2align 3\n"
    "v_dot2_f32_bf16 v240, v208, s70, 0\n"
    "v_dot2_f32_bf16 v241, v208, s71, 0\n"
    "v_dot2_f32_bf16 v242, v209, s70, 0\n"
    "v_dot2_f32_bf16 v243, v209, s71, 0\n"
    "v_dot2_f32_bf16 v244, v210, s70, 0\n"
    "v_dot2_f32_bf16 v245, v210, s71, 0\n"
    "v_dot2_f32_bf16 v246, v211, s70, 0\n"
    "v_dot2_f32_bf16 v247, v211, s71, 0\n"
    "v_dot2_f32_bf16 v240, v212, s70, v240\n"
    "v_dot2_f32_bf16 v241, v212, s71, v241\n"
    "v_dot2_f32_bf16 v242, v213, s70, v242\n"
    "v_dot2_f32_bf16 v243, v213, s71, v243\n"
    "v_dot2_f32_bf16 v244, v214, s70, v244\n"
    "v_dot2_f32_bf16 v245, v214, s71, v245\n"
    "v_dot2_f32_bf16 v246, v215, s70, v246\n"
    "v_dot2_f32_bf16 v247, v215, s71, v247\n"
    "v_dot2_f32_bf16 v240, v216, s70, v240\n"
    "v_dot2_f32_bf16 v241, v216, s71, v241\n"
    "v_dot2_f32_bf16 v242, v217, s70, v242\n"
    "v_dot2_f32_bf16 v243, v217, s71, v243\n"
    "v_dot2_f32_bf16 v244, v218, s70, v244\n"
    "v_dot2_f32_bf16 v245, v218, s71, v245\n"
    "v_dot2_f32_bf16 v246, v219, s70, v246\n"
    "v_dot2_f32_bf16 v247, v219, s71, v247\n"
    "v_dot2_f32_bf16 v240, v220, s70, v240\n"
    "v_dot2_f32_bf16 v241, v220, s71, v241\n"
    "v_dot2_f32_bf16 v242, v221, s70, v242\n"
    "v_dot2_f32_bf16 v243, v221, s71, v243\n"
    "v_dot2_f32_bf16 v244, v222, s70, v244\n"
    "v_dot2_f32_bf16 v245, v222, s71, v245\n"
    "v_dot2_f32_bf16 v246, v223, s70, v246\n"
    "v_dot2_f32_bf16 v247, v223, s71, v247\n"
    "v_dot2_f32_bf16 v240, v224, s70, v240\n"
    "v_dot2_f32_bf16 v241, v224, s71, v241\n"
    "v_dot2_f32_bf16 v242, v225, s70, v242\n"
    "v_dot2_f32_bf16 v243, v225, s71, v243\n"
    "v_dot2_f32_bf16 v244, v226, s70, v244\n"
    "v_dot2_f32_bf16 v245, v226, s71, v245\n"
    "v_dot2_f32_bf16 v246, v227, s70, v246\n"
    "v_dot2_f32_bf16 v247, v227, s71, v247\n"
    "v_dot2_f32_bf16 v240, v228, s70, v240\n"
    "v_dot2_f32_bf16 v241, v228, s71, v241\n"
    "v_dot2_f32_bf16 v242, v229, s70, v242\n"
    "v_dot2_f32_bf16 v243, v229, s71, v243\n"
    "v_dot2_f32_bf16 v244, v230, s70, v244\n"
    "v_dot2_f32_bf16 v245, v230, s71, v245\n"
    "v_dot2_f32_bf16 v246, v231, s70, v246\n"
    "v_dot2_f32_bf16 v247, v231, s71, v247\n"
    "v_dot2_f32_bf16 v240, v232, s70, v240\n"
    "v_dot2_f32_bf16 v241, v232, s71, v241\n"
    "v_dot2_f32_bf16 v242, v233, s70, v242\n"
    "v_dot2_f32_bf16 v243, v233, s71, v243\n"
    "v_dot2_f32_bf16 v244, v234, s70, v244\n"
    "v_dot2_f32_bf16 v245, v234, s71, v245\n"
    "v_dot2_f32_bf16 v246, v235, s70, v246\n"
    "v_dot2_f32_bf16 v247, v235, s71, v247\n"
    "v_dot2_f32_bf16 v240, v236, s70, v240\n"
    "v_dot2_f32_bf16 v241, v236, s71, v241\n"
    "v_dot2_f32_bf16 v242, v237, s70, v242\n"
    "v_dot2_f32_bf16 v243, v237, s71, v243\n"
    "v_dot2_f32_bf16 v244, v238, s70, v244\n"
    "v_dot2_f32_bf16 v245, v238, s71, v245\n"
    "v_dot2_f32_bf16 v246, v239, s70, v246\n"
    "v_dot2_f32_bf16 v247, v239, s71, v247\n"
    "v_cvt_pk_bf16_f32 v248, v240, v241\n"
    "v_cvt_pk_bf16_f32 v249, v242, v243\n"
    "v_cvt_pk_bf16_f32 v250, v244, v245\n"
    "v_cvt_pk_bf16_f32 v251, v246, v247\n"
    "v_add_u32 v204, s57, v203\n"
    "ds_write_b64 v204, v[248:249]\n"
    "v_add_u32 v204, s58, v203\n"
    "ds_write_b64 v204, v[250:251]\n"
    "s_waitcnt vmcnt(0)\n"
    "v_dot2_f32_bf16 v240, v160, s70, 0\n"
    "v_dot2_f32_bf16 v241, v160, s71, 0\n"
    "v_dot2_f32_bf16 v242, v161, s70, 0\n"
    "v_dot2_f32_bf16 v243, v161, s71, 0\n"
    "v_dot2_f32_bf16 v240, v162, s70, v240\n"
    "v_dot2_f32_bf16 v241, v162, s71, v241\n"
    "v_dot2_f32_bf16 v242, v163, s70, v242\n"
    "v_dot2_f32_bf16 v243, v163, s71, v243\n"
    "v_dot2_f32_bf16 v240, v164, s70, v240\n"
    "v_dot2_f32_bf16 v241, v164, s71, v241\n"
    "v_dot2_f32_bf16 v242, v165, s70, v242\n"
    "v_dot2_f32_bf16 v243, v165, s71, v243\n"
    "v_dot2_f32_bf16 v240, v166, s70, v240\n"
    "v_dot2_f32_bf16 v241, v166, s71, v241\n"
    "v_dot2_f32_bf16 v242, v167, s70, v242\n"
    "v_dot2_f32_bf16 v243, v167, s71, v243\n"
    "v_dot2_f32_bf16 v240, v168, s70, v240\n"
    "v_dot2_f32_bf16 v241, v168, s71, v241\n"
    "v_dot2_f32_bf16 v242, v169, s70, v242\n"
    "v_dot2_f32_bf16 v243, v169, s71, v243\n"
    "v_dot2_f32_bf16 v240, v170, s70, v240\n"
    "v_dot2_f32_bf16 v241, v170, s71, v241\n"
    "v_dot2_f32_bf16 v242, v171, s70, v242\n"
    "v_dot2_f32_bf16 v243, v171, s71, v243\n"
    "v_dot2_f32_bf16 v240, v172, s70, v240\n"
    "v_dot2_f32_bf16 v241, v172, s71, v241\n"
    "v_dot2_f32_bf16 v242, v173, s70, v242\n"
    "v_dot2_f32_bf16 v243, v173, s71, v243\n"
    "v_dot2_f32_bf16 v240, v174, s70, v240\n"
    "v_dot2_f32_bf16 v241, v174, s71, v241\n"
    "v_dot2_f32_bf16 v242, v175, s70, v242\n"
    "v_dot2_f32_bf16 v243, v175, s71, v243\n"
    "s_nop 0\n"
    "v_cvt_pk_bf16_f32 v248, v240, v241\n"
    "s_nop 0\n"
    "v_cvt_pk_bf16_f32 v249, v242, v243\n"
    "v_add_u32 v204, s59, v203\n"
    "ds_write_b64 v204, v[248:249]\n"
    "s_waitcnt lgkmcnt(0)\n"
    "s_barrier\n"
    "ds_read_b128 v[64:67], v128 offset:0\n"
    "ds_read_b128 v[68:71], v130 offset:0\n"
    "ds_read_b128 v[72:75], v132 offset:0\n"
    "ds_read_b128 v[76:79], v134 offset:0\n"
    "ds_read_b128 v[80:83], v136 offset:0\n"
    "ds_read_b128 v[84:87], v138 offset:0\n"
    "s_waitcnt lgkmcnt(5)\n"
    "buffer_store_dwordx4 v[64:67], v129, s[20:23], s82 offen offset:0\n"
    "s_waitcnt lgkmcnt(4)\n"
    "buffer_store_dwordx4 v[68:71], v131, s[20:23], s82 offen offset:0\n"
    "s_waitcnt lgkmcnt(3)\n"
    "buffer_store_dwordx4 v[72:75], v133, s[20:23], s82 offen offset:0\n"
    "s_waitcnt lgkmcnt(2)\n"
    "buffer_store_dwordx4 v[76:79], v135, s[20:23], s82 offen offset:0\n"
    "s_waitcnt lgkmcnt(1)\n"
    "buffer_store_dwordx4 v[80:83], v137, s[20:23], s82 offen offset:0\n"
    "s_waitcnt lgkmcnt(0)\n"
    "buffer_store_dwordx4 v[84:87], v139, s[20:23], s82 offen offset:0\n"
    "s_cmp_lg_u32 s48, 0\n"
    "s_cbranch_scc1 L_fin_%=\n"
    "s_mov_b32 s65, 50000\n"
    "L_lin_%=:\n"
    "buffer_load_dword v187, v183, s[44:47], s62 offen sc1\n"
    "buffer_load_dword v188, v183, s[44:47], s60 offen sc1\n"
    "s_waitcnt vmcnt(0)\n"
    "v_cmp_ne_u32 vcc, v188, v181\n"
    "s_nop 4\n"
    "s_cbranch_vccz L_ne2_%=\n"
    "v_mov_b32_e64 v181, v188\n"
    "v_add_u32 v188, 1, v181\n"
    "buffer_store_dword v188, v182, s[44:47], s63 offen sc1\n"
    "s_nop 1\n"
    "L_ne2_%=:\n"
    "v_cmp_ne_u32 vcc, v187, v185\n"
    "s_nop 4\n"
    "s_and_b32 s74, vcc_lo, 255\n"
    "s_cmp_eq_u32 s74, 255\n"
    "s_cbranch_scc1 L_fin_%=\n"
    "s_sleep 1\n"
    "s_sub_u32 s65, s65, 1\n"
    "s_cmp_lg_u32 s65, 0\n"
    "s_cbranch_scc1 L_lin_%=\n"
    "L_fin_%=:\n"
    "s_waitcnt vmcnt(0) lgkmcnt(0)\n"
    "s_waitcnt vmcnt(0) lgkmcnt(0)\n"
    "s_branch L_end_%=\n"
    "L_last_%=:\n"
    "L_stub0_%=:\n"
    "s_mov_b32 s78, 0\n"
    "s_branch L_rare1_%=\n"
    "L_stub1_%=:\n"
    "s_mov_b32 s78, 1\n"
    "s_branch L_rare0_%=\n"
    "L_stub2_%=:\n"
    "s_mov_b32 s78, 2\n"
    "s_branch L_rare1_%=\n"
    "L_stub3_%=:\n"
    "s_mov_b32 s78, 3\n"
    "s_branch L_rare0_%=\n"
    "L_stub4_%=:\n"
    "s_mov_b32 s78, 4\n"
    "s_branch L_rare1_%=\n"
    "L_stub5_%=:\n"
    "s_mov_b32 s78, 5\n"
    "s_branch L_rare0_%=\n"
    "L_stub6_%=:\n"
    "s_mov_b32 s78, 6\n"
    "s_branch L_rare1_%=\n"
    "L_rare0_%=:\n"
    "s_nop 15\n"
    "v_max3_f32 v172, v96, v97, v98\n"
    "v_max3_f32 v172, v172, v99, v108\n"
    "v_max3_f32 v172, v172, v109, v110\n"
    "v_max3_f32 v172, v172, v111, 0\n"
    "v_max3_f32 v173, v100, v101, v102\n"
    "v_max3_f32 v173, v173, v103, v112\n"
    "v_max3_f32 v173, v173, v113, v114\n"
    "v_max3_f32 v173, v173, v115, 0\n"
    "v_max3_f32 v174, v104, v105, v106\n"
    "v_max3_f32 v174, v174, v107, v116\n"
    "v_max3_f32 v174, v174, v117, v118\n"
    "v_max3_f32 v174, v174, v119, 0\n"
    "v_mov_b32_e64 v175, v172\n"
    "v_mov_b32_e64 v176, v173\n"
    "v_mov_b32_e64 v177, v174\n"
    "v_permlane16_swap_b32 v172, v175\n"
    "v_permlane16_swap_b32 v173, v176\n"
    "v_permlane16_swap_b32 v174, v177\n"
    "v_max_f32_e64 v172, v172, v175\n"
    "v_max_f32_e64 v173, v173, v176\n"
    "v_max_f32_e64 v174, v174, v177\n"
    "v_mov_b32_e64 v175, v172\n"
    "v_mov_b32_e64 v176, v173\n"
    "v_mov_b32_e64 v177, v174\n"
    "v_permlane32_swap_b32 v172, v175\n"
    "v_permlane32_swap_b32 v173, v176\n"
    "v_permlane32_swap_b32 v174, v177\n"
    "v_max_f32_e64 v172, v172, v175\n"
    "v_max_f32_e64 v173, v173, v176\n"
    "v_max_f32_e64 v174, v174, v177\n"
    "v_sub_f32_e64 v156, v156, v172\n"
    "v_sub_f32_e64 v157, v157, v172\n"
    "v_sub_f32_e64 v158, v158, v172\n"
    "v_sub_f32_e64 v159, v159, v172\n"
    "v_sub_f32_e64 v96, v96, v172\n"
    "v_sub_f32_e64 v97, v97, v172\n"
    "v_sub_f32_e64 v98, v98, v172\n"
    "v_sub_f32_e64 v99, v99, v172\n"
    "v_sub_f32_e64 v108, v108, v172\n"
    "v_sub_f32_e64 v109, v109, v172\n"
    "v_sub_f32_e64 v110, v110, v172\n"
    "v_sub_f32_e64 v111, v111, v172\n"
    "v_exp_f32_e64 v175, -v172\n"
    "v_sub_f32_e64 v160, v160, v173\n"
    "v_sub_f32_e64 v161, v161, v173\n"
    "v_sub_f32_e64 v162, v162, v173\n"
    "v_sub_f32_e64 v163, v163, v173\n"
    "v_sub_f32_e64 v100, v100, v173\n"
    "v_sub_f32_e64 v101, v101, v173\n"
    "v_sub_f32_e64 v102, v102, v173\n"
    "v_sub_f32_e64 v103, v103, v173\n"
    "v_sub_f32_e64 v112, v112, v173\n"
    "v_sub_f32_e64 v113, v113, v173\n"
    "v_sub_f32_e64 v114, v114, v173\n"
    "v_sub_f32_e64 v115, v115, v173\n"
    "v_exp_f32_e64 v176, -v173\n"
    "v_sub_f32_e64 v164, v164, v174\n"
    "v_sub_f32_e64 v165, v165, v174\n"
    "v_sub_f32_e64 v166, v166, v174\n"
    "v_sub_f32_e64 v167, v167, v174\n"
    "v_sub_f32_e64 v104, v104, v174\n"
    "v_sub_f32_e64 v105, v105, v174\n"
    "v_sub_f32_e64 v106, v106, v174\n"
    "v_sub_f32_e64 v107, v107, v174\n"
    "v_sub_f32_e64 v116, v116, v174\n"
    "v_sub_f32_e64 v117, v117, v174\n"
    "v_sub_f32_e64 v118, v118, v174\n"
    "v_sub_f32_e64 v119, v119, v174\n"
    "v_exp_f32_e64 v177, -v174\n"
    "v_mul_f32_e64 v168, v168, v175\n"
    "v_mul_f32_e64 v169, v169, v176\n"
    "v_mul_f32_e64 v170, v170, v177\n"
    "s_branch L_rareo_%=\n"
    "L_rare1_%=:\n"
    "s_nop 15\n"
    "v_max3_f32 v172, v120, v121, v122\n"
    "v_max3_f32 v172, v172, v123, v132\n"
    "v_max3_f32 v172, v172, v133, v134\n"
    "v_max3_f32 v172, v172, v135, 0\n"
    "v_max3_f32 v173, v124, v125, v126\n"
    "v_max3_f32 v173, v173, v127, v136\n"
    "v_max3_f32 v173, v173, v137, v138\n"
    "v_max3_f32 v173, v173, v139, 0\n"
    "v_max3_f32 v174, v128, v129, v130\n"
    "v_max3_f32 v174, v174, v131, v140\n"
    "v_max3_f32 v174, v174, v141, v142\n"
    "v_max3_f32 v174, v174, v143, 0\n"
    "v_mov_b32_e64 v175, v172\n"
    "v_mov_b32_e64 v176, v173\n"
    "v_mov_b32_e64 v177, v174\n"
    "v_permlane16_swap_b32 v172, v175\n"
    "v_permlane16_swap_b32 v173, v176\n"
    "v_permlane16_swap_b32 v174, v177\n"
    "v_max_f32_e64 v172, v172, v175\n"
    "v_max_f32_e64 v173, v173, v176\n"
    "v_max_f32_e64 v174, v174, v177\n"
    "v_mov_b32_e64 v175, v172\n"
    "v_mov_b32_e64 v176, v173\n"
    "v_mov_b32_e64 v177, v174\n"
    "v_permlane32_swap_b32 v172, v175\n"
    "v_permlane32_swap_b32 v173, v176\n"
    "v_permlane32_swap_b32 v174, v177\n"
    "v_max_f32_e64 v172, v172, v175\n"
    "v_max_f32_e64 v173, v173, v176\n"
    "v_max_f32_e64 v174, v174, v177\n"
    "v_sub_f32_e64 v156, v156, v172\n"
    "v_sub_f32_e64 v157, v157, v172\n"
    "v_sub_f32_e64 v158, v158, v172\n"
    "v_sub_f32_e64 v159, v159, v172\n"
    "v_sub_f32_e64 v120, v120, v172\n"
    "v_sub_f32_e64 v121, v121, v172\n"
    "v_sub_f32_e64 v122, v122, v172\n"
    "v_sub_f32_e64 v123, v123, v172\n"
    "v_sub_f32_e64 v132, v132, v172\n"
    "v_sub_f32_e64 v133, v133, v172\n"
    "v_sub_f32_e64 v134, v134, v172\n"
    "v_sub_f32_e64 v135, v135, v172\n"
    "v_exp_f32_e64 v175, -v172\n"
    "v_sub_f32_e64 v160, v160, v173\n"
    "v_sub_f32_e64 v161, v161, v173\n"
    "v_sub_f32_e64 v162, v162, v173\n"
    "v_sub_f32_e64 v163, v163, v173\n"
    "v_sub_f32_e64 v124, v124, v173\n"
    "v_sub_f32_e64 v125, v125, v173\n"
    "v_sub_f32_e64 v126, v126, v173\n"
    "v_sub_f32_e64 v127, v127, v173\n"
    "v_sub_f32_e64 v136, v136, v173\n"
    "v_sub_f32_e64 v137, v137, v173\n"
    "v_sub_f32_e64 v138, v138, v173\n"
    "v_sub_f32_e64 v139, v139, v173\n"
    "v_exp_f32_e64 v176, -v173\n"
    "v_sub_f32_e64 v164, v164, v174\n"
    "v_sub_f32_e64 v165, v165, v174\n"
    "v_sub_f32_e64 v166, v166, v174\n"
    "v_sub_f32_e64 v167, v167, v174\n"
    "v_sub_f32_e64 v128, v128, v174\n"
    "v_sub_f32_e64 v129, v129, v174\n"
    "v_sub_f32_e64 v130, v130, v174\n"
    "v_sub_f32_e64 v131, v131, v174\n"
    "v_sub_f32_e64 v140, v140, v174\n"
    "v_sub_f32_e64 v141, v141, v174\n"
    "v_sub_f32_e64 v142, v142, v174\n"
    "v_sub_f32_e64 v143, v143, v174\n"
    "v_exp_f32_e64 v177, -v174\n"
    "v_mul_f32_e64 v168, v168, v175\n"
    "v_mul_f32_e64 v169, v169, v176\n"
    "v_mul_f32_e64 v170, v170, v177\n"
    "L_rareo_%=:\n"
    "v_accvgpr_read_b32 v178, a0\n"
    "v_accvgpr_read_b32 v179, a1\n"
    "v_accvgpr_read_b32 v171, a2\n"
    "v_accvgpr_read_b32 v189, a3\n"
    "v_mul_f32_e64 v178, v178, v175\n"
    "v_mul_f32_e64 v179, v179, v175\n"
    "v_mul_f32_e64 v171, v171, v175\n"
    "v_mul_f32_e64 v189, v189, v175\n"
    "v_accvgpr_write_b32 a0, v178\n"
    "v_accvgpr_write_b32 a1, v179\n"
    "v_accvgpr_write_b32 a2, v171\n"
    "v_accvgpr_write_b32 a3, v189\n"
    "v_accvgpr_read_b32 v178, a4\n"
    "v_accvgpr_read_b32 v179, a5\n"
    "v_accvgpr_read_b32 v171, a6\n"
    "v_accvgpr_read_b32 v189, a7\n"
    "v_mul_f32_e64 v178, v178, v176\n"
    "v_mul_f32_e64 v179, v179, v176\n"
    "v_mul_f32_e64 v171, v171, v176\n"
    "v_mul_f32_e64 v189, v189, v176\n"
    "v_accvgpr_write_b32 a4, v178\n"
    "v_accvgpr_write_b32 a5, v179\n"
    "v_accvgpr_write_b32 a6, v171\n"
    "v_accvgpr_write_b32 a7, v189\n"
    "v_accvgpr_read_b32 v178, a8\n"
    "v_accvgpr_read_b32 v179, a9\n"
    "v_accvgpr_read_b32 v171, a10\n"
    "v_accvgpr_read_b32 v189, a11\n"
    "v_mul_f32_e64 v178, v178, v177\n"
    "v_mul_f32_e64 v179, v179, v177\n"
    "v_mul_f32_e64 v171, v171, v177\n"
    "v_mul_f32_e64 v189, v189, v177\n"
    "v_accvgpr_write_b32 a8, v178\n"
    "v_accvgpr_write_b32 a9, v179\n"
    "v_accvgpr_write_b32 a10, v171\n"
    "v_accvgpr_write_b32 a11, v189\n"
    "v_accvgpr_read_b32 v178, a12\n"
    "v_accvgpr_read_b32 v179, a13\n"
    "v_accvgpr_read_b32 v171, a14\n"
    "v_accvgpr_read_b32 v189, a15\n"
    "v_mul_f32_e64 v178, v178, v175\n"
    "v_mul_f32_e64 v179, v179, v175\n"
    "v_mul_f32_e64 v171, v171, v175\n"
    "v_mul_f32_e64 v189, v189, v175\n"
    "v_accvgpr_write_b32 a12, v178\n"
    "v_accvgpr_write_b32 a13, v179\n"
    "v_accvgpr_write_b32 a14, v171\n"
    "v_accvgpr_write_b32 a15, v189\n"
    "v_accvgpr_read_b32 v178, a16\n"
    "v_accvgpr_read_b32 v179, a17\n"
    "v_accvgpr_read_b32 v171, a18\n"
    "v_accvgpr_read_b32 v189, a19\n"
    "v_mul_f32_e64 v178, v178, v176\n"
    "v_mul_f32_e64 v179, v179, v176\n"
    "v_mul_f32_e64 v171, v171, v176\n"
    "v_mul_f32_e64 v189, v189, v176\n"
    "v_accvgpr_write_b32 a16, v178\n"
    "v_accvgpr_write_b32 a17, v179\n"
    "v_accvgpr_write_b32 a18, v171\n"
    "v_accvgpr_write_b32 a19, v189\n"
    "v_accvgpr_read_b32 v178, a20\n"
    "v_accvgpr_read_b32 v179, a21\n"
    "v_accvgpr_read_b32 v171, a22\n"
    "v_accvgpr_read_b32 v189, a23\n"
    "v_mul_f32_e64 v178, v178, v177\n"
    "v_mul_f32_e64 v179, v179, v177\n"
    "v_mul_f32_e64 v171, v171, v177\n"
    "v_mul_f32_e64 v189, v189, v177\n"
    "v_accvgpr_write_b32 a20, v178\n"
    "v_accvgpr_write_b32 a21, v179\n"
    "v_accvgpr_write_b32 a22, v171\n"
    "v_accvgpr_write_b32 a23, v189\n"
    "v_accvgpr_read_b32 v178, a24\n"
    "v_accvgpr_read_b32 v179, a25\n"
    "v_accvgpr_read_b32 v171, a26\n"
    "v_accvgpr_read_b32 v189, a27\n"
    "v_mul_f32_e64 v178, v178, v175\n"
    "v_mul_f32_e64 v179, v179, v175\n"
    "v_mul_f32_e64 v171, v171, v175\n"
    "v_mul_f32_e64 v189, v189, v175\n"
    "v_accvgpr_write_b32 a24, v178\n"
    "v_accvgpr_write_b32 a25, v179\n"
    "v_accvgpr_write_b32 a26, v171\n"
    "v_accvgpr_write_b32 a27, v189\n"
    "v_accvgpr_read_b32 v178, a28\n"
    "v_accvgpr_read_b32 v179, a29\n"
    "v_accvgpr_read_b32 v171, a30\n"
    "v_accvgpr_read_b32 v189, a31\n"
    "v_mul_f32_e64 v178, v178, v176\n"
    "v_mul_f32_e64 v179, v179, v176\n"
    "v_mul_f32_e64 v171, v171, v176\n"
    "v_mul_f32_e64 v189, v189, v176\n"
    "v_accvgpr_write_b32 a28, v178\n"
    "v_accvgpr_write_b32 a29, v179\n"
    "v_accvgpr_write_b32 a30, v171\n"
    "v_accvgpr_write_b32 a31, v189\n"
    "v_accvgpr_read_b32 v178, a32\n"
    "v_accvgpr_read_b32 v179, a33\n"
    "v_accvgpr_read_b32 v171, a34\n"
    "v_accvgpr_read_b32 v189, a35\n"
    "v_mul_f32_e64 v178, v178, v177\n"
    "v_mul_f32_e64 v179, v179, v177\n"
    "v_mul_f32_e64 v171, v171, v177\n"
    "v_mul_f32_e64 v189, v189, v177\n"
    "v_accvgpr_write_b32 a32, v178\n"
    "v_accvgpr_write_b32 a33, v179\n"
    "v_accvgpr_write_b32 a34, v171\n"
    "v_accvgpr_write_b32 a35, v189\n"
    "v_accvgpr_read_b32 v178, a36\n"
    "v_accvgpr_read_b32 v179, a37\n"
    "v_accvgpr_read_b32 v171, a38\n"
    "v_accvgpr_read_b32 v189, a39\n"
    "v_mul_f32_e64 v178, v178, v175\n"
    "v_mul_f32_e64 v179, v179, v175\n"
    "v_mul_f32_e64 v171, v171, v175\n"
    "v_mul_f32_e64 v189, v189, v175\n"
    "v_accvgpr_write_b32 a36, v178\n"
    "v_accvgpr_write_b32 a37, v179\n"
    "v_accvgpr_write_b32 a38, v171\n"
    "v_accvgpr_write_b32 a39, v189\n"
    "v_accvgpr_read_b32 v178, a40\n"
    "v_accvgpr_read_b32 v179, a41\n"
    "v_accvgpr_read_b32 v171, a42\n"
    "v_accvgpr_read_b32 v189, a43\n"
    "v_mul_f32_e64 v178, v178, v176\n"
    "v_mul_f32_e64 v179, v179, v176\n"
    "v_mul_f32_e64 v171, v171, v176\n"
    "v_mul_f32_e64 v189, v189, v176\n"
    "v_accvgpr_write_b32 a40, v178\n"
    "v_accvgpr_write_b32 a41, v179\n"
    "v_accvgpr_write_b32 a42, v171\n"
    "v_accvgpr_write_b32 a43, v189\n"
    "v_accvgpr_read_b32 v178, a44\n"
    "v_accvgpr_read_b32 v179, a45\n"
    "v_accvgpr_read_b32 v171, a46\n"
    "v_accvgpr_read_b32 v189, a47\n"
    "v_mul_f32_e64 v178, v178, v177\n"
    "v_mul_f32_e64 v179, v179, v177\n"
    "v_mul_f32_e64 v171, v171, v177\n"
    "v_mul_f32_e64 v189, v189, v177\n"
    "v_accvgpr_write_b32 a44, v178\n"
    "v_accvgpr_write_b32 a45, v179\n"
    "v_accvgpr_write_b32 a46, v171\n"
    "v_accvgpr_write_b32 a47, v189\n"
    "v_accvgpr_read_b32 v178, a48\n"
    "v_accvgpr_read_b32 v179, a49\n"
    "v_accvgpr_read_b32 v171, a50\n"
    "v_accvgpr_read_b32 v189, a51\n"
    "v_mul_f32_e64 v178, v178, v175\n"
    "v_mul_f32_e64 v179, v179, v175\n"
    "v_mul_f32_e64 v171, v171, v175\n"
    "v_mul_f32_e64 v189, v189, v175\n"
    "v_accvgpr_write_b32 a48, v178\n"
    "v_accvgpr_write_b32 a49, v179\n"
    "v_accvgpr_write_b32 a50, v171\n"
    "v_accvgpr_write_b32 a51, v189\n"
    "v_accvgpr_read_b32 v178, a52\n"
    "v_accvgpr_read_b32 v179, a53\n"
    "v_accvgpr_read_b32 v171, a54\n"
    "v_accvgpr_read_b32 v189, a55\n"
    "v_mul_f32_e64 v178, v178, v176\n"
    "v_mul_f32_e64 v179, v179, v176\n"
    "v_mul_f32_e64 v171, v171, v176\n"
    "v_mul_f32_e64 v189, v189, v176\n"
    "v_accvgpr_write_b32 a52, v178\n"
    "v_accvgpr_write_b32 a53, v179\n"
    "v_accvgpr_write_b32 a54, v171\n"
    "v_accvgpr_write_b32 a55, v189\n"
    "v_accvgpr_read_b32 v178, a56\n"
    "v_accvgpr_read_b32 v179, a57\n"
    "v_accvgpr_read_b32 v171, a58\n"
    "v_accvgpr_read_b32 v189, a59\n"
    "v_mul_f32_e64 v178, v178, v177\n"
    "v_mul_f32_e64 v179, v179, v177\n"
    "v_mul_f32_e64 v171, v171, v177\n"
    "v_mul_f32_e64 v189, v189, v177\n"
    "v_accvgpr_write_b32 a56, v178\n"
    "v_accvgpr_write_b32 a57, v179\n"
    "v_accvgpr_write_b32 a58, v171\n"
    "v_accvgpr_write_b32 a59, v189\n"
    "v_accvgpr_read_b32 v178, a60\n"
    "v_accvgpr_read_b32 v179, a61\n"
    "v_accvgpr_read_b32 v171, a62\n"
    "v_accvgpr_read_b32 v189, a63\n"
    "v_mul_f32_e64 v178, v178, v175\n"
    "v_mul_f32_e64 v179, v179, v175\n"
    "v_mul_f32_e64 v171, v171, v175\n"
    "v_mul_f32_e64 v189, v189, v175\n"
    "v_accvgpr_write_b32 a60, v178\n"
    "v_accvgpr_write_b32 a61, v179\n"
    "v_accvgpr_write_b32 a62, v171\n"
    "v_accvgpr_write_b32 a63, v189\n"
    "v_accvgpr_read_b32 v178, a64\n"
    "v_accvgpr_read_b32 v179, a65\n"
    "v_accvgpr_read_b32 v171, a66\n"
    "v_accvgpr_read_b32 v189, a67\n"
    "v_mul_f32_e64 v178, v178, v176\n"
    "v_mul_f32_e64 v179, v179, v176\n"
    "v_mul_f32_e64 v171, v171, v176\n"
    "v_mul_f32_e64 v189, v189, v176\n"
    "v_accvgpr_write_b32 a64, v178\n"
    "v_accvgpr_write_b32 a65, v179\n"
    "v_accvgpr_write_b32 a66, v171\n"
    "v_accvgpr_write_b32 a67, v189\n"
    "v_accvgpr_read_b32 v178, a68\n"
    "v_accvgpr_read_b32 v179, a69\n"
    "v_accvgpr_read_b32 v171, a70\n"
    "v_accvgpr_read_b32 v189, a71\n"
    "v_mul_f32_e64 v178, v178, v177\n"
    "v_mul_f32_e64 v179, v179, v177\n"
    "v_mul_f32_e64 v171, v171, v177\n"
    "v_mul_f32_e64 v189, v189, v177\n"
    "v_accvgpr_write_b32 a68, v178\n"
    "v_accvgpr_write_b32 a69, v179\n"
    "v_accvgpr_write_b32 a70, v171\n"
    "v_accvgpr_write_b32 a71, v189\n"
    "v_accvgpr_read_b32 v178, a72\n"
    "v_accvgpr_read_b32 v179, a73\n"
    "v_accvgpr_read_b32 v171, a74\n"
    "v_accvgpr_read_b32 v189, a75\n"
    "v_mul_f32_e64 v178, v178, v175\n"
    "v_mul_f32_e64 v179, v179, v175\n"
    "v_mul_f32_e64 v171, v171, v175\n"
    "v_mul_f32_e64 v189, v189, v175\n"
    "v_accvgpr_write_b32 a72, v178\n"
    "v_accvgpr_write_b32 a73, v179\n"
    "v_accvgpr_write_b32 a74, v171\n"
    "v_accvgpr_write_b32 a75, v189\n"
    "v_accvgpr_read_b32 v178, a76\n"
    "v_accvgpr_read_b32 v179, a77\n"
    "v_accvgpr_read_b32 v171, a78\n"
    "v_accvgpr_read_b32 v189, a79\n"
    "v_mul_f32_e64 v178, v178, v176\n"
    "v_mul_f32_e64 v179, v179, v176\n"
    "v_mul_f32_e64 v171, v171, v176\n"
    "v_mul_f32_e64 v189, v189, v176\n"
    "v_accvgpr_write_b32 a76, v178\n"
    "v_accvgpr_write_b32 a77, v179\n"
    "v_accvgpr_write_b32 a78, v171\n"
    "v_accvgpr_write_b32 a79, v189\n"
    "v_accvgpr_read_b32 v178, a80\n"
    "v_accvgpr_read_b32 v179, a81\n"
    "v_accvgpr_read_b32 v171, a82\n"
    "v_accvgpr_read_b32 v189, a83\n"
    "v_mul_f32_e64 v178, v178, v177\n"
    "v_mul_f32_e64 v179, v179, v177\n"
    "v_mul_f32_e64 v171, v171, v177\n"
    "v_mul_f32_e64 v189, v189, v177\n"
    "v_accvgpr_write_b32 a80, v178\n"
    "v_accvgpr_write_b32 a81, v179\n"
    "v_accvgpr_write_b32 a82, v171\n"
    "v_accvgpr_write_b32 a83, v189\n"
    "v_accvgpr_read_b32 v178, a84\n"
    "v_accvgpr_read_b32 v179, a85\n"
    "v_accvgpr_read_b32 v171, a86\n"
    "v_accvgpr_read_b32 v189, a87\n"
    "v_mul_f32_e64 v178, v178, v175\n"
    "v_mul_f32_e64 v179, v179, v175\n"
    "v_mul_f32_e64 v171, v171, v175\n"
    "v_mul_f32_e64 v189, v189, v175\n"
    "v_accvgpr_write_b32 a84, v178\n"
    "v_accvgpr_write_b32 a85, v179\n"
    "v_accvgpr_write_b32 a86, v171\n"
    "v_accvgpr_write_b32 a87, v189\n"
    "v_accvgpr_read_b32 v178, a88\n"
    "v_accvgpr_read_b32 v179, a89\n"
    "v_accvgpr_read_b32 v171, a90\n"
    "v_accvgpr_read_b32 v189, a91\n"
    "v_mul_f32_e64 v178, v178, v176\n"
    "v_mul_f32_e64 v179, v179, v176\n"
    "v_mul_f32_e64 v171, v171, v176\n"
    "v_mul_f32_e64 v189, v189, v176\n"
    "v_accvgpr_write_b32 a88, v178\n"
    "v_accvgpr_write_b32 a89, v179\n"
    "v_accvgpr_write_b32 a90, v171\n"
    "v_accvgpr_write_b32 a91, v189\n"
    "v_accvgpr_read_b32 v178, a92\n"
    "v_accvgpr_read_b32 v179, a93\n"
    "v_accvgpr_read_b32 v171, a94\n"
    "v_accvgpr_read_b32 v189, a95\n"
    "v_mul_f32_e64 v178, v178, v177\n"
    "v_mul_f32_e64 v179, v179, v177\n"
    "v_mul_f32_e64 v171, v171, v177\n"
    "v_mul_f32_e64 v189, v189, v177\n"
    "v_accvgpr_write_b32 a92, v178\n"
    "v_accvgpr_write_b32 a93, v179\n"
    "v_accvgpr_write_b32 a94, v171\n"
    "v_accvgpr_write_b32 a95, v189\n"
    "v_accvgpr_read_b32 v178, a96\n"
    "v_accvgpr_read_b32 v179, a97\n"
    "v_accvgpr_read_b32 v171, a98\n"
    "v_accvgpr_read_b32 v189, a99\n"
    "v_mul_f32_e64 v178, v178, v175\n"
    "v_mul_f32_e64 v179, v179, v175\n"
    "v_mul_f32_e64 v171, v171, v175\n"
    "v_mul_f32_e64 v189, v189, v175\n"
    "v_accvgpr_write_b32 a96, v178\n"
    "v_accvgpr_write_b32 a97, v179\n"
    "v_accvgpr_write_b32 a98, v171\n"
    "v_accvgpr_write_b32 a99, v189\n"
    "v_accvgpr_read_b32 v178, a100\n"
    "v_accvgpr_read_b32 v179, a101\n"
    "v_accvgpr_read_b32 v171, a102\n"
    "v_accvgpr_read_b32 v189, a103\n"
    "v_mul_f32_e64 v178, v178, v176\n"
    "v_mul_f32_e64 v179, v179, v176\n"
    "v_mul_f32_e64 v171, v171, v176\n"
    "v_mul_f32_e64 v189, v189, v176\n"
    "v_accvgpr_write_b32 a100, v178\n"
    "v_accvgpr_write_b32 a101, v179\n"
    "v_accvgpr_write_b32 a102, v171\n"
    "v_accvgpr_write_b32 a103, v189\n"
    "v_accvgpr_read_b32 v178, a104\n"
    "v_accvgpr_read_b32 v179, a105\n"
    "v_accvgpr_read_b32 v171, a106\n"
    "v_accvgpr_read_b32 v189, a107\n"
    "v_mul_f32_e64 v178, v178, v177\n"
    "v_mul_f32_e64 v179, v179, v177\n"
    "v_mul_f32_e64 v171, v171, v177\n"
    "v_mul_f32_e64 v189, v189, v177\n"
    "v_accvgpr_write_b32 a104, v178\n"
    "v_accvgpr_write_b32 a105, v179\n"
    "v_accvgpr_write_b32 a106, v171\n"
    "v_accvgpr_write_b32 a107, v189\n"
    "v_accvgpr_read_b32 v178, a108\n"
    "v_accvgpr_read_b32 v179, a109\n"
    "v_accvgpr_read_b32 v171, a110\n"
    "v_accvgpr_read_b32 v189, a111\n"
    "v_mul_f32_e64 v178, v178, v175\n"
    "v_mul_f32_e64 v179, v179, v175\n"
    "v_mul_f32_e64 v171, v171, v175\n"
    "v_mul_f32_e64 v189, v189, v175\n"
    "v_accvgpr_write_b32 a108, v178\n"
    "v_accvgpr_write_b32 a109, v179\n"
    "v_accvgpr_write_b32 a110, v171\n"
    "v_accvgpr_write_b32 a111, v189\n"
    "v_accvgpr_read_b32 v178, a112\n"
    "v_accvgpr_read_b32 v179, a113\n"
    "v_accvgpr_read_b32 v171, a114\n"
    "v_accvgpr_read_b32 v189, a115\n"
    "v_mul_f32_e64 v178, v178, v176\n"
    "v_mul_f32_e64 v179, v179, v176\n"
    "v_mul_f32_e64 v171, v171, v176\n"
    "v_mul_f32_e64 v189, v189, v176\n"
    "v_accvgpr_write_b32 a112, v178\n"
    "v_accvgpr_write_b32 a113, v179\n"
    "v_accvgpr_write_b32 a114, v171\n"
    "v_accvgpr_write_b32 a115, v189\n"
    "v_accvgpr_read_b32 v178, a116\n"
    "v_accvgpr_read_b32 v179, a117\n"
    "v_accvgpr_read_b32 v171, a118\n"
    "v_accvgpr_read_b32 v189, a119\n"
    "v_mul_f32_e64 v178, v178, v177\n"
    "v_mul_f32_e64 v179, v179, v177\n"
    "v_mul_f32_e64 v171, v171, v177\n"
    "v_mul_f32_e64 v189, v189, v177\n"
    "v_accvgpr_write_b32 a116, v178\n"
    "v_accvgpr_write_b32 a117, v179\n"
    "v_accvgpr_write_b32 a118, v171\n"
    "v_accvgpr_write_b32 a119, v189\n"
    "v_accvgpr_read_b32 v178, a120\n"
    "v_accvgpr_read_b32 v179, a121\n"
    "v_accvgpr_read_b32 v171, a122\n"
    "v_accvgpr_read_b32 v189, a123\n"
    "v_mul_f32_e64 v178, v178, v175\n"
    "v_mul_f32_e64 v179, v179, v175\n"
    "v_mul_f32_e64 v171, v171, v175\n"
    "v_mul_f32_e64 v189, v189, v175\n"
    "v_accvgpr_write_b32 a120, v178\n"
    "v_accvgpr_write_b32 a121, v179\n"
    "v_accvgpr_write_b32 a122, v171\n"
    "v_accvgpr_write_b32 a123, v189\n"
    "v_accvgpr_read_b32 v178, a124\n"
    "v_accvgpr_read_b32 v179, a125\n"
    "v_accvgpr_read_b32 v171, a126\n"
    "v_accvgpr_read_b32 v189, a127\n"
    "v_mul_f32_e64 v178, v178, v176\n"
    "v_mul_f32_e64 v179, v179, v176\n"
    "v_mul_f32_e64 v171, v171, v176\n"
    "v_mul_f32_e64 v189, v189, v176\n"
    "v_accvgpr_write_b32 a124, v178\n"
    "v_accvgpr_write_b32 a125, v179\n"
    "v_accvgpr_write_b32 a126, v171\n"
    "v_accvgpr_write_b32 a127, v189\n"
    "v_accvgpr_read_b32 v178, a128\n"
    "v_accvgpr_read_b32 v179, a129\n"
    "v_accvgpr_read_b32 v171, a130\n"
    "v_accvgpr_read_b32 v189, a131\n"
    "v_mul_f32_e64 v178, v178, v177\n"
    "v_mul_f32_e64 v179, v179, v177\n"
    "v_mul_f32_e64 v171, v171, v177\n"
    "v_mul_f32_e64 v189, v189, v177\n"
    "v_accvgpr_write_b32 a128, v178\n"
    "v_accvgpr_write_b32 a129, v179\n"
    "v_accvgpr_write_b32 a130, v171\n"
    "v_accvgpr_write_b32 a131, v189\n"
    "v_accvgpr_read_b32 v178, a132\n"
    "v_accvgpr_read_b32 v179, a133\n"
    "v_accvgpr_read_b32 v171, a134\n"
    "v_accvgpr_read_b32 v189, a135\n"
    "v_mul_f32_e64 v178, v178, v175\n"
    "v_mul_f32_e64 v179, v179, v175\n"
    "v_mul_f32_e64 v171, v171, v175\n"
    "v_mul_f32_e64 v189, v189, v175\n"
    "v_accvgpr_write_b32 a132, v178\n"
    "v_accvgpr_write_b32 a133, v179\n"
    "v_accvgpr_write_b32 a134, v171\n"
    "v_accvgpr_write_b32 a135, v189\n"
    "v_accvgpr_read_b32 v178, a136\n"
    "v_accvgpr_read_b32 v179, a137\n"
    "v_accvgpr_read_b32 v171, a138\n"
    "v_accvgpr_read_b32 v189, a139\n"
    "v_mul_f32_e64 v178, v178, v176\n"
    "v_mul_f32_e64 v179, v179, v176\n"
    "v_mul_f32_e64 v171, v171, v176\n"
    "v_mul_f32_e64 v189, v189, v176\n"
    "v_accvgpr_write_b32 a136, v178\n"
    "v_accvgpr_write_b32 a137, v179\n"
    "v_accvgpr_write_b32 a138, v171\n"
    "v_accvgpr_write_b32 a139, v189\n"
    "v_accvgpr_read_b32 v178, a140\n"
    "v_accvgpr_read_b32 v179, a141\n"
    "v_accvgpr_read_b32 v171, a142\n"
    "v_accvgpr_read_b32 v189, a143\n"
    "v_mul_f32_e64 v178, v178, v177\n"
    "v_mul_f32_e64 v179, v179, v177\n"
    "v_mul_f32_e64 v171, v171, v177\n"
    "v_mul_f32_e64 v189, v189, v177\n"
    "v_accvgpr_write_b32 a140, v178\n"
    "v_accvgpr_write_b32 a141, v179\n"
    "v_accvgpr_write_b32 a142, v171\n"
    "v_accvgpr_write_b32 a143, v189\n"
    "v_accvgpr_read_b32 v178, a144\n"
    "v_accvgpr_read_b32 v179, a145\n"
    "v_accvgpr_read_b32 v171, a146\n"
    "v_accvgpr_read_b32 v189, a147\n"
    "v_mul_f32_e64 v178, v178, v175\n"
    "v_mul_f32_e64 v179, v179, v175\n"
    "v_mul_f32_e64 v171, v171, v175\n"
    "v_mul_f32_e64 v189, v189, v175\n"
    "v_accvgpr_write_b32 a144, v178\n"
    "v_accvgpr_write_b32 a145, v179\n"
    "v_accvgpr_write_b32 a146, v171\n"
    "v_accvgpr_write_b32 a147, v189\n"
    "v_accvgpr_read_b32 v178, a148\n"
    "v_accvgpr_read_b32 v179, a149\n"
    "v_accvgpr_read_b32 v171, a150\n"
    "v_accvgpr_read_b32 v189, a151\n"
    "v_mul_f32_e64 v178, v178, v176\n"
    "v_mul_f32_e64 v179, v179, v176\n"
    "v_mul_f32_e64 v171, v171, v176\n"
    "v_mul_f32_e64 v189, v189, v176\n"
    "v_accvgpr_write_b32 a148, v178\n"
    "v_accvgpr_write_b32 a149, v179\n"
    "v_accvgpr_write_b32 a150, v171\n"
    "v_accvgpr_write_b32 a151, v189\n"
    "v_accvgpr_read_b32 v178, a152\n"
    "v_accvgpr_read_b32 v179, a153\n"
    "v_accvgpr_read_b32 v171, a154\n"
    "v_accvgpr_read_b32 v189, a155\n"
    "v_mul_f32_e64 v178, v178, v177\n"
    "v_mul_f32_e64 v179, v179, v177\n"
    "v_mul_f32_e64 v171, v171, v177\n"
    "v_mul_f32_e64 v189, v189, v177\n"
    "v_accvgpr_write_b32 a152, v178\n"
    "v_accvgpr_write_b32 a153, v179\n"
    "v_accvgpr_write_b32 a154, v171\n"
    "v_accvgpr_write_b32 a155, v189\n"
    "v_accvgpr_read_b32 v178, a156\n"
    "v_accvgpr_read_b32 v179, a157\n"
    "v_accvgpr_read_b32 v171, a158\n"
    "v_accvgpr_read_b32 v189, a159\n"
    "v_mul_f32_e64 v178, v178, v175\n"
    "v_mul_f32_e64 v179, v179, v175\n"
    "v_mul_f32_e64 v171, v171, v175\n"
    "v_mul_f32_e64 v189, v189, v175\n"
    "v_accvgpr_write_b32 a156, v178\n"
    "v_accvgpr_write_b32 a157, v179\n"
    "v_accvgpr_write_b32 a158, v171\n"
    "v_accvgpr_write_b32 a159, v189\n"
    "v_accvgpr_read_b32 v178, a160\n"
    "v_accvgpr_read_b32 v179, a161\n"
    "v_accvgpr_read_b32 v171, a162\n"
    "v_accvgpr_read_b32 v189, a163\n"
    "v_mul_f32_e64 v178, v178, v176\n"
    "v_mul_f32_e64 v179, v179, v176\n"
    "v_mul_f32_e64 v171, v171, v176\n"
    "v_mul_f32_e64 v189, v189, v176\n"
    "v_accvgpr_write_b32 a160, v178\n"
    "v_accvgpr_write_b32 a161, v179\n"
    "v_accvgpr_write_b32 a162, v171\n"
    "v_accvgpr_write_b32 a163, v189\n"
    "v_accvgpr_read_b32 v178, a164\n"
    "v_accvgpr_read_b32 v179, a165\n"
    "v_accvgpr_read_b32 v171, a166\n"
    "v_accvgpr_read_b32 v189, a167\n"
    "v_mul_f32_e64 v178, v178, v177\n"
    "v_mul_f32_e64 v179, v179, v177\n"
    "v_mul_f32_e64 v171, v171, v177\n"
    "v_mul_f32_e64 v189, v189, v177\n"
    "v_accvgpr_write_b32 a164, v178\n"
    "v_accvgpr_write_b32 a165, v179\n"
    "v_accvgpr_write_b32 a166, v171\n"
    "v_accvgpr_write_b32 a167, v189\n"
    "v_accvgpr_read_b32 v178, a168\n"
    "v_accvgpr_read_b32 v179, a169\n"
    "v_accvgpr_read_b32 v171, a170\n"
    "v_accvgpr_read_b32 v189, a171\n"
    "v_mul_f32_e64 v178, v178, v175\n"
    "v_mul_f32_e64 v179, v179, v175\n"
    "v_mul_f32_e64 v171, v171, v175\n"
    "v_mul_f32_e64 v189, v189, v175\n"
    "v_accvgpr_write_b32 a168, v178\n"
    "v_accvgpr_write_b32 a169, v179\n"
    "v_accvgpr_write_b32 a170, v171\n"
    "v_accvgpr_write_b32 a171, v189\n"
    "v_accvgpr_read_b32 v178, a172\n"
    "v_accvgpr_read_b32 v179, a173\n"
    "v_accvgpr_read_b32 v171, a174\n"
    "v_accvgpr_read_b32 v189, a175\n"
    "v_mul_f32_e64 v178, v178, v176\n"
    "v_mul_f32_e64 v179, v179, v176\n"
    "v_mul_f32_e64 v171, v171, v176\n"
    "v_mul_f32_e64 v189, v189, v176\n"
    "v_accvgpr_write_b32 a172, v178\n"
    "v_accvgpr_write_b32 a173, v179\n"
    "v_accvgpr_write_b32 a174, v171\n"
    "v_accvgpr_write_b32 a175, v189\n"
    "v_accvgpr_read_b32 v178, a176\n"
    "v_accvgpr_read_b32 v179, a177\n"
    "v_accvgpr_read_b32 v171, a178\n"
    "v_accvgpr_read_b32 v189, a179\n"
    "v_mul_f32_e64 v178, v178, v177\n"
    "v_mul_f32_e64 v179, v179, v177\n"
    "v_mul_f32_e64 v171, v171, v177\n"
    "v_mul_f32_e64 v189, v189, v177\n"
    "v_accvgpr_write_b32 a176, v178\n"
    "v_accvgpr_write_b32 a177, v179\n"
    "v_accvgpr_write_b32 a178, v171\n"
    "v_accvgpr_write_b32 a179, v189\n"
    "v_accvgpr_read_b32 v178, a180\n"
    "v_accvgpr_read_b32 v179, a181\n"
    "v_accvgpr_read_b32 v171, a182\n"
    "v_accvgpr_read_b32 v189, a183\n"
    "v_mul_f32_e64 v178, v178, v175\n"
    "v_mul_f32_e64 v179, v179, v175\n"
    "v_mul_f32_e64 v171, v171, v175\n"
    "v_mul_f32_e64 v189, v189, v175\n"
    "v_accvgpr_write_b32 a180, v178\n"
    "v_accvgpr_write_b32 a181, v179\n"
    "v_accvgpr_write_b32 a182, v171\n"
    "v_accvgpr_write_b32 a183, v189\n"
    "v_accvgpr_read_b32 v178, a184\n"
    "v_accvgpr_read_b32 v179, a185\n"
    "v_accvgpr_read_b32 v171, a186\n"
    "v_accvgpr_read_b32 v189, a187\n"
    "v_mul_f32_e64 v178, v178, v176\n"
    "v_mul_f32_e64 v179, v179, v176\n"
    "v_mul_f32_e64 v171, v171, v176\n"
    "v_mul_f32_e64 v189, v189, v176\n"
    "v_accvgpr_write_b32 a184, v178\n"
    "v_accvgpr_write_b32 a185, v179\n"
    "v_accvgpr_write_b32 a186, v171\n"
    "v_accvgpr_write_b32 a187, v189\n"
    "v_accvgpr_read_b32 v178, a188\n"
    "v_accvgpr_read_b32 v179, a189\n"
    "v_accvgpr_read_b32 v171, a190\n"
    "v_accvgpr_read_b32 v189, a191\n"
    "v_mul_f32_e64 v178, v178, v177\n"
    "v_mul_f32_e64 v179, v179, v177\n"
    "v_mul_f32_e64 v171, v171, v177\n"
    "v_mul_f32_e64 v189, v189, v177\n"
    "v_accvgpr_write_b32 a188, v178\n"
    "v_accvgpr_write_b32 a189, v179\n"
    "v_accvgpr_write_b32 a190, v171\n"
    "v_accvgpr_write_b32 a191, v189\n"
    "s_nop 3\n"
    "s_cmp_eq_u32 s78, 0\n"
    "s_cbranch_scc1 L_ret0_%=\n"
    "s_cmp_eq_u32 s78, 1\n"
    "s_cbranch_scc1 L_ret1_%=\n"
    "s_cmp_eq_u32 s78, 2\n"
    "s_cbranch_scc1 L_ret2_%=\n"
    "s_cmp_eq_u32 s78, 3\n"
    "s_cbranch_scc1 L_ret3_%=\n"
    "s_cmp_eq_u32 s78, 4\n"
    "s_cbranch_scc1 L_ret4_%=\n"
    "s_cmp_eq_u32 s78, 5\n"
    "s_cbranch_scc1 L_ret5_%=\n"
    "s_cmp_eq_u32 s78, 6\n"
    "s_cbranch_scc1 L_ret6_%=\n"
    "L_end_%=:\n"
    :
    : [xlo] "s"((unsigned)xb), [xhi] "s"((unsigned)(xb >> 32)), [klo] "s"((unsigned)kb), [khi] "s"((unsigned)(kb >> 32)),
      [vlo] "s"((unsigned)vb), [vhi] "s"((unsigned)(vb >> 32)), [wqlo] "s"((unsigned)wqb), [wqhi] "s"((unsigned)(wqb >> 32)),
      [wolo] "s"((unsigned)wob), [wohi] "s"((unsigned)(wob >> 32)), [plo] "s"((unsigned)pb), [phi] "s"((unsigned)(pb >> 32)),
      [olo] "s"((unsigned)ob), [ohi] "s"((unsigned)(ob >> 32)), [pk] "s"(hp | (j << 4) | (w << 12)), [ldsb] "s"(ldsb),
      [dw] "v"(dw), [dkv] "v"(dkv), [dx] "v"(dx), [bx] "v"(bx), [bk] "v"(bk), [ba] "v"(ba), [bb] "v"(bb)
    : "v16", "v17", "v18", "v19", "v20", "v21", "v22", "v23", "v24", "v25", "v26", "v27", "v28", "v29", "v30", "v31", "v32", "v33", "v34", "v35", "v36", "v37", "v38", "v39",
      "v40", "v41", "v42", "v43", "v44", "v45", "v46", "v47", "v48", "v49", "v50", "v51", "v52", "v53", "v54", "v55", "v56", "v57", "v58", "v59", "v60", "v61", "v62", "v63",
      "v64", "v65", "v66", "v67", "v68", "v69", "v70", "v71", "v72", "v73", "v74", "v75", "v76", "v77", "v78", "v79", "v80", "v81", "v82", "v83", "v84", "v85", "v86", "v87",
      "v88", "v89", "v90", "v91", "v92", "v93", "v94", "v95", "v96", "v97", "v98", "v99", "v100", "v101", "v102", "v103", "v104", "v105", "v106", "v107", "v108", "v109", "v110", "v111",
      "v112", "v113", "v114", "v115", "v116", "v117", "v118", "v119", "v120", "v121", "v122", "v123", "v124", "v125", "v126", "v127", "v128", "v129", "v130", "v131", "v132", "v133", "v134", "v135",
      "v136", "v137", "v138", "v139", "v140", "v141", "v142", "v143", "v144", "v145", "v146", "v147", "v148", "v149", "v150", "v151", "v152", "v153", "v154", "v155", "v156", "v157", "v158", "v159",
      "v160", "v161", "v162", "v163", "v164", "v165", "v166", "v167", "v168", "v169", "v170", "v171", "v172", "v173", "v174", "v175", "v176", "v177", "v178", "v179", "v180", "v181", "v182", "v183",
      "v184", "v185", "v186", "v187", "v188", "v189", "v190", "v191", "v192", "v193", "v194", "v195", "v196", "v197", "v198", "v199", "v200", "v201", "v202", "v203", "v204", "v205", "v206", "v207",
      "v208", "v209", "v210", "v211", "v212", "v213", "v214", "v215", "v216", "v217", "v218", "v219", "v220", "v221", "v222", "v223", "v224", "v225", "v226", "v227", "v228", "v229", "v230", "v231",
      "v232", "v233", "v234", "v235", "v236", "v237", "v238", "v239", "v240", "v241", "v242", "v243", "v244", "v245", "v246", "v247", "v248", "v249", "v250", "v251", "v252", "v253", "v254", "v255",
      "a0", "a1", "a2", "a3", "a4", "a5", "a6", "a7", "a8", "a9", "a10", "a11", "a12", "a13", "a14", "a15", "a16", "a17", "a18", "a19", "a20", "a21", "a22", "a23",
      "a24", "a25", "a26", "a27", "a28", "a29", "a30", "a31", "a32", "a33", "a34", "a35", "a36", "a37", "a38", "a39", "a40", "a41", "a42", "a43", "a44", "a45", "a46", "a47",
      "a48", "a49", "a50", "a51", "a52", "a53", "a54", "a55", "a56", "a57", "a58", "a59", "a60", "a61", "a62", "a63", "a64", "a65", "a66", "a67", "a68", "a69", "a70", "a71",
      "a72", "a73", "a74", "a75", "a76", "a77", "a78", "a79", "a80", "a81", "a82", "a83", "a84", "a85", "a86", "a87", "a88", "a89", "a90", "a91", "a92", "a93", "a94", "a95",
      "a96", "a97", "a98", "a99", "a100", "a101", "a102", "a103", "a104", "a105", "a106", "a107", "a108", "a109", "a110", "a111", "a112", "a113", "a114", "a115", "a116", "a117", "a118", "a119",
      "a120", "a121", "a122", "a123", "a124", "a125", "a126", "a127", "a128", "a129", "a130", "a131", "a132", "a133", "a134", "a135", "a136", "a137", "a138", "a139", "a140", "a141", "a142", "a143",
      "a144", "a145", "a146", "a147", "a148", "a149", "a150", "a151", "a152", "a153", "a154", "a155", "a156", "a157", "a158", "a159", "a160", "a161", "a162", "a163", "a164", "a165", "a166", "a167",
      "a168", "a169", "a170", "a171", "a172", "a173", "a174", "a175", "a176", "a177", "a178", "a179", "a180", "a181", "a182", "a183", "a184", "a185", "a186", "a187", "a188", "a189", "a190", "a191",
      "a192", "a193", "a194", "a195", "a196", "a197", "a198", "a199", "a200", "a201", "a202", "a203", "a204", "a205", "a206", "a207", "a208", "a209", "a210", "a211", "a212", "a213", "a214", "a215",
      "a216", "a217", "a218", "a219", "a220", "a221", "a222", "a223", "a224", "a225", "a226", "a227", "a228", "a229", "a230", "a231", "a232", "a233", "a234", "a235", "a236", "a237", "a238", "a239",
      "a240", "a241", "a242", "a243", "a244", "a245", "a246", "a247", "a248", "a249", "a250", "a251", "a252", "a253", "a254", "a255", "s20", "s21", "s22", "s23", "s24", "s25", "s26", "s27",
      "s28", "s29", "s30", "s31", "s33", "s34", "s35", "s36", "s37", "s38", "s39", "s40", "s41", "s42", "s43", "s44", "s45", "s46", "s47", "s48", "s49", "s50", "s51", "s52",
      "s53", "s54", "s55", "s56", "s57", "s58", "s59", "s60", "s61", "s62", "s63", "s64", "s65", "s66", "s67", "s68", "s69", "s70", "s71", "s72", "s73", "s74", "s75", "s76",
      "s77", "s78", "s79", "s80", "s81", "s82", "s83", "s84", "s85", "s86", "s87", "s88", "s89", "s90", "s91", "s92", "s93", "s94", "s95", "s96", "s97", "s98", "s99", "s100",
      "s101", "vcc", "scc", "memory");
}

extern "C" void kernel_launch(void* const* d_in, const int* in_sizes, int n_in, void* d_out, int out_size, void* d_ws, size_t ws_size,
                              hipStream_t stream) {
  (void)in_sizes; (void)n_in; (void)out_size; (void)ws_size;
  char* part = (char*)d_ws + 0;
  hipLaunchKernelGGL(mha_main, dim3(256), dim3(256), 0, stream, (const void*)d_in[0], (const void*)d_in[1], (const void*)d_in[2],
                     (const void*)d_in[3], (const void*)d_in[4], (void*)part, d_out);

}
```
